```python
import torch

def _softmax(x, axis=-1):
    return torch.softmax(x, dim=axis)
def _gelu(x):
    return torch.nn.functional.gelu(x, approximate="tanh")
def _silu(x):
    return torch.nn.functional.silu(x)
def _sigmoid(x):
    return torch.sigmoid(x)
def _layernorm(x, axis=-1, eps=1e-5):
    m = x.mean(dim=axis, keepdim=True)
    v = x.var(dim=axis, keepdim=True, unbiased=False)
    return (x - m) / torch.sqrt(v + eps)
def _scatter_add(tgt, idx, src, axis=0):
    return tgt.clone().index_add_(axis, idx, src)
def _segment_sum(src, seg, n):
    out = torch.zeros((n,) + src.shape[1:], dtype=src.dtype)
    return out.index_add_(0, seg, src)
def _pad(x, pw):
    flat = tuple(v for pair in reversed(pw) for v in pair)
    return torch.nn.functional.pad(x, flat)

kernel_name = 'gather_scatter__hybrid_graph_tf_graphgps__graph'

def setup_inputs():

    _w = (lambda a: (lambda t: t.to(torch.bfloat16) if t.is_floating_point() else t)(torch.as_tensor(a)))
    N, deg, D = 10000, 64, 64
    E = N * deg
    src = (lambda lo, hi, *s: torch.randint(lo, hi, s))(0, N, E).to(torch.int64)
    dst = (lambda lo, hi, *s: torch.randint(lo, hi, s))(0, N, E).to(torch.int64)
    deg_cnt = torch.bincount(dst, minlength=N).to(float) + 1.0
    sc = 1.0 / float(D) ** 0.5
    return {
        "x": _w((lambda *s: torch.randn(*s, dtype=torch.bfloat16) * 0.05)(N, D)),
        "src": _w(src),
        "dst": _w(dst),
        "deg_inv": _w((1.0 / deg_cnt).to(float)),
        "w_msg": _w((lambda *s: torch.randn(*s, dtype=torch.bfloat16) * 0.4)(D, D) * sc),
        "wq": _w((lambda *s: torch.randn(*s, dtype=torch.bfloat16) * 1.6)(D, D) * sc),
        "wk": _w((lambda *s: torch.randn(*s, dtype=torch.bfloat16) * 1.6)(D, D) * sc),
        "wv": _w((lambda *s: torch.randn(*s, dtype=torch.bfloat16) * 0.2)(D, D) * sc),
        "w_out": _w((lambda *s: torch.randn(*s, dtype=torch.bfloat16) * 0.2)(D, D) * sc),
    }

def reference(x, src, dst, deg_inv, w_msg, wq, wk, wv, w_out):
    N, D = x.shape[0], x.shape[1]
    h = _layernorm(x, axis=-1)
    m_e = torch.einsum('nd,de->ne', h, w_msg)
    h_local = _segment_sum(m_e[src], dst, N) * deg_inv[:, None]
    q = torch.einsum('nd,de->ne', h, wq)
    k = torch.einsum('nd,de->ne', h, wk)
    v = torch.einsum('nd,de->ne', h, wv)
    logits = torch.einsum('id,jd->ij', q, k) / (float(D) ** 0.5)
    attn = _softmax(logits, axis=-1)
    h_global = torch.einsum('ij,jd->id', attn, v)
    y = torch.einsum('nd,de->ne', h_local + h_global, w_out)
    return x + y
```

```cpp
#include <hip/hip_runtime.h>
#include <stdint.h>

#define DI __device__ __forceinline__
typedef unsigned u32x4 __attribute__((ext_vector_type(4)));
typedef unsigned u32x2 __attribute__((ext_vector_type(2)));
typedef float f32x2 __attribute__((ext_vector_type(2)));
typedef float f32x4 __attribute__((ext_vector_type(4)));
typedef float f32x16 __attribute__((ext_vector_type(16)));
typedef float f32x8 __attribute__((ext_vector_type(8)));
typedef __bf16 bf16x2 __attribute__((ext_vector_type(2)));
typedef __bf16 bf16x8 __attribute__((ext_vector_type(8)));
typedef short s16x4 __attribute__((ext_vector_type(4)));
typedef __attribute__((address_space(3))) char* ldsptr;
#define LDSP(T, off) ((__attribute__((address_space(3))) T*)(lds + (off)))

constexpr int NN = 10000;
constexpr int BN = 40, NB = 250;
constexpr int CHP = 2592;
constexpr int KS = 625;
constexpr int KX = 9984;

constexpr unsigned WS_HP = 0;
constexpr unsigned WS_FL = 1280000;
constexpr unsigned WS_FW = WS_FL + 252 * 128;
constexpr unsigned WS_EP = WS_FW + 128;
constexpr unsigned WS_WF = WS_EP + 128;
constexpr unsigned WS_TB = WS_WF + 18432;
constexpr unsigned WS_ED = WS_TB + NB * 2048;
constexpr unsigned WS_IB = WS_ED + NB * CHP * 4;
constexpr unsigned WS_FB = WS_IB + NB * 15360;
constexpr unsigned WS_QF = WS_FB + 252 * 40960;
constexpr unsigned WS_END = WS_QF + 252 * 20480;
constexpr unsigned OOB = 0x80000000u;
constexpr int SPIN_MAX = 1 << 20;

constexpr int LDS_REG = 30720;
constexpr int LDS_QOFF = 20480;
constexpr int LDS_ENT = 4 * LDS_REG + BN * 272;
constexpr int LDS_ACC = LDS_ENT;
constexpr int LDS_M = LDS_ENT + CHP * 4 + 2048;
constexpr int LDS_TOTAL = 163840;
static_assert(WS_FL == 1280000 && WS_FW == 1312256 && WS_EP == 1312384 && WS_WF == 1312512 && WS_TB == 1330944 &&
              WS_ED == 1842944 && WS_IB == 4434944 && WS_END == 23757824 && LDS_REG == 30720 && LDS_QOFF == 20480 &&
              LDS_ENT == 133760 && LDS_M == 146176 && LDS_TOTAL == 163840, "offsets used by the assembly");

DI unsigned pk2(float a, float b) { f32x2 v = {a, b}; return __builtin_bit_cast(unsigned, __builtin_convertvector(v, bf16x2)); }
DI float bflo(unsigned w) { return __uint_as_float(w << 16); }
DI float bfhi(unsigned w) { return __uint_as_float(w & 0xffff0000u); }
DI bf16x8 asbf(u32x4 v) { return __builtin_bit_cast(bf16x8, v); }
DI f32x16 mfma32(u32x4 a, u32x4 b, f32x16 c) { return __builtin_amdgcn_mfma_f32_32x32x16_bf16(asbf(a), asbf(b), c, 0, 0, 0); }
DI f32x4 mfma16(u32x4 a, u32x4 b, f32x4 c) { return __builtin_amdgcn_mfma_f32_16x16x32_bf16(asbf(a), asbf(b), c, 0, 0, 0); }
DI float half_sum(float v) { auto r = __builtin_amdgcn_permlane32_swap(__float_as_uint(v), __float_as_uint(v), false, false); return __uint_as_float(r[0]) + __uint_as_float(r[1]); }
DI float half_max(float v) { auto r = __builtin_amdgcn_permlane32_swap(__float_as_uint(v), __float_as_uint(v), false, false); return fmaxf(__uint_as_float(r[0]), __uint_as_float(r[1])); }
DI float half_lo(float v) { auto r = __builtin_amdgcn_permlane32_swap(__float_as_uint(v), __float_as_uint(v), false, false); return __uint_as_float(r[0]); }
DI u32x4 pack8(const f32x16& a, int o) { return u32x4{pk2(a[o], a[o + 1]), pk2(a[o + 2], a[o + 3]), pk2(a[o + 4], a[o + 5]), pk2(a[o + 6], a[o + 7])}; }
DI __amdgpu_buffer_rsrc_t mkrsrc(const void* p, unsigned bytes) { return __builtin_amdgcn_make_buffer_rsrc((void*)p, 0, bytes, 0x00020000); }

DI float quad_sum(float v) {
    auto r = __builtin_amdgcn_permlane16_swap(__float_as_uint(v), __float_as_uint(v), false, false);
    return half_sum(__uint_as_float(r[0]) + __uint_as_float(r[1]));
}
DI float quad_lo(float v) {
    auto r = __builtin_amdgcn_permlane16_swap(__float_as_uint(v), __float_as_uint(v), false, false);
    return half_lo(__uint_as_float(r[0]));
}
DI u32x4 ld16(__amdgpu_buffer_rsrc_t r, unsigned off) { return __builtin_amdgcn_raw_buffer_load_b128(r, off, 0, 16); }
DI u32x2 ld8(__amdgpu_buffer_rsrc_t r, unsigned off) { return __builtin_amdgcn_raw_buffer_load_b64(r, off, 0, 16); }
DI void st16(u32x4 v, __amdgpu_buffer_rsrc_t r, unsigned off) { __builtin_amdgcn_raw_buffer_store_b128(v, r, off, 0, 16); }
DI void st8(u32x2 v, __amdgpu_buffer_rsrc_t r, unsigned off) { __builtin_amdgcn_raw_buffer_store_b64(v, r, off, 0, 16); }
DI void lds_barrier() { asm volatile("s_waitcnt lgkmcnt(0)\n\ts_barrier" ::: "memory"); }
DI void drain() { asm volatile("s_waitcnt vmcnt(0)" ::: "memory"); }
DI bool tagged(u32x2 v, u32x2 tag) { return v[1] == tag[1] && (v[0] ^ tag[0]) < (1u << 24); }
DI u32x2 poll8(__amdgpu_buffer_rsrc_t r, unsigned off, u32x2 tag) {
    u32x2 v;
    for (int spin = 0; spin < SPIN_MAX; ++spin) {
        v = ld8(r, off);
        if (__builtin_amdgcn_ballot_w64(!tagged(v, tag)) == 0ull) break;
        asm volatile("s_sleep 1" ::: "memory");
    }
    asm volatile("" ::: "memory");
    return v;
}

DI void prep_weights(int w, int lane, const u32x4* wv, const u32x4* wm, const unsigned short* wo, __amdgpu_buffer_rsrc_t rws) {
    const int r = lane & 31, h = lane >> 5, eb = w & 1;
    const u32x4* wl = (w >> 1) ? wm : wv;
    u32x4 bfr[4];
#pragma unroll
    for (int s = 0; s < 4; ++s)
#pragma unroll
        for (int j = 0; j < 4; ++j) {
            const int m = 16 * s + 8 * h + 2 * j;
            bfr[s][j] = (unsigned)wo[m * 64 + 32 * eb + r] | ((unsigned)wo[(m + 1) * 64 + 32 * eb + r] << 16);
        }
    f32x16 d[2]; float sum = 0.f;
#pragma unroll
    for (int a = 0; a < 2; ++a) {
        d[a] = f32x16{};
#pragma unroll
        for (int s = 0; s < 4; ++s) d[a] = mfma32(wl[(32 * a + r) * 8 + 2 * s + h], bfr[s], d[a]);
    }
#pragma unroll
    for (int i = 0; i < 16; ++i) sum += d[0][i] + d[1][i];
    const float kap = (half_sum(sum) * 0.125f - half_lo(d[0][0])) * (1.f / 7.f);
#pragma unroll
    for (int i = 0; i < 16; ++i) { d[0][i] -= kap; d[1][i] -= kap; }
    if (h == 0) d[0][0] = 0.f;
    const int e = 32 * eb + r;
#pragma unroll
    for (int a = 0; a < 2; ++a)
#pragma unroll
        for (int g = 0; g < 4; ++g)
            st8(u32x2{pk2(d[a][4 * g], d[a][4 * g + 1]), pk2(d[a][4 * g + 2], d[a][4 * g + 3])}, rws,
                WS_WF + (w >> 1) * 8192 + (((e & 3) * 2 + a) * 64 + (e >> 2) + 16 * g) * 16 + 8 * h);
}

DI void prep_last_keys(int w, int lane, const u32x4* x, __amdgpu_buffer_rsrc_t rws) {
    const int n16 = lane & 15, g4 = lane >> 4;
    float xf[16], sum = 0.f, sq = 0.f;
#pragma unroll
    for (int k = 0; k < 2; ++k) {
        const u32x4 v = x[(KX + n16) * 8 + 4 * k + g4];
#pragma unroll
        for (int j = 0; j < 4; ++j) { xf[8 * k + 2 * j] = bflo(v[j]); xf[8 * k + 2 * j + 1] = bfhi(v[j]); sum += bflo(v[j]) + bfhi(v[j]); }
    }
    const float mean = quad_sum(sum) * (1.f / 64.f);
#pragma unroll
    for (int i = 0; i < 16; ++i) { xf[i] -= mean; sq += xf[i] * xf[i]; }
    const float rstd = __builtin_amdgcn_rsqf(quad_sum(sq) * (1.f / 64.f) + 1e-5f), sh = quad_lo(xf[0]) * rstd * (1.f / 7.f);
    f32x4 z = {};
#pragma unroll
    for (int k = 0; k < 2; ++k) {
        u32x4 hp;
#pragma unroll
        for (int j = 0; j < 4; ++j) hp[j] = pk2(__builtin_fmaf(xf[8 * k + 2 * j], rstd, sh), __builtin_fmaf(xf[8 * k + 2 * j + 1], rstd, sh));
        z = mfma16(hp, ld16(rws, WS_WF + (w * 2 + k) * 1024 + lane * 16), z);
    }
    st8(u32x2{pk2(z[0], z[1]), pk2(z[2], z[3])}, rws, WS_WF + 16384 + (w >> 1) * 1024 + lane * 16 + (w & 1) * 8);
}

DI void attn_true_max(int qb, int s, int w, int lane, char* ws, ldsptr lds, f32x4 (&own)[10]) {
    const int r = lane & 31, h = lane >> 5;
    const __amdgpu_buffer_rsrc_t rall = mkrsrc(ws + WS_HP, NN * 128);
    const __amdgpu_buffer_rsrc_t rq = mkrsrc(ws + WS_QF, 252 * 20480);
    const unsigned qo = (4 * qb + s) * 20480 + (lane ^ (8 * w)) * 16;
#pragma unroll 1
    for (int t = 0; t < 5; ++t) {
        u32x4 q[4];
#pragma unroll
        for (int k = 0; k < 4; ++k) q[k] = ld16(rq, qo + t * 4096 + k * 1024);
        float m = -INFINITY;
#pragma unroll 1
        for (int k0 = 32 * w; k0 < NN; k0 += 128) {
            f32x16 sc = {};
#pragma unroll
            for (int k = 0; k < 4; ++k) sc = mfma32(__builtin_amdgcn_raw_buffer_load_b128(rall, (k0 + r) * 128 + 32 * k + 16 * h, 0, 16), q[k], sc);
#pragma unroll
            for (int i = 0; i < 16; ++i) if (k0 + (i & 3) + 8 * (i >> 2) + 4 * h < NN) m = fmaxf(m, sc[i]);
        }
        m = half_max(m);
        if (h == 0) *LDSP(float, LDS_ACC + ((w * 5 + t) * 32 + (r ^ (8 * w))) * 4) = m;
    }
    __syncthreads();
    const int kb = (4 * s + w) * KS;
    f32x4* fb = (f32x4*)(ws + WS_FB) + ((qb * 4 + s) * 4 + w) * 640 + lane;
#pragma unroll 1
    for (int t = 0; t < 5; ++t) {
        u32x4 q[4];
#pragma unroll
        for (int k = 0; k < 4; ++k) q[k] = ld16(rq, qo + t * 4096 + k * 1024);
        float m = -INFINITY;
#pragma unroll
        for (int v = 0; v < 4; ++v) m = fmaxf(m, *LDSP(float, LDS_ACC + ((v * 5 + t) * 32 + (r ^ (8 * w))) * 4));
        f32x16 o[2] = {};
#pragma unroll 1
        for (int k0 = 0; k0 < KS; k0 += 32) {
            f32x16 sc = {};
#pragma unroll
            for (int k = 0; k < 4; ++k) sc = mfma32(__builtin_amdgcn_raw_buffer_load_b128(rall, (kb + k0 + r) * 128 + 32 * k + 16 * h, 0, 16), q[k], sc);
#pragma unroll
            for (int i = 0; i < 16; ++i) sc[i] = (k0 + (i & 3) + 8 * (i >> 2) + 4 * h < KS) ? __builtin_amdgcn_exp2f(sc[i] - m) : 0.f;
#pragma unroll
            for (int ks = 0; ks < 2; ++ks) {
                const u32x4 pf = pack8(sc, 8 * ks);
#pragma unroll
                for (int b = 0; b < 2; ++b) {
                    u32x4 vf;
#pragma unroll
                    for (int j = 0; j < 4; ++j) {
                        const int key = kb + k0 + 16 * ks + 8 * (j >> 1) + 4 * h + 2 * (j & 1);
                        vf[j] = (unsigned)__builtin_amdgcn_raw_buffer_load_b16(rall, key * 128 + 64 * b + 2 * r, 0, 16) |
                                ((unsigned)__builtin_amdgcn_raw_buffer_load_b16(rall, key * 128 + 128 + 64 * b + 2 * r, 0, 16) << 16);
                    }
                    o[b] = mfma32(pf, vf, o[b]);
                }
            }
        }
#pragma unroll
        for (int b = 0; b < 2; ++b) {
            fb[(2 * t + b) * 64] = f32x4{o[b][0], o[b][1], o[b][2], o[b][3]};
#pragma unroll
            for (int g = 1; g < 4; ++g)
                *LDSP(f32x4, w * LDS_REG + ((g - 1) * 10 + 2 * t + b) * 1024 + lane * 16) = f32x4{o[b][4 * g], o[b][4 * g + 1], o[b][4 * g + 2], o[b][4 * g + 3]};
        }
    }
    asm volatile("s_waitcnt vmcnt(0)" ::: "memory");
#pragma unroll
    for (int i = 0; i < 10; ++i) own[i] = fb[i * 64];
}

__global__ __launch_bounds__(256, 1) __attribute__((amdgpu_waves_per_eu(1, 1), aligned(4096)))
void gps_fused(const unsigned short* __restrict__ x, const int* __restrict__ src, const int* __restrict__ dst,
               const unsigned short* __restrict__ dinv, const u32x4* __restrict__ wq, const u32x4* __restrict__ wk, char* ws,
               unsigned short* __restrict__ out, const u32x4* __restrict__ wm, const u32x4* __restrict__ wv,
               const unsigned short* __restrict__ wo) {
    __shared__ __attribute__((aligned(128))) char smem[LDS_TOTAL];
    const ldsptr lds = (ldsptr)smem;
    int b; unsigned status, tv; uint64_t epoch; f32x8 own0, own4; f32x16 agx; f32x4 lx;
    asm volatile(
        "s_load_dwordx16 s[4:19], %[karg], 0x0\n"
        "s_getpc_b64 s[80:81]\n"
        "v_mov_b32 v8, %[tid]\n"
        "s_mov_b32 s72, %[blk]\n"
        "v_readfirstlane_b32 s59, v8\n"
        "v_and_b32 v9, 63, v8\n"
        "s_lshr_b32 s84, s72, 3\n"
        "s_lshr_b32 s59, s59, 6\n"
        "s_cmp_lg_u32 s59, 2\n"
        "s_cbranch_scc1 12f\n"
        "v_lshl_add_u32 v232, v9, 5, s84\n"
        "v_min_u32 v232, 415, v232\n"
        "v_lshlrev_b32 v232, 7, v232\n"
        "global_load_dword v255, v232, s[80:81]\n"
        "12:\n"
        "s_and_b32 s73, s72, 7\n"
        "s_lshr_b32 s74, s73, 1\n"
        "s_lshl_b32 s75, s73, 1\n"
        "s_lshr_b32 s76, 38976, s75\n"
        "s_and_b32 s76, s76, 3\n"
        "s_lshl_b32 s77, s74, 5\n"
        "s_add_u32 s77, s77, s84\n"
        "s_sub_u32 s77, s77, s76\n"
        "s_and_b32 s76, s77, 1\n"
        "s_and_b32 s75, s75, 2\n"
        "s_lshl1_add_u32 s58, s77, s75\n"
        "s_sub_u32 s58, s58, s76\n"
        "v_cmp_gt_u32 vcc, 160, v8\n"
        "v_and_b32 v233, 0xfc, v8\n"
        "v_add_lshl_u32 v232, v8, v233, 4\n"
        "s_mul_i32 s72, s58, 5120\n"
        "v_bfrev_b32 v235, 1\n"
        "v_add_u32 v232, s72, v232\n"
        "v_cndmask_b32 v231, v235, v232, vcc\n"
        "s_mov_b32 s94, 1280000\n"
        "s_mov_b32 s95, 0x20000\n"
        "s_cmp_eq_u32 s59, 3\n"
        "s_waitcnt lgkmcnt(0)\n"
        "s_mov_b32 s92, s4\n"
        "s_cbranch_scc1 11f\n"
        "s_and_b32 s93, s5, 0xffff\n"
        "buffer_load_dwordx4 v[176:179], v231, s[92:95], 0 offen offset:0\n"
        "buffer_load_dwordx4 v[180:183], v231, s[92:95], 0 offen offset:64\n"
        "s_branch 13f\n"
        "11:\n"
        "s_and_b32 s93, s5, 0xffff\n"
        "v_bfe_u32 v232, v9, 4, 1\n"
        "v_xor_b32 v232, v232, v9\n"
        "v_lshlrev_b32 v84, 4, v232\n"
        "v_xor_b32 v85, 32, v84\n"
        "v_xor_b32 v86, 64, v84\n"
        "v_xor_b32 v87, 96, v84\n"
        "s_mov_b32 m0, 92160\n"
        "s_nop 0\n"
        "global_load_lds_dwordx4 v84, s[12:13] offset:0\n"
        "global_load_lds_dwordx4 v85, s[12:13] offset:1024\n"
        "global_load_lds_dwordx4 v86, s[12:13] offset:2048\n"
        "global_load_lds_dwordx4 v87, s[12:13] offset:3072\n"
        "s_mov_b32 m0, 100352\n"
        "s_nop 0\n"
        "global_load_lds_dwordx4 v84, s[14:15] offset:0\n"
        "global_load_lds_dwordx4 v85, s[14:15] offset:1024\n"
        "global_load_lds_dwordx4 v86, s[14:15] offset:2048\n"
        "global_load_lds_dwordx4 v87, s[14:15] offset:3072\n"
        "s_mov_b32 m0, 96256\n"
        "s_add_u32 s12, s12, 0x1000\n"
        "s_addc_u32 s13, s13, 0\n"
        "global_load_lds_dwordx4 v84, s[12:13] offset:0\n"
        "global_load_lds_dwordx4 v85, s[12:13] offset:1024\n"
        "global_load_lds_dwordx4 v86, s[12:13] offset:2048\n"
        "global_load_lds_dwordx4 v87, s[12:13] offset:3072\n"
        "s_mov_b32 m0, 104448\n"
        "s_add_u32 s14, s14, 0x1000\n"
        "s_addc_u32 s15, s15, 0\n"
        "global_load_lds_dwordx4 v84, s[14:15] offset:0\n"
        "global_load_lds_dwordx4 v85, s[14:15] offset:1024\n"
        "global_load_lds_dwordx4 v86, s[14:15] offset:2048\n"
        "global_load_lds_dwordx4 v87, s[14:15] offset:3072\n"
        "13:\n"
        "s_cmp_eq_u32 s84, 31\n"
        "s_cselect_b32 s76, 90, 0\n"
        "s_bitcmp1_b32 s76, s73\n"
        "s_cbranch_scc1 39f\n"
        "v_bfe_u32 v60, v8, 4, 2\n"
        "v_xor_b32 v234, v9, v60\n"
        "v_lshlrev_b32 v228, 4, v234\n"
        "v_xor_b32 v229, 64, v228\n"
        "s_lshr_b32 s61, s58, 2\n"
        "s_mul_i32 s62, s59, 30720\n"
        "s_mul_i32 s72, s61, 20480\n"
        "s_lshl_b32 s73, s59, 11\n"
        "s_add_u32 s72, s72, s73\n"
        "s_add_u32 s73, s72, 6144\n"
        "s_add_u32 s77, s72, 14336\n"
        "v_mov_b32 v30, 1312384\n"
        "s_add_u32 m0, s62, 18432\n"
        "s_nop 0\n"
        "buffer_load_dwordx4 v228, s[92:95], s73 offen offset:0 lds\n"
        "buffer_load_dwordx4 v229, s[92:95], s73 offen offset:1024 lds\n"
        "s_cmp_eq_u32 s59, 3\n"
        "s_cbranch_scc1 15f\n"
        "s_add_u32 m0, s62, 16384\n"
        "s_nop 0\n"
        "buffer_load_dwordx4 v228, s[92:95], s72 offen offset:0 lds\n"
        "buffer_load_dwordx4 v229, s[92:95], s72 offen offset:1024 lds\n"
        "s_add_u32 m0, s62, 25600\n"
        "s_nop 0\n"
        "buffer_load_dwordx4 v228, s[92:95], s77 offen offset:0 lds\n"
        "buffer_load_dwordx4 v229, s[92:95], s77 offen offset:1024 lds\n"
        "global_load_dwordx2 v[30:31], v30, s[16:17] sc1\n"
        "v_and_b32 v59, 15, v8\n"
        "v_and_b32 v247, 7, v9\n"
        "v_lshrrev_b32 v246, 3, v9\n"
        "s_and_b32 s60, s58, 3\n"
        "s_brev_b32 s83, 1\n"
        "s_cmp_lt_u32 s58, 250\n"
        "s_cselect_b32 s69, 0, s83\n"
        "s_mov_b32 s82, 0\n"
        "s_movk_i32 s66, 0x1000\n"
        "s_movk_i32 s67, 0x2000\n"
        "s_movk_i32 s68, 0x3000\n"
        "s_movk_i32 s70, 0x80\n"
        "v_lshlrev_b32 v248, 4, v9\n"
        "v_lshlrev_b32 v249, 4, v8\n"
        "v_lshrrev_b32 v232, 1, v59\n"
        "v_xor_b32 v232, v232, v60\n"
        "v_lshlrev_b32 v232, 4, v232\n"
        "v_lshl_add_u32 v232, v59, 7, v232\n"
        "v_add_u32 v226, s62, v232\n"
        "v_xor_b32 v227, 64, v226\n"
        "v_add_u32 v44, 146176, v248\n"
        "v_min_u32 v232, 39, v9\n"
        "v_lshlrev_b32 v57, 2, v232\n"
        "v_add_u32 v57, 163680, v57\n"
        "v_mul_u32_u24 v58, 272, v232\n"
        "v_add_u32 v58, 106496, v58\n"
        "v_mov_b32 v61, 2\n"
        "v_mov_b32 v63, 154368\n"
        "v_mov_b32 v236, -1\n"
        "v_mov_b32 v237, -1\n"
        "v_mov_b32 v238, -1\n"
        "v_mov_b32 v239, -1\n"
        "v_mov_b32 v240, 0\n"
        "v_mov_b32 v241, 0\n"
        "v_add_u32 v232, 122880, v249\n"
        "v_lshlrev_b32 v234, 3, v8\n"
        "v_add_u32 v234, 144000, v234\n"
        "ds_write_b64 v234, v[240:241]\n"
        "ds_write_b32 v57, v58\n"
        "ds_write_b64 v63, v[240:241]\n"
        "ds_write_b128 v232, v[236:239] offset:0\n"
        "ds_write_b128 v232, v[236:239] offset:3072\n"
        "ds_write_b128 v232, v[236:239] offset:6144\n"
        "ds_write_b128 v232, v[236:239] offset:7808\n"
        "s_min_u32 s76, s58, 249\n"
        "s_mul_i32 s76, s76, 10240\n"
        "v_add_u32 v90, s76, v249\n"
        "v_add_u32 v91, 0x1000, v90\n"
        "s_add_u32 s76, s76, 0x2000\n"
        "v_lshl_add_u32 v92, v8, 3, s76\n"
        "v_mov_b32 v253, 0x3f80\n"
        "v_and_b32 v232, 3, v9\n"
        "v_cmp_eq_u32 vcc, 0, v232\n"
        "v_mov_b32 v233, 0x3f803f80\n"
        "v_and_b32 v232, 0x33, v9\n"
        "v_cndmask_b32 v233, 0, v233, vcc\n"
        "v_cmp_eq_u32 vcc, 1, v232\n"
        "v_accvgpr_write_b32 a249, v233\n"
        "v_accvgpr_write_b32 a250, v233\n"
        "v_accvgpr_write_b32 a251, v233\n"
        "v_accvgpr_write_b32 a252, v233\n"
        "v_accvgpr_write_b32 a253, v233\n"
        "v_accvgpr_write_b32 a254, v233\n"
        "v_accvgpr_write_b32 a255, v233\n"
        "v_cndmask_b32 v232, v233, v253, vcc\n"
        "v_accvgpr_write_b32 a248, v232\n"
        "s_mov_b32 s36, s16\n"
        "s_and_b32 s37, s17, 0xffff\n"
        "s_mov_b32 s38, 23757824\n"
        "s_mov_b32 s39, 0x20000\n"
        "v_cmp_eq_u32 vcc, 0, v60\n"
        "v_mov_b32 v251, 0xc1c0\n"
        "v_mov_b32 v242, 4\n"
        "v_cndmask_b32 v243, 0, -1.0, vcc\n"
        "v_cndmask_b32 v251, 0, v251, vcc\n"
        "v_bfe_i32 v252, v233, 7, 1\n"
        "v_lshrrev_b32 v252, 16, v252\n"
        "v_or_b32 v231, s69, v231\n"
        "s_waitcnt vmcnt(7)\n"
        "v_lshlrev_b32 v128, 16, v176\n"
        "v_and_b32 v129, 0xffff0000, v176\n"
        "v_lshlrev_b32 v130, 16, v177\n"
        "v_and_b32 v131, 0xffff0000, v177\n"
        "v_lshlrev_b32 v132, 16, v178\n"
        "v_and_b32 v133, 0xffff0000, v178\n"
        "v_lshlrev_b32 v134, 16, v179\n"
        "v_and_b32 v135, 0xffff0000, v179\n"
        "v_lshlrev_b32 v136, 16, v180\n"
        "v_and_b32 v137, 0xffff0000, v180\n"
        "v_lshlrev_b32 v138, 16, v181\n"
        "v_and_b32 v139, 0xffff0000, v181\n"
        "v_lshlrev_b32 v140, 16, v182\n"
        "v_and_b32 v141, 0xffff0000, v182\n"
        "v_lshlrev_b32 v142, 16, v183\n"
        "v_and_b32 v143, 0xffff0000, v183\n"
        "v_pk_add_f32 v[144:145], v[128:129], v[130:131]\n"
        "v_pk_add_f32 v[144:145], v[144:145], v[132:133]\n"
        "v_pk_add_f32 v[144:145], v[144:145], v[134:135]\n"
        "v_pk_add_f32 v[144:145], v[144:145], v[136:137]\n"
        "v_pk_add_f32 v[144:145], v[144:145], v[138:139]\n"
        "v_pk_add_f32 v[144:145], v[144:145], v[140:141]\n"
        "v_pk_add_f32 v[144:145], v[144:145], v[142:143]\n"
        "v_add_f32 v144, v144, v145\n"
        "v_mov_b32 v148, 0x3727c5ac\n"
        "s_nop 0\n"
        "v_add_f32_dpp v144, v144, v144 quad_perm:[1,0,3,2] row_mask:0xf bank_mask:0xf\n"
        "s_nop 1\n"
        "v_add_f32_dpp v144, v144, v144 quad_perm:[2,3,0,1] row_mask:0xf bank_mask:0xf\n"
        "v_mul_f32 v144, 0xbc800000, v144\n"
        "v_pk_add_f32 v[128:129], v[128:129], v[144:145] op_sel_hi:[1,0]\n"
        "v_pk_add_f32 v[130:131], v[130:131], v[144:145] op_sel_hi:[1,0]\n"
        "v_pk_add_f32 v[132:133], v[132:133], v[144:145] op_sel_hi:[1,0]\n"
        "v_pk_add_f32 v[134:135], v[134:135], v[144:145] op_sel_hi:[1,0]\n"
        "v_pk_add_f32 v[136:137], v[136:137], v[144:145] op_sel_hi:[1,0]\n"
        "v_pk_add_f32 v[138:139], v[138:139], v[144:145] op_sel_hi:[1,0]\n"
        "v_pk_add_f32 v[140:141], v[140:141], v[144:145] op_sel_hi:[1,0]\n"
        "v_pk_add_f32 v[142:143], v[142:143], v[144:145] op_sel_hi:[1,0]\n"
        "v_pk_mul_f32 v[146:147], v[128:129], v[128:129]\n"
        "v_pk_fma_f32 v[146:147], v[130:131], v[130:131], v[146:147]\n"
        "v_pk_fma_f32 v[146:147], v[132:133], v[132:133], v[146:147]\n"
        "v_pk_fma_f32 v[146:147], v[134:135], v[134:135], v[146:147]\n"
        "v_pk_fma_f32 v[146:147], v[136:137], v[136:137], v[146:147]\n"
        "v_pk_fma_f32 v[146:147], v[138:139], v[138:139], v[146:147]\n"
        "v_pk_fma_f32 v[146:147], v[140:141], v[140:141], v[146:147]\n"
        "v_pk_fma_f32 v[146:147], v[142:143], v[142:143], v[146:147]\n"
        "v_add_f32 v146, v146, v147\n"
        "v_mov_b32_dpp v150, v128 quad_perm:[0,0,0,0] row_mask:0xf bank_mask:0xf\n"
        "v_mul_f32 v150, 0x3e124925, v150\n"
        "v_add_f32_dpp v146, v146, v146 quad_perm:[1,0,3,2] row_mask:0xf bank_mask:0xf\n"
        "s_nop 1\n"
        "v_add_f32_dpp v146, v146, v146 quad_perm:[2,3,0,1] row_mask:0xf bank_mask:0xf\n"
        "v_fmac_f32 v148, 0x3c800000, v146\n"
        "v_rsq_f32 v148, v148\n"
        "s_nop 0\n"
        "v_mul_f32 v150, v150, v148\n"
        "v_pk_fma_f32 v[146:147], v[128:129], v[148:149], v[150:151] op_sel_hi:[1,0,0]\n"
        "v_cvt_pk_bf16_f32 v164, v146, v147\n"
        "v_bfi_b32 v164, v252, v253, v164\n"
        "v_pk_fma_f32 v[146:147], v[130:131], v[148:149], v[150:151] op_sel_hi:[1,0,0]\n"
        "v_cvt_pk_bf16_f32 v165, v146, v147\n"
        "v_pk_fma_f32 v[146:147], v[132:133], v[148:149], v[150:151] op_sel_hi:[1,0,0]\n"
        "v_cvt_pk_bf16_f32 v166, v146, v147\n"
        "v_pk_fma_f32 v[146:147], v[134:135], v[148:149], v[150:151] op_sel_hi:[1,0,0]\n"
        "v_cvt_pk_bf16_f32 v167, v146, v147\n"
        "buffer_store_dwordx4 v[164:167], v231, s[36:39], 0 offen offset:0 sc1\n"
        "v_pk_fma_f32 v[146:147], v[136:137], v[148:149], v[150:151] op_sel_hi:[1,0,0]\n"
        "v_cvt_pk_bf16_f32 v168, v146, v147\n"
        "v_pk_fma_f32 v[146:147], v[138:139], v[148:149], v[150:151] op_sel_hi:[1,0,0]\n"
        "v_cvt_pk_bf16_f32 v169, v146, v147\n"
        "v_pk_fma_f32 v[146:147], v[140:141], v[148:149], v[150:151] op_sel_hi:[1,0,0]\n"
        "v_cvt_pk_bf16_f32 v170, v146, v147\n"
        "v_pk_fma_f32 v[146:147], v[142:143], v[148:149], v[150:151] op_sel_hi:[1,0,0]\n"
        "v_cvt_pk_bf16_f32 v171, v146, v147\n"
        "buffer_store_dwordx4 v[168:171], v231, s[36:39], 0 offen offset:64 sc1\n"
        "s_waitcnt vmcnt(3)\n"
        "ds_read_b128 v[112:115], v226 offset:18432\n"
        "ds_read_b128 v[116:119], v227 offset:18432\n"
        "ds_read_b128 v[104:107], v226 offset:16384\n"
        "ds_read_b128 v[108:111], v227 offset:16384\n"
        "ds_read_b128 v[120:123], v226 offset:25600\n"
        "ds_read_b128 v[124:127], v227 offset:25600\n"
        "s_mov_b64 s[40:41], s[36:37]\n"
        "s_mov_b64 s[42:43], s[94:95]\n"
        "s_and_b32 s72, s60, 1\n"
        "s_lshl2_add_u32 s72, s72, s59\n"
        "s_movk_i32 s77, 640\n"
        "s_cmp_lt_u32 s60, 2\n"
        "s_cselect_b32 s77, s77, 608\n"
        "s_cselect_b32 s73, 0, 5120\n"
        "s_cselect_b32 s85, 0, s66\n"
        "s_mul_i32 s72, s72, s77\n"
        "s_add_u32 s72, s72, s73\n"
        "s_lshl_b32 s73, s72, 7\n"
        "s_add_u32 s44, s36, s73\n"
        "s_addc_u32 s45, s37, 0\n"
        "s_lshl_b32 s46, s77, 7\n"
        "s_mov_b32 s47, s39\n"
        "s_add_u32 s85, s85, s62\n"
        "s_mov_b32 s48, s18\n"
        "s_and_b32 s49, s19, 0xffff\n"
        "s_mov_b64 s[50:51], s[42:43]\n"
        "s_mul_i32 s63, s58, -160\n"
        "s_add_u32 s63, s63, 203680\n"
        "s_or_b32 s63, s63, s69\n"
        "s_mov_b32 s65, 0\n"
        "s_mul_i32 s74, s72, 52429\n"
        "s_lshr_b32 s74, s74, 21\n"
        "s_add_u32 s73, s72, s77\n"
        "s_sub_u32 s73, s73, 1\n"
        "s_mul_i32 s73, s73, 52429\n"
        "s_lshr_b32 s73, s73, 21\n"
        "v_mul_u32_u24 v233, 43, v9\n"
        "v_lshrrev_b32 v233, 7, v233\n"
        "v_add_u32 v232, s74, v233\n"
        "v_min_u32 v232, s73, v232\n"
        "v_mad_i32_i24 v233, v233, -3, v9\n"
        "v_lshlrev_b32 v233, 3, v233\n"
        "v_lshl_add_u32 v45, v232, 7, v233\n"
        "v_add_u32 v45, 1280000, v45\n"
        "s_lshl_b32 s74, s58, 7\n"
        "s_lshl3_add_u32 s74, s59, s74\n"
        "s_add_u32 s74, s74, 1280000\n"
        "s_or_b32 s74, s74, s69\n"
        "s_cmp_eq_u32 s59, 3\n"
        "s_cselect_b32 s74, s83, s74\n"
        "v_cmp_eq_u32 vcc, 0, v9\n"
        "v_mov_b32 v232, s74\n"
        "s_nop 0\n"
        "v_cndmask_b32 v254, v235, v232, vcc\n"
        "s_waitcnt vmcnt(2)\n"
        "v_readfirstlane_b32 s52, v30\n"
        "v_readfirstlane_b32 s53, v31\n"
        "s_add_u32 s52, s52, 1\n"
        "s_addc_u32 s53, s53, 0\n"
        "s_mov_b32 s54, s52\n"
        "s_and_b32 s55, s53, 0xff\n"
        "s_lshl_b64 s[56:57], s[52:53], 24\n"
        "v_mov_b32 v30, s56\n"
        "v_mov_b32 v31, s57\n"
        "s_waitcnt lgkmcnt(3)\n"
        "v_mfma_f32_16x16x32_bf16 v[144:147], a[248:251], v[104:107], 0\n"
        "v_mfma_f32_16x16x32_bf16 v[208:211], a[248:251], v[112:115], 0\n"
        "s_waitcnt lgkmcnt(1)\n"
        "v_mfma_f32_16x16x32_bf16 v[168:171], a[248:251], v[120:123], 0\n"
        "v_mfma_f32_16x16x32_bf16 v[144:147], a[252:255], v[108:111], v[144:147]\n"
        "v_mfma_f32_16x16x32_bf16 v[208:211], a[252:255], v[116:119], v[208:211]\n"
        "s_waitcnt lgkmcnt(0)\n"
        "v_mfma_f32_16x16x32_bf16 v[168:171], a[252:255], v[124:127], v[168:171]\n"
        "v_lshlrev_b32 v128, 16, v104\n"
        "v_lshlrev_b32 v192, 16, v112\n"
        "v_lshlrev_b32 v152, 16, v120\n"
        "v_and_b32 v129, 0xffff0000, v104\n"
        "v_and_b32 v193, 0xffff0000, v112\n"
        "v_and_b32 v153, 0xffff0000, v120\n"
        "v_lshlrev_b32 v130, 16, v105\n"
        "v_lshlrev_b32 v194, 16, v113\n"
        "v_lshlrev_b32 v154, 16, v121\n"
        "v_and_b32 v131, 0xffff0000, v105\n"
        "v_and_b32 v195, 0xffff0000, v113\n"
        "v_and_b32 v155, 0xffff0000, v121\n"
        "v_lshlrev_b32 v132, 16, v106\n"
        "v_lshlrev_b32 v196, 16, v114\n"
        "v_lshlrev_b32 v156, 16, v122\n"
        "v_and_b32 v133, 0xffff0000, v106\n"
        "v_and_b32 v197, 0xffff0000, v114\n"
        "v_and_b32 v157, 0xffff0000, v122\n"
        "v_lshlrev_b32 v134, 16, v107\n"
        "v_lshlrev_b32 v198, 16, v115\n"
        "v_lshlrev_b32 v158, 16, v123\n"
        "v_and_b32 v135, 0xffff0000, v107\n"
        "v_and_b32 v199, 0xffff0000, v115\n"
        "v_and_b32 v159, 0xffff0000, v123\n"
        "v_lshlrev_b32 v136, 16, v108\n"
        "v_lshlrev_b32 v200, 16, v116\n"
        "v_lshlrev_b32 v160, 16, v124\n"
        "v_and_b32 v137, 0xffff0000, v108\n"
        "v_and_b32 v201, 0xffff0000, v116\n"
        "v_and_b32 v161, 0xffff0000, v124\n"
        "v_lshlrev_b32 v138, 16, v109\n"
        "v_lshlrev_b32 v202, 16, v117\n"
        "v_lshlrev_b32 v162, 16, v125\n"
        "v_and_b32 v139, 0xffff0000, v109\n"
        "v_and_b32 v203, 0xffff0000, v117\n"
        "v_and_b32 v163, 0xffff0000, v125\n"
        "v_lshlrev_b32 v140, 16, v110\n"
        "v_lshlrev_b32 v204, 16, v118\n"
        "v_lshlrev_b32 v164, 16, v126\n"
        "v_and_b32 v141, 0xffff0000, v110\n"
        "v_and_b32 v205, 0xffff0000, v118\n"
        "v_and_b32 v165, 0xffff0000, v126\n"
        "v_lshlrev_b32 v142, 16, v111\n"
        "v_lshlrev_b32 v206, 16, v119\n"
        "v_lshlrev_b32 v166, 16, v127\n"
        "v_and_b32 v143, 0xffff0000, v111\n"
        "v_and_b32 v207, 0xffff0000, v119\n"
        "v_and_b32 v167, 0xffff0000, v127\n"
        "v_mul_f32 v148, 0xbc800000, v144\n"
        "v_mul_f32 v212, 0xbc800000, v208\n"
        "v_mul_f32 v172, 0xbc800000, v168\n"
        "v_pk_add_f32 v[128:129], v[128:129], v[148:149] op_sel_hi:[1,0]\n"
        "v_pk_add_f32 v[192:193], v[192:193], v[212:213] op_sel_hi:[1,0]\n"
        "v_pk_add_f32 v[152:153], v[152:153], v[172:173] op_sel_hi:[1,0]\n"
        "v_pk_add_f32 v[130:131], v[130:131], v[148:149] op_sel_hi:[1,0]\n"
        "v_pk_add_f32 v[194:195], v[194:195], v[212:213] op_sel_hi:[1,0]\n"
        "v_pk_add_f32 v[154:155], v[154:155], v[172:173] op_sel_hi:[1,0]\n"
        "v_pk_add_f32 v[132:133], v[132:133], v[148:149] op_sel_hi:[1,0]\n"
        "v_pk_add_f32 v[196:197], v[196:197], v[212:213] op_sel_hi:[1,0]\n"
        "v_pk_add_f32 v[156:157], v[156:157], v[172:173] op_sel_hi:[1,0]\n"
        "v_pk_add_f32 v[134:135], v[134:135], v[148:149] op_sel_hi:[1,0]\n"
        "v_pk_add_f32 v[198:199], v[198:199], v[212:213] op_sel_hi:[1,0]\n"
        "v_pk_add_f32 v[158:159], v[158:159], v[172:173] op_sel_hi:[1,0]\n"
        "v_pk_add_f32 v[136:137], v[136:137], v[148:149] op_sel_hi:[1,0]\n"
        "v_pk_add_f32 v[200:201], v[200:201], v[212:213] op_sel_hi:[1,0]\n"
        "v_pk_add_f32 v[160:161], v[160:161], v[172:173] op_sel_hi:[1,0]\n"
        "v_pk_add_f32 v[138:139], v[138:139], v[148:149] op_sel_hi:[1,0]\n"
        "v_pk_add_f32 v[202:203], v[202:203], v[212:213] op_sel_hi:[1,0]\n"
        "v_pk_add_f32 v[162:163], v[162:163], v[172:173] op_sel_hi:[1,0]\n"
        "v_pk_add_f32 v[140:141], v[140:141], v[148:149] op_sel_hi:[1,0]\n"
        "v_pk_add_f32 v[204:205], v[204:205], v[212:213] op_sel_hi:[1,0]\n"
        "v_pk_add_f32 v[164:165], v[164:165], v[172:173] op_sel_hi:[1,0]\n"
        "v_pk_add_f32 v[142:143], v[142:143], v[148:149] op_sel_hi:[1,0]\n"
        "v_pk_add_f32 v[206:207], v[206:207], v[212:213] op_sel_hi:[1,0]\n"
        "v_pk_add_f32 v[166:167], v[166:167], v[172:173] op_sel_hi:[1,0]\n"
        "v_pk_mul_f32 v[150:151], v[128:129], v[128:129]\n"
        "v_pk_mul_f32 v[214:215], v[192:193], v[192:193]\n"
        "v_pk_mul_f32 v[174:175], v[152:153], v[152:153]\n"
        "v_pk_fma_f32 v[150:151], v[130:131], v[130:131], v[150:151]\n"
        "v_pk_fma_f32 v[214:215], v[194:195], v[194:195], v[214:215]\n"
        "v_pk_fma_f32 v[174:175], v[154:155], v[154:155], v[174:175]\n"
        "v_pk_fma_f32 v[150:151], v[132:133], v[132:133], v[150:151]\n"
        "v_pk_fma_f32 v[214:215], v[196:197], v[196:197], v[214:215]\n"
        "v_pk_fma_f32 v[174:175], v[156:157], v[156:157], v[174:175]\n"
        "v_pk_fma_f32 v[150:151], v[134:135], v[134:135], v[150:151]\n"
        "v_pk_fma_f32 v[214:215], v[198:199], v[198:199], v[214:215]\n"
        "v_pk_fma_f32 v[174:175], v[158:159], v[158:159], v[174:175]\n"
        "v_pk_fma_f32 v[150:151], v[136:137], v[136:137], v[150:151]\n"
        "v_pk_fma_f32 v[214:215], v[200:201], v[200:201], v[214:215]\n"
        "v_pk_fma_f32 v[174:175], v[160:161], v[160:161], v[174:175]\n"
        "v_pk_fma_f32 v[150:151], v[138:139], v[138:139], v[150:151]\n"
        "v_pk_fma_f32 v[214:215], v[202:203], v[202:203], v[214:215]\n"
        "v_pk_fma_f32 v[174:175], v[162:163], v[162:163], v[174:175]\n"
        "v_pk_fma_f32 v[150:151], v[140:141], v[140:141], v[150:151]\n"
        "v_pk_fma_f32 v[214:215], v[204:205], v[204:205], v[214:215]\n"
        "v_pk_fma_f32 v[174:175], v[164:165], v[164:165], v[174:175]\n"
        "v_pk_fma_f32 v[150:151], v[142:143], v[142:143], v[150:151]\n"
        "v_pk_fma_f32 v[214:215], v[206:207], v[206:207], v[214:215]\n"
        "v_pk_fma_f32 v[174:175], v[166:167], v[166:167], v[174:175]\n"
        "v_add_f32 v150, v150, v151\n"
        "v_add_f32 v214, v214, v215\n"
        "v_add_f32 v174, v174, v175\n"
        "v_mov_b32 v151, v150\n"
        "v_mov_b32 v215, v214\n"
        "v_mov_b32 v175, v174\n"
        "v_cvt_pk_bf16_f32 v104, v128, v129\n"
        "v_cvt_pk_bf16_f32 v112, v192, v193\n"
        "v_cvt_pk_bf16_f32 v120, v152, v153\n"
        "v_permlane16_swap_b32 v150, v151\n"
        "v_permlane16_swap_b32 v214, v215\n"
        "v_permlane16_swap_b32 v174, v175\n"
        "v_add_f32 v150, v150, v151\n"
        "v_add_f32 v214, v214, v215\n"
        "v_add_f32 v174, v174, v175\n"
        "v_mov_b32 v151, v150\n"
        "v_mov_b32 v215, v214\n"
        "v_mov_b32 v175, v174\n"
        "v_mov_b32 v220, 0x3727c5ac\n"
        "v_mov_b32 v222, 0x3727c5ac\n"
        "v_mov_b32 v224, 0x3727c5ac\n"
        "v_permlane32_swap_b32 v150, v151\n"
        "v_permlane32_swap_b32 v214, v215\n"
        "v_permlane32_swap_b32 v174, v175\n"
        "v_add_f32 v150, v150, v151\n"
        "s_waitcnt vmcnt(0)\n"
        "buffer_store_dwordx2 v[30:31], v254, s[36:39], 0 offen sc1\n"
        "global_load_dwordx4 v[64:67], v90, s[8:9] nt\n"
        "global_load_dwordx4 v[68:71], v91, s[8:9] nt\n"
        "global_load_dwordx2 v[72:73], v92, s[8:9] nt\n"
        "global_load_dwordx4 v[74:77], v90, s[6:7] nt\n"
        "global_load_dwordx4 v[78:81], v91, s[6:7] nt\n"
        "global_load_dwordx2 v[82:83], v92, s[6:7] nt\n"
        "v_add_f32 v214, v214, v215\n"
        "v_add_f32 v174, v174, v175\n"
        "v_fmac_f32 v220, 0x3c800000, v150\n"
        "v_fmac_f32 v222, 0x3c800000, v214\n"
        "v_fmac_f32 v224, 0x3c800000, v174\n"
        "v_rsq_f32 v220, v220\n"
        "v_rsq_f32 v222, v222\n"
        "v_rsq_f32 v224, v224\n"
        "v_cvt_pk_bf16_f32 v105, v130, v131\n"
        "v_cvt_pk_bf16_f32 v113, v194, v195\n"
        "v_cvt_pk_bf16_f32 v121, v154, v155\n"
        "v_cvt_pk_bf16_f32 v106, v132, v133\n"
        "v_cvt_pk_bf16_f32 v114, v196, v197\n"
        "v_cvt_pk_bf16_f32 v122, v156, v157\n"
        "v_cvt_pk_bf16_f32 v107, v134, v135\n"
        "v_cvt_pk_bf16_f32 v115, v198, v199\n"
        "v_cvt_pk_bf16_f32 v123, v158, v159\n"
        "v_cvt_pk_bf16_f32 v108, v136, v137\n"
        "v_cvt_pk_bf16_f32 v116, v200, v201\n"
        "v_cvt_pk_bf16_f32 v124, v160, v161\n"
        "v_cvt_pk_bf16_f32 v109, v138, v139\n"
        "v_cvt_pk_bf16_f32 v117, v202, v203\n"
        "v_cvt_pk_bf16_f32 v125, v162, v163\n"
        "v_cvt_pk_bf16_f32 v110, v140, v141\n"
        "v_cvt_pk_bf16_f32 v118, v204, v205\n"
        "v_cvt_pk_bf16_f32 v126, v164, v165\n"
        "v_cvt_pk_bf16_f32 v111, v142, v143\n"
        "v_cvt_pk_bf16_f32 v119, v206, v207\n"
        "v_cvt_pk_bf16_f32 v127, v166, v167\n"
        "v_mul_f32 v220, 0x3e38aa3b, v220\n"
        "v_mul_f32 v222, 0x3e38aa3b, v222\n"
        "v_mul_f32 v224, 0x3e38aa3b, v224\n"
        "s_branch 16f\n"
        "15:\n"
        "global_load_dwordx2 v[30:31], v30, s[16:17] sc1\n"
        "v_and_b32 v59, 15, v8\n"
        "v_and_b32 v247, 7, v9\n"
        "v_lshrrev_b32 v246, 3, v9\n"
        "s_and_b32 s60, s58, 3\n"
        "s_brev_b32 s83, 1\n"
        "s_cmp_lt_u32 s58, 250\n"
        "s_cselect_b32 s69, 0, s83\n"
        "s_mov_b32 s82, 0\n"
        "s_movk_i32 s66, 0x1000\n"
        "s_movk_i32 s67, 0x2000\n"
        "s_movk_i32 s68, 0x3000\n"
        "s_movk_i32 s70, 0x80\n"
        "v_lshlrev_b32 v248, 4, v9\n"
        "v_lshlrev_b32 v249, 4, v8\n"
        "v_lshrrev_b32 v232, 1, v59\n"
        "v_xor_b32 v232, v232, v60\n"
        "v_lshlrev_b32 v232, 4, v232\n"
        "v_lshl_add_u32 v232, v59, 7, v232\n"
        "v_add_u32 v226, s62, v232\n"
        "v_xor_b32 v227, 64, v226\n"
        "v_add_u32 v44, 146176, v248\n"
        "v_min_u32 v232, 39, v9\n"
        "v_lshlrev_b32 v57, 2, v232\n"
        "v_add_u32 v57, 163680, v57\n"
        "v_mul_u32_u24 v58, 272, v232\n"
        "v_add_u32 v58, 106496, v58\n"
        "v_mov_b32 v61, 2\n"
        "v_mov_b32 v63, 154368\n"
        "s_min_u32 s76, s58, 249\n"
        "s_mul_i32 s76, s76, 10240\n"
        "v_add_u32 v90, s76, v249\n"
        "v_add_u32 v91, 0x1000, v90\n"
        "s_add_u32 s76, s76, 0x2000\n"
        "v_lshl_add_u32 v92, v8, 3, s76\n"
        "v_mov_b32 v253, 0x3f80\n"
        "v_and_b32 v232, 3, v9\n"
        "v_cmp_eq_u32 vcc, 0, v232\n"
        "v_mov_b32 v233, 0x3f803f80\n"
        "v_and_b32 v232, 0x33, v9\n"
        "v_cndmask_b32 v233, 0, v233, vcc\n"
        "v_cmp_eq_u32 vcc, 1, v232\n"
        "v_accvgpr_write_b32 a249, v233\n"
        "v_accvgpr_write_b32 a250, v233\n"
        "v_accvgpr_write_b32 a251, v233\n"
        "v_accvgpr_write_b32 a252, v233\n"
        "v_accvgpr_write_b32 a253, v233\n"
        "v_accvgpr_write_b32 a254, v233\n"
        "v_accvgpr_write_b32 a255, v233\n"
        "v_cndmask_b32 v232, v233, v253, vcc\n"
        "v_accvgpr_write_b32 a248, v232\n"
        "s_mov_b32 s36, s16\n"
        "s_and_b32 s37, s17, 0xffff\n"
        "s_mov_b32 s38, 23757824\n"
        "s_mov_b32 s39, 0x20000\n"
        "v_cmp_eq_u32 vcc, 0, v60\n"
        "v_mov_b32 v251, 0xc1c0\n"
        "v_mov_b32 v242, 4\n"
        "v_cndmask_b32 v243, 0, -1.0, vcc\n"
        "v_cndmask_b32 v251, 0, v251, vcc\n"
        "v_lshlrev_b32 v232, 7, v59\n"
        "v_and_b32 v233, 0x600, v232\n"
        "v_add_u32 v232, v233, v226\n"
        "v_xor_b32 v233, 64, v232\n"
        "s_waitcnt vmcnt(15)\n"
        "ds_read_b128 v[128:131], v232 offset:0\n"
        "ds_read_b128 v[132:135], v233 offset:0\n"
        "ds_read_b128 v[136:139], v232 offset:512\n"
        "ds_read_b128 v[140:143], v233 offset:512\n"
        "s_waitcnt vmcnt(11)\n"
        "ds_read_b128 v[160:163], v232 offset:8192\n"
        "ds_read_b128 v[164:167], v233 offset:8192\n"
        "ds_read_b128 v[168:171], v232 offset:8704\n"
        "ds_read_b128 v[172:175], v233 offset:8704\n"
        "s_waitcnt vmcnt(7)\n"
        "ds_read_b128 v[144:147], v232 offset:4096\n"
        "ds_read_b128 v[148:151], v233 offset:4096\n"
        "ds_read_b128 v[152:155], v232 offset:4608\n"
        "ds_read_b128 v[156:159], v233 offset:4608\n"
        "s_waitcnt vmcnt(3)\n"
        "ds_read_b128 v[176:179], v232 offset:12288\n"
        "ds_read_b128 v[180:183], v233 offset:12288\n"
        "ds_read_b128 v[184:187], v232 offset:12800\n"
        "ds_read_b128 v[188:191], v233 offset:12800\n"
        "s_waitcnt vmcnt(1)\n"
        "ds_read_b128 v[112:115], v226 offset:18432\n"
        "ds_read_b128 v[116:119], v227 offset:18432\n"
        "ds_read_b128 v[104:107], v226 offset:18432\n"
        "ds_read_b128 v[108:111], v227 offset:18432\n"
        "ds_read_b128 v[120:123], v226 offset:18432\n"
        "ds_read_b128 v[124:127], v227 offset:18432\n"
        "s_waitcnt lgkmcnt(14)\n"
        "v_mfma_f32_16x16x32_bf16 v[192:195], v[128:131], v[160:163], 0\n"
        "s_mov_b64 s[40:41], s[36:37]\n"
        "v_mfma_f32_16x16x32_bf16 v[196:199], v[136:139], v[160:163], 0\n"
        "s_mov_b64 s[42:43], s[94:95]\n"
        "v_mfma_f32_16x16x32_bf16 v[192:195], v[132:135], v[164:167], v[192:195]\n"
        "s_and_b32 s72, s60, 1\n"
        "v_mfma_f32_16x16x32_bf16 v[196:199], v[140:143], v[164:167], v[196:199]\n"
        "s_lshl2_add_u32 s72, s72, s59\n"
        "v_mfma_f32_16x16x32_bf16 v[200:203], v[128:131], v[168:171], 0\n"
        "s_movk_i32 s77, 640\n"
        "v_mfma_f32_16x16x32_bf16 v[204:207], v[136:139], v[168:171], 0\n"
        "s_cmp_lt_u32 s60, 2\n"
        "v_mfma_f32_16x16x32_bf16 v[200:203], v[132:135], v[172:175], v[200:203]\n"
        "s_cselect_b32 s77, s77, 608\n"
        "v_mfma_f32_16x16x32_bf16 v[204:207], v[140:143], v[172:175], v[204:207]\n"
        "s_cselect_b32 s73, 0, 5120\n"
        "s_waitcnt lgkmcnt(13)\n"
        "v_mfma_f32_16x16x32_bf16 v[208:211], v[144:147], v[160:163], 0\n"
        "s_cselect_b32 s85, 0, s66\n"
        "v_cvt_pk_bf16_f32 v216, v192, v193\n"
        "s_waitcnt lgkmcnt(11)\n"
        "v_mfma_f32_16x16x32_bf16 v[212:215], v[152:155], v[160:163], 0\n"
        "s_mul_i32 s72, s72, s77\n"
        "v_cvt_pk_bf16_f32 v217, v194, v195\n"
        "v_mfma_f32_16x16x32_bf16 v[208:211], v[148:151], v[164:167], v[208:211]\n"
        "s_add_u32 s72, s72, s73\n"
        "v_cvt_pk_bf16_f32 v218, v196, v197\n"
        "s_waitcnt lgkmcnt(10)\n"
        "v_mfma_f32_16x16x32_bf16 v[212:215], v[156:159], v[164:167], v[212:215]\n"
        "s_lshl_b32 s73, s72, 7\n"
        "v_cvt_pk_bf16_f32 v219, v198, v199\n"
        "ds_write_b128 v44, v[216:219] offset:0\n"
        "v_mfma_f32_16x16x32_bf16 v[192:195], v[144:147], v[168:171], 0\n"
        "s_add_u32 s44, s36, s73\n"
        "v_cvt_pk_bf16_f32 v216, v200, v201\n"
        "s_waitcnt lgkmcnt(6)\n"
        "v_lshlrev_b32 v12, 16, v112\n"
        "v_mfma_f32_16x16x32_bf16 v[196:199], v[152:155], v[168:171], 0\n"
        "s_addc_u32 s45, s37, 0\n"
        "v_cvt_pk_bf16_f32 v217, v202, v203\n"
        "v_and_b32 v13, 0xffff0000, v112\n"
        "v_mfma_f32_16x16x32_bf16 v[192:195], v[148:151], v[172:175], v[192:195]\n"
        "s_lshl_b32 s46, s77, 7\n"
        "v_cvt_pk_bf16_f32 v218, v204, v205\n"
        "v_lshlrev_b32 v14, 16, v113\n"
        "v_mfma_f32_16x16x32_bf16 v[196:199], v[156:159], v[172:175], v[196:199]\n"
        "s_mov_b32 s47, s39\n"
        "v_cvt_pk_bf16_f32 v219, v206, v207\n"
        "v_and_b32 v15, 0xffff0000, v113\n"
        "ds_write_b128 v44, v[216:219] offset:2048\n"
        "v_mfma_f32_16x16x32_bf16 v[200:203], v[128:131], v[176:179], 0\n"
        "s_add_u32 s85, s85, s62\n"
        "v_cvt_pk_bf16_f32 v216, v208, v209\n"
        "v_lshlrev_b32 v16, 16, v114\n"
        "v_mfma_f32_16x16x32_bf16 v[204:207], v[136:139], v[176:179], 0\n"
        "s_mov_b32 s48, s18\n"
        "v_cvt_pk_bf16_f32 v217, v210, v211\n"
        "v_and_b32 v17, 0xffff0000, v114\n"
        "v_mfma_f32_16x16x32_bf16 v[200:203], v[132:135], v[180:183], v[200:203]\n"
        "s_and_b32 s49, s19, 0xffff\n"
        "v_cvt_pk_bf16_f32 v218, v212, v213\n"
        "v_lshlrev_b32 v18, 16, v115\n"
        "v_mfma_f32_16x16x32_bf16 v[204:207], v[140:143], v[180:183], v[204:207]\n"
        "s_mov_b64 s[50:51], s[42:43]\n"
        "v_cvt_pk_bf16_f32 v219, v214, v215\n"
        "v_and_b32 v19, 0xffff0000, v115\n"
        "ds_write_b128 v44, v[216:219] offset:1024\n"
        "v_mfma_f32_16x16x32_bf16 v[208:211], v[144:147], v[176:179], 0\n"
        "s_mul_i32 s63, s58, -160\n"
        "v_cvt_pk_bf16_f32 v216, v192, v193\n"
        "s_waitcnt lgkmcnt(7)\n"
        "v_lshlrev_b32 v20, 16, v116\n"
        "v_mfma_f32_16x16x32_bf16 v[212:215], v[152:155], v[176:179], 0\n"
        "s_add_u32 s63, s63, 203680\n"
        "v_cvt_pk_bf16_f32 v217, v194, v195\n"
        "v_and_b32 v21, 0xffff0000, v116\n"
        "v_mfma_f32_16x16x32_bf16 v[208:211], v[148:151], v[180:183], v[208:211]\n"
        "s_or_b32 s63, s63, s69\n"
        "v_cvt_pk_bf16_f32 v218, v196, v197\n"
        "v_lshlrev_b32 v22, 16, v117\n"
        "v_mfma_f32_16x16x32_bf16 v[212:215], v[156:159], v[180:183], v[212:215]\n"
        "s_mov_b32 s65, 0\n"
        "v_cvt_pk_bf16_f32 v219, v198, v199\n"
        "v_and_b32 v23, 0xffff0000, v117\n"
        "ds_write_b128 v44, v[216:219] offset:3072\n"
        "v_mfma_f32_16x16x32_bf16 v[46:49], a[248:251], v[112:115], 0\n"
        "v_mfma_f32_16x16x32_bf16 v[46:49], a[252:255], v[116:119], v[46:49]\n"
        "v_mfma_f32_16x16x32_bf16 v[192:195], v[128:131], v[184:187], 0\n"
        "s_mul_i32 s74, s72, 52429\n"
        "v_cvt_pk_bf16_f32 v216, v200, v201\n"
        "v_lshlrev_b32 v24, 16, v118\n"
        "v_mfma_f32_16x16x32_bf16 v[196:199], v[136:139], v[184:187], 0\n"
        "s_lshr_b32 s74, s74, 21\n"
        "v_cvt_pk_bf16_f32 v217, v202, v203\n"
        "v_and_b32 v25, 0xffff0000, v118\n"
        "v_mfma_f32_16x16x32_bf16 v[192:195], v[132:135], v[188:191], v[192:195]\n"
        "s_add_u32 s73, s72, s77\n"
        "v_cvt_pk_bf16_f32 v218, v204, v205\n"
        "v_lshlrev_b32 v26, 16, v119\n"
        "v_mfma_f32_16x16x32_bf16 v[196:199], v[140:143], v[188:191], v[196:199]\n"
        "s_sub_u32 s73, s73, 1\n"
        "v_cvt_pk_bf16_f32 v219, v206, v207\n"
        "v_and_b32 v27, 0xffff0000, v119\n"
        "ds_write_b128 v44, v[216:219] offset:4096\n"
        "v_mfma_f32_16x16x32_bf16 v[200:203], v[144:147], v[184:187], 0\n"
        "s_mul_i32 s73, s73, 52429\n"
        "v_cvt_pk_bf16_f32 v216, v208, v209\n"
        "v_mfma_f32_16x16x32_bf16 v[204:207], v[152:155], v[184:187], 0\n"
        "s_lshr_b32 s73, s73, 21\n"
        "v_cvt_pk_bf16_f32 v217, v210, v211\n"
        "v_mfma_f32_16x16x32_bf16 v[200:203], v[148:151], v[188:191], v[200:203]\n"
        "v_mul_u32_u24 v233, 43, v9\n"
        "v_cvt_pk_bf16_f32 v218, v212, v213\n"
        "v_mfma_f32_16x16x32_bf16 v[204:207], v[156:159], v[188:191], v[204:207]\n"
        "v_lshrrev_b32 v233, 7, v233\n"
        "v_cvt_pk_bf16_f32 v219, v214, v215\n"
        "ds_write_b128 v44, v[216:219] offset:5120\n"
        "v_add_u32 v232, s74, v233\n"
        "v_min_u32 v232, s73, v232\n"
        "v_mad_i32_i24 v233, v233, -3, v9\n"
        "v_lshlrev_b32 v233, 3, v233\n"
        "v_lshl_add_u32 v45, v232, 7, v233\n"
        "v_add_u32 v45, 1280000, v45\n"
        "s_lshl_b32 s74, s58, 7\n"
        "s_lshl3_add_u32 s74, s59, s74\n"
        "v_cvt_pk_bf16_f32 v216, v192, v193\n"
        "v_cvt_pk_bf16_f32 v217, v194, v195\n"
        "v_cvt_pk_bf16_f32 v218, v196, v197\n"
        "v_cvt_pk_bf16_f32 v219, v198, v199\n"
        "ds_write_b128 v44, v[216:219] offset:6144\n"
        "s_add_u32 s74, s74, 1280000\n"
        "s_or_b32 s74, s74, s69\n"
        "s_cmp_eq_u32 s59, 3\n"
        "s_cselect_b32 s74, s83, s74\n"
        "v_cmp_eq_u32 vcc, 0, v9\n"
        "v_mov_b32 v232, s74\n"
        "s_nop 0\n"
        "v_cndmask_b32 v254, v235, v232, vcc\n"
        "v_cvt_pk_bf16_f32 v216, v200, v201\n"
        "v_cvt_pk_bf16_f32 v217, v202, v203\n"
        "v_cvt_pk_bf16_f32 v218, v204, v205\n"
        "v_cvt_pk_bf16_f32 v219, v206, v207\n"
        "ds_write_b128 v44, v[216:219] offset:7168\n"
        "v_mul_f32 v50, 0xbc800000, v46\n"
        "v_pk_add_f32 v[12:13], v[12:13], v[50:51] op_sel_hi:[1,0]\n"
        "v_pk_add_f32 v[14:15], v[14:15], v[50:51] op_sel_hi:[1,0]\n"
        "v_pk_add_f32 v[16:17], v[16:17], v[50:51] op_sel_hi:[1,0]\n"
        "v_pk_add_f32 v[18:19], v[18:19], v[50:51] op_sel_hi:[1,0]\n"
        "v_pk_add_f32 v[20:21], v[20:21], v[50:51] op_sel_hi:[1,0]\n"
        "v_pk_add_f32 v[22:23], v[22:23], v[50:51] op_sel_hi:[1,0]\n"
        "v_pk_add_f32 v[24:25], v[24:25], v[50:51] op_sel_hi:[1,0]\n"
        "v_pk_add_f32 v[26:27], v[26:27], v[50:51] op_sel_hi:[1,0]\n"
        "v_pk_mul_f32 v[52:53], v[12:13], v[12:13]\n"
        "v_pk_fma_f32 v[52:53], v[14:15], v[14:15], v[52:53]\n"
        "v_pk_fma_f32 v[52:53], v[16:17], v[16:17], v[52:53]\n"
        "v_pk_fma_f32 v[52:53], v[18:19], v[18:19], v[52:53]\n"
        "v_pk_fma_f32 v[52:53], v[20:21], v[20:21], v[52:53]\n"
        "v_pk_fma_f32 v[52:53], v[22:23], v[22:23], v[52:53]\n"
        "v_pk_fma_f32 v[52:53], v[24:25], v[24:25], v[52:53]\n"
        "v_pk_fma_f32 v[52:53], v[26:27], v[26:27], v[52:53]\n"
        "v_add_f32 v52, v52, v53\n"
        "v_mov_b32 v53, v52\n"
        "v_cvt_pk_bf16_f32 v112, v12, v13\n"
        "s_nop 0\n"
        "v_permlane16_swap_b32 v52, v53\n"
        "v_add_f32 v52, v52, v53\n"
        "v_mov_b32 v53, v52\n"
        "v_mov_b32 v222, 0x3727c5ac\n"
        "s_nop 0\n"
        "v_permlane32_swap_b32 v52, v53\n"
        "v_add_f32 v52, v52, v53\n"
        "v_fmac_f32 v222, 0x3c800000, v52\n"
        "v_rsq_f32 v222, v222\n"
        "v_cvt_pk_bf16_f32 v113, v14, v15\n"
        "v_cvt_pk_bf16_f32 v114, v16, v17\n"
        "v_cvt_pk_bf16_f32 v115, v18, v19\n"
        "v_cvt_pk_bf16_f32 v116, v20, v21\n"
        "v_cvt_pk_bf16_f32 v117, v22, v23\n"
        "v_cvt_pk_bf16_f32 v118, v24, v25\n"
        "v_cvt_pk_bf16_f32 v119, v26, v27\n"
        "v_mul_f32 v222, 0x3e38aa3b, v222\n"
        "v_mov_b32 v220, 0\n"
        "v_mov_b32 v224, 0\n"
        "s_waitcnt vmcnt(0)\n"
        "v_readfirstlane_b32 s52, v30\n"
        "v_readfirstlane_b32 s53, v31\n"
        "s_add_u32 s52, s52, 1\n"
        "s_addc_u32 s53, s53, 0\n"
        "s_mov_b32 s54, s52\n"
        "s_and_b32 s55, s53, 0xff\n"
        "s_lshl_b64 s[56:57], s[52:53], 24\n"
        "v_mov_b32 v30, s56\n"
        "v_mov_b32 v31, s57\n"
        "buffer_store_dwordx2 v[30:31], v254, s[36:39], 0 offen sc1\n"
        "global_load_dwordx4 v[64:67], v90, s[8:9] nt\n"
        "global_load_dwordx4 v[68:71], v91, s[8:9] nt\n"
        "global_load_dwordx2 v[72:73], v92, s[8:9] nt\n"
        "global_load_dwordx4 v[74:77], v90, s[6:7] nt\n"
        "global_load_dwordx4 v[78:81], v91, s[6:7] nt\n"
        "global_load_dwordx2 v[82:83], v92, s[6:7] nt\n"
        "16:\n"
        "s_waitcnt lgkmcnt(0)\n"
        "s_barrier\n"
        "ds_read_b128 v[128:131], v44 offset:0\n"
        "ds_read_b128 v[136:139], v44 offset:2048\n"
        "ds_read_b128 v[144:147], v44 offset:4096\n"
        "ds_read_b128 v[152:155], v44 offset:6144\n"
        "ds_read_b128 v[132:135], v44 offset:1024\n"
        "ds_read_b128 v[140:143], v44 offset:3072\n"
        "ds_read_b128 v[148:151], v44 offset:5120\n"
        "ds_read_b128 v[156:159], v44 offset:7168\n"
        "s_cmp_eq_u32 s59, 3\n"
        "s_cselect_b32 s74, s83, 0\n"
        "v_lshrrev_b32 v232, 1, v60\n"
        "v_mul_u32_u24 v232, 29696, v232\n"
        "v_lshl_add_u32 v232, v60, 9, v232\n"
        "v_lshl_add_u32 v232, v247, 4, v232\n"
        "v_add_u32 v232, 20480, v232\n"
        "s_mul_i32 s76, s60, -5\n"
        "s_lshl1_add_u32 s76, s59, s76\n"
        "v_lshrrev_b32 v234, 3, v59\n"
        "v_add_u32 v234, s76, v234\n"
        "v_add_u32 v244, 16, v234\n"
        "v_add_u32 v233, -4, v234\n"
        "v_min_u32 v244, v244, v233\n"
        "v_and_b32 v233, 28, v244\n"
        "v_add_u32 v244, v244, v233\n"
        "s_waitcnt lgkmcnt(7)\n"
        "v_mfma_f32_16x16x32_bf16 v[160:163], v[128:131], v[104:107], 0\n"
        "v_lshl_add_u32 v244, v244, 7, v232\n"
        "v_or_b32 v244, s74, v244\n"
        "s_waitcnt lgkmcnt(6)\n"
        "v_mfma_f32_16x16x32_bf16 v[164:167], v[136:139], v[104:107], 0\n"
        "v_add_u32 v245, 22, v234\n"
        "v_add_u32 v233, 2, v234\n"
        "s_waitcnt lgkmcnt(5)\n"
        "v_mfma_f32_16x16x32_bf16 v[168:171], v[144:147], v[104:107], 0\n"
        "v_min_u32 v245, v245, v233\n"
        "v_and_b32 v233, 28, v245\n"
        "s_waitcnt lgkmcnt(4)\n"
        "v_mfma_f32_16x16x32_bf16 v[172:175], v[152:155], v[104:107], 0\n"
        "v_add_u32 v245, v245, v233\n"
        "v_lshl_add_u32 v245, v245, 7, v232\n"
        "s_waitcnt lgkmcnt(3)\n"
        "v_mfma_f32_16x16x32_bf16 v[160:163], v[132:135], v[108:111], v[160:163]\n"
        "v_add_u32 v250, 30, v234\n"
        "v_add_u32 v233, 10, v234\n"
        "s_waitcnt lgkmcnt(2)\n"
        "v_mfma_f32_16x16x32_bf16 v[164:167], v[140:143], v[108:111], v[164:167]\n"
        "v_min_u32 v250, v250, v233\n"
        "v_and_b32 v233, 28, v250\n"
        "s_waitcnt lgkmcnt(1)\n"
        "v_mfma_f32_16x16x32_bf16 v[168:171], v[148:151], v[108:111], v[168:171]\n"
        "v_add_u32 v250, v250, v233\n"
        "v_lshl_add_u32 v250, v250, 7, v232\n"
        "s_waitcnt lgkmcnt(0)\n"
        "v_mfma_f32_16x16x32_bf16 v[172:175], v[156:159], v[108:111], v[172:175]\n"
        "v_or_b32 v250, s74, v250\n"
        "v_lshrrev_b32 v234, 5, v9\n"
        "v_mfma_f32_16x16x32_bf16 v[176:179], v[128:131], v[112:115], 0\n"
        "v_lshrrev_b32 v232, 2, v9\n"
        "v_and_or_b32 v233, v232, 4, v234\n"
        "v_mfma_f32_16x16x32_bf16 v[180:183], v[136:139], v[112:115], 0\n"
        "v_xor_b32 v233, v233, v9\n"
        "v_lshlrev_b32 v10, 4, v233\n"
        "v_mfma_f32_16x16x32_bf16 v[184:187], v[144:147], v[112:115], 0\n"
        "v_xor_b32 v11, 32, v10\n"
        "v_and_b32 v232, 3, v232\n"
        "v_mfma_f32_16x16x32_bf16 v[188:191], v[152:155], v[112:115], 0\n"
        "v_lshlrev_b32 v233, 1, v9\n"
        "v_and_or_b32 v233, v233, 4, v232\n"
        "v_mfma_f32_16x16x32_bf16 v[176:179], v[132:135], v[116:119], v[176:179]\n"
        "v_xor_b32 v233, v233, v234\n"
        "v_and_b32 v12, 31, v9\n"
        "v_mfma_f32_16x16x32_bf16 v[180:183], v[140:143], v[116:119], v[180:183]\n"
        "v_lshl_add_u32 v12, v12, 3, v233\n"
        "v_lshl_add_u32 v12, v12, 4, s62\n"
        "v_mfma_f32_16x16x32_bf16 v[184:187], v[148:151], v[116:119], v[184:187]\n"
        "v_xor_b32 v13, 32, v12\n"
        "v_xor_b32 v14, 64, v12\n"
        "v_mfma_f32_16x16x32_bf16 v[188:191], v[156:159], v[116:119], v[188:191]\n"
        "v_xor_b32 v15, 96, v12\n"
        "v_lshl_add_u32 v16, v234, 2, v232\n"
        "v_mfma_f32_16x16x32_bf16 v[192:195], v[128:131], v[120:123], 0\n"
        "v_lshrrev_b32 v233, 1, v9\n"
        "v_and_b32 v233, 5, v233\n"
        "v_mfma_f32_16x16x32_bf16 v[196:199], v[136:139], v[120:123], 0\n"
        "v_and_or_b32 v233, v246, 2, v233\n"
        "v_xor_b32 v233, v233, v234\n"
        "v_mfma_f32_16x16x32_bf16 v[200:203], v[144:147], v[120:123], 0\n"
        "v_lshl_add_u32 v16, v16, 3, v233\n"
        "v_and_b32 v232, 1, v9\n"
        "v_mfma_f32_16x16x32_bf16 v[204:207], v[152:155], v[120:123], 0\n"
        "v_lshl_add_u32 v16, v16, 1, v232\n"
        "v_lshl_add_u32 v16, v16, 3, s62\n"
        "v_mfma_f32_16x16x32_bf16 v[192:195], v[132:135], v[124:127], v[192:195]\n"
        "v_xor_b32 v17, 1056, v16\n"
        "v_xor_b32 v18, 64, v16\n"
        "v_mfma_f32_16x16x32_bf16 v[196:199], v[140:143], v[124:127], v[196:199]\n"
        "v_xor_b32 v19, 1120, v16\n"
        "v_add_u32 v20, s62, v248\n"
        "v_mfma_f32_16x16x32_bf16 v[200:203], v[148:151], v[124:127], v[200:203]\n"
        "s_lshl_b32 s78, s59, 7\n"
        "v_xor_b32 v22, s78, v248\n"
        "v_mfma_f32_16x16x32_bf16 v[204:207], v[156:159], v[124:127], v[204:207]\n"
        "v_add_u32 v23, 61440, v22\n"
        "v_pk_add_f32 v[208:209], v[160:161], v[162:163]\n"
        "v_pk_add_f32 v[210:211], v[176:177], v[178:179]\n"
        "v_pk_add_f32 v[212:213], v[192:193], v[194:195]\n"
        "v_pk_add_f32 v[208:209], v[208:209], v[164:165]\n"
        "v_pk_add_f32 v[210:211], v[210:211], v[180:181]\n"
        "v_pk_add_f32 v[212:213], v[212:213], v[196:197]\n"
        "v_pk_add_f32 v[208:209], v[208:209], v[166:167]\n"
        "v_pk_add_f32 v[210:211], v[210:211], v[182:183]\n"
        "v_pk_add_f32 v[212:213], v[212:213], v[198:199]\n"
        "v_pk_add_f32 v[208:209], v[208:209], v[168:169]\n"
        "v_pk_add_f32 v[210:211], v[210:211], v[184:185]\n"
        "v_pk_add_f32 v[212:213], v[212:213], v[200:201]\n"
        "v_pk_add_f32 v[208:209], v[208:209], v[170:171]\n"
        "v_pk_add_f32 v[210:211], v[210:211], v[186:187]\n"
        "v_pk_add_f32 v[212:213], v[212:213], v[202:203]\n"
        "v_pk_add_f32 v[208:209], v[208:209], v[172:173]\n"
        "v_pk_add_f32 v[210:211], v[210:211], v[188:189]\n"
        "v_pk_add_f32 v[212:213], v[212:213], v[204:205]\n"
        "v_pk_add_f32 v[208:209], v[208:209], v[174:175]\n"
        "v_pk_add_f32 v[210:211], v[210:211], v[190:191]\n"
        "v_pk_add_f32 v[212:213], v[212:213], v[206:207]\n"
        "v_add_f32 v208, v208, v209\n"
        "v_add_f32 v210, v210, v211\n"
        "v_add_f32 v212, v212, v213\n"
        "v_mul_f32 v208, 0x3e000000, v208\n"
        "v_mul_f32 v210, 0x3e000000, v210\n"
        "v_mul_f32 v212, 0x3e000000, v212\n"
        "v_fmac_f32 v208, v160, v243\n"
        "v_fmac_f32 v210, v176, v243\n"
        "v_fmac_f32 v212, v192, v243\n"
        "v_mov_b32 v209, v208\n"
        "v_mov_b32 v211, v210\n"
        "v_mov_b32 v213, v212\n"
        "v_permlane16_swap_b32 v208, v209\n"
        "v_permlane16_swap_b32 v210, v211\n"
        "v_permlane16_swap_b32 v212, v213\n"
        "v_add_f32 v208, v208, v209\n"
        "v_add_f32 v210, v210, v211\n"
        "v_add_f32 v212, v212, v213\n"
        "v_mov_b32 v209, v208\n"
        "v_mov_b32 v211, v210\n"
        "v_mov_b32 v213, v212\n"
        "v_permlane32_swap_b32 v208, v209\n"
        "v_permlane32_swap_b32 v210, v211\n"
        "v_permlane32_swap_b32 v212, v213\n"
        "v_add_f32 v208, v208, v209\n"
        "v_add_f32 v210, v210, v211\n"
        "v_add_f32 v212, v212, v213\n"
        "v_mul_f32 v208, 0xbe124925, v208\n"
        "v_mul_f32 v210, 0xbe124925, v210\n"
        "v_mul_f32 v212, 0xbe124925, v212\n"
        "v_mul_f32 v208, v208, v220\n"
        "v_mul_f32 v210, v210, v222\n"
        "v_mul_f32 v212, v212, v224\n"
        "v_pk_fma_f32 v[160:161], v[160:161], v[220:221], v[208:209] op_sel_hi:[1,0,0]\n"
        "v_fmac_f32 v160, v160, v243\n"
        "v_cvt_pk_bf16_f32 v104, v160, v161\n"
        "v_or_b32 v104, v104, v251\n"
        "v_pk_fma_f32 v[162:163], v[162:163], v[220:221], v[208:209] op_sel_hi:[1,0,0]\n"
        "v_cvt_pk_bf16_f32 v105, v162, v163\n"
        "v_pk_fma_f32 v[164:165], v[164:165], v[220:221], v[208:209] op_sel_hi:[1,0,0]\n"
        "v_cvt_pk_bf16_f32 v106, v164, v165\n"
        "v_pk_fma_f32 v[166:167], v[166:167], v[220:221], v[208:209] op_sel_hi:[1,0,0]\n"
        "v_cvt_pk_bf16_f32 v107, v166, v167\n"
        "ds_write_b128 v244, v[104:107] offset:0\n"
        "v_pk_fma_f32 v[168:169], v[168:169], v[220:221], v[208:209] op_sel_hi:[1,0,0]\n"
        "v_cvt_pk_bf16_f32 v108, v168, v169\n"
        "v_pk_fma_f32 v[170:171], v[170:171], v[220:221], v[208:209] op_sel_hi:[1,0,0]\n"
        "v_cvt_pk_bf16_f32 v109, v170, v171\n"
        "v_pk_fma_f32 v[172:173], v[172:173], v[220:221], v[208:209] op_sel_hi:[1,0,0]\n"
        "v_cvt_pk_bf16_f32 v110, v172, v173\n"
        "v_pk_fma_f32 v[174:175], v[174:175], v[220:221], v[208:209] op_sel_hi:[1,0,0]\n"
        "v_cvt_pk_bf16_f32 v111, v174, v175\n"
        "ds_write_b128 v244, v[108:111] offset:61440\n"
        "v_pk_fma_f32 v[176:177], v[176:177], v[222:223], v[210:211] op_sel_hi:[1,0,0]\n"
        "v_fmac_f32 v176, v176, v243\n"
        "v_cvt_pk_bf16_f32 v112, v176, v177\n"
        "v_or_b32 v112, v112, v251\n"
        "v_pk_fma_f32 v[178:179], v[178:179], v[222:223], v[210:211] op_sel_hi:[1,0,0]\n"
        "v_cvt_pk_bf16_f32 v113, v178, v179\n"
        "v_pk_fma_f32 v[180:181], v[180:181], v[222:223], v[210:211] op_sel_hi:[1,0,0]\n"
        "v_cvt_pk_bf16_f32 v114, v180, v181\n"
        "v_pk_fma_f32 v[182:183], v[182:183], v[222:223], v[210:211] op_sel_hi:[1,0,0]\n"
        "v_cvt_pk_bf16_f32 v115, v182, v183\n"
        "ds_write_b128 v245, v[112:115] offset:0\n"
        "v_pk_fma_f32 v[184:185], v[184:185], v[222:223], v[210:211] op_sel_hi:[1,0,0]\n"
        "v_cvt_pk_bf16_f32 v116, v184, v185\n"
        "v_pk_fma_f32 v[186:187], v[186:187], v[222:223], v[210:211] op_sel_hi:[1,0,0]\n"
        "v_cvt_pk_bf16_f32 v117, v186, v187\n"
        "v_pk_fma_f32 v[188:189], v[188:189], v[222:223], v[210:211] op_sel_hi:[1,0,0]\n"
        "v_cvt_pk_bf16_f32 v118, v188, v189\n"
        "v_pk_fma_f32 v[190:191], v[190:191], v[222:223], v[210:211] op_sel_hi:[1,0,0]\n"
        "v_cvt_pk_bf16_f32 v119, v190, v191\n"
        "ds_write_b128 v245, v[116:119] offset:61440\n"
        "v_pk_fma_f32 v[192:193], v[192:193], v[224:225], v[212:213] op_sel_hi:[1,0,0]\n"
        "v_fmac_f32 v192, v192, v243\n"
        "v_cvt_pk_bf16_f32 v120, v192, v193\n"
        "v_or_b32 v120, v120, v251\n"
        "v_pk_fma_f32 v[194:195], v[194:195], v[224:225], v[212:213] op_sel_hi:[1,0,0]\n"
        "v_cvt_pk_bf16_f32 v121, v194, v195\n"
        "buffer_load_dwordx2 v[216:217], v45, s[36:39], 0 offen sc1\n"
        "v_pk_fma_f32 v[196:197], v[196:197], v[224:225], v[212:213] op_sel_hi:[1,0,0]\n"
        "v_cvt_pk_bf16_f32 v122, v196, v197\n"
        "v_pk_fma_f32 v[198:199], v[198:199], v[224:225], v[212:213] op_sel_hi:[1,0,0]\n"
        "v_cvt_pk_bf16_f32 v123, v198, v199\n"
        "ds_write_b128 v250, v[120:123] offset:0\n"
        "v_pk_fma_f32 v[200:201], v[200:201], v[224:225], v[212:213] op_sel_hi:[1,0,0]\n"
        "v_cvt_pk_bf16_f32 v124, v200, v201\n"
        "v_pk_fma_f32 v[202:203], v[202:203], v[224:225], v[212:213] op_sel_hi:[1,0,0]\n"
        "v_cvt_pk_bf16_f32 v125, v202, v203\n"
        "v_pk_fma_f32 v[204:205], v[204:205], v[224:225], v[212:213] op_sel_hi:[1,0,0]\n"
        "v_cvt_pk_bf16_f32 v126, v204, v205\n"
        "v_pk_fma_f32 v[206:207], v[206:207], v[224:225], v[212:213] op_sel_hi:[1,0,0]\n"
        "v_cvt_pk_bf16_f32 v127, v206, v207\n"
        "ds_write_b128 v250, v[124:127] offset:61440\n"
        "s_mov_b32 s72, 144128\n"
        "s_mov_b32 s73, 40000\n"
        "s_waitcnt vmcnt(4)\n"
        "v_mul_u32_u24 v84, 52429, v64\n"
        "v_lshrrev_b32 v84, 21, v84\n"
        "v_lshl_add_u32 v84, v84, 2, s72\n"
        "v_mfma_f32_16x16x32_bf16 v[160:163], v[104:107], v[104:107], 0\n"
        "ds_add_rtn_u32 v94, v84, v242\n"
        "v_mul_u32_u24 v85, 52429, v65\n"
        "v_lshrrev_b32 v85, 21, v85\n"
        "v_lshl_add_u32 v85, v85, 2, s72\n"
        "v_mad_i32_i24 v64, v64, -4, s73\n"
        "v_mfma_f32_16x16x32_bf16 v[160:163], v[108:111], v[108:111], v[160:163]\n"
        "ds_add_rtn_u32 v95, v85, v242\n"
        "v_mul_u32_u24 v86, 52429, v66\n"
        "v_lshrrev_b32 v86, 21, v86\n"
        "v_lshl_add_u32 v86, v86, 2, s72\n"
        "v_mad_i32_i24 v65, v65, -4, s73\n"
        "v_mfma_f32_16x16x32_bf16 v[176:179], v[112:115], v[112:115], 0\n"
        "ds_add_rtn_u32 v96, v86, v242\n"
        "v_mul_u32_u24 v87, 52429, v67\n"
        "v_lshrrev_b32 v87, 21, v87\n"
        "v_lshl_add_u32 v87, v87, 2, s72\n"
        "v_mad_i32_i24 v66, v66, -4, s73\n"
        "v_mfma_f32_16x16x32_bf16 v[176:179], v[116:119], v[116:119], v[176:179]\n"
        "ds_add_rtn_u32 v97, v87, v242\n"
        "v_mul_u32_u24 v88, 52429, v68\n"
        "v_lshrrev_b32 v88, 21, v88\n"
        "v_lshl_add_u32 v88, v88, 2, s72\n"
        "v_mad_i32_i24 v67, v67, -4, s73\n"
        "v_mfma_f32_16x16x32_bf16 v[192:195], v[120:123], v[120:123], 0\n"
        "ds_add_rtn_u32 v98, v88, v242\n"
        "v_mul_u32_u24 v89, 52429, v69\n"
        "v_lshrrev_b32 v89, 21, v89\n"
        "v_lshl_add_u32 v89, v89, 2, s72\n"
        "v_mad_i32_i24 v68, v68, -4, s73\n"
        "v_mfma_f32_16x16x32_bf16 v[192:195], v[124:127], v[124:127], v[192:195]\n"
        "ds_add_rtn_u32 v99, v89, v242\n"
        "v_mul_u32_u24 v90, 52429, v70\n"
        "v_lshrrev_b32 v90, 21, v90\n"
        "v_lshl_add_u32 v90, v90, 2, s72\n"
        "v_mad_i32_i24 v69, v69, -4, s73\n"
        "ds_add_rtn_u32 v100, v90, v242\n"
        "v_mul_u32_u24 v91, 52429, v71\n"
        "v_lshrrev_b32 v91, 21, v91\n"
        "v_lshl_add_u32 v91, v91, 2, s72\n"
        "v_mad_i32_i24 v70, v70, -4, s73\n"
        "ds_add_rtn_u32 v101, v91, v242\n"
        "v_mul_u32_u24 v92, 52429, v72\n"
        "v_lshrrev_b32 v92, 21, v92\n"
        "v_lshl_add_u32 v92, v92, 2, s72\n"
        "v_mad_i32_i24 v71, v71, -4, s73\n"
        "ds_add_rtn_u32 v102, v92, v242\n"
        "v_mul_u32_u24 v93, 52429, v73\n"
        "v_lshrrev_b32 v93, 21, v93\n"
        "v_lshl_add_u32 v93, v93, 2, s72\n"
        "v_mad_i32_i24 v72, v72, -4, s73\n"
        "ds_add_rtn_u32 v103, v93, v242\n"
        "v_mad_i32_i24 v73, v73, -4, s73\n"
        "v_max3_f32 v232, v160, v161, v162\n"
        "v_max3_f32 v232, v232, v163, v176\n"
        "v_max3_f32 v232, v232, v177, v178\n"
        "v_max3_f32 v232, v232, v179, v192\n"
        "v_max3_f32 v232, v232, v193, v194\n"
        "v_max_f32 v232, v232, v195\n"
        "v_cmp_lt_f32 vcc, 0x44288000, v232\n"
        "v_add_u32 v234, 144128, v248\n"
        "v_lshl_add_u32 v233, v8, 2, s72\n"
        "v_cndmask_b32 v232, v235, v63, vcc\n"
        "ds_write_b32 v232, v61\n"
        "s_waitcnt lgkmcnt(0)\n"
        "s_barrier\n"
        "ds_read_b128 v[46:49], v234\n"
        "ds_read_b32 v214, v63\n"
        "s_mov_b32 s71, 0x100000\n"
        "s_waitcnt vmcnt(0)\n"
        "v_lshrrev_b64 v[218:219], 24, v[216:217]\n"
        "v_cmp_ne_u64 vcc, s[54:55], v[218:219]\n"
        "s_mov_b32 m0, s85\n"
        "s_cbranch_vccz 19f\n"
        "10:\n"
        "s_sleep 2\n"
        "buffer_load_dwordx2 v[216:217], v45, s[36:39], 0 offen sc1\n"
        "s_sub_u32 s71, s71, 1\n"
        "s_waitcnt vmcnt(0)\n"
        "s_cbranch_scc1 19f\n"
        "v_lshrrev_b64 v[218:219], 24, v[216:217]\n"
        "v_cmp_ne_u64 vcc, s[54:55], v[218:219]\n"
        "s_cbranch_vccnz 10b\n"
        "19:\n"
        "buffer_load_dwordx4 v10, s[44:47], s65 offen offset:0 sc1 lds\n"
        "buffer_load_dwordx4 v11, s[44:47], s65 offen offset:1024 sc1 lds\n"
        "buffer_load_dwordx4 v10, s[44:47], s65 offen offset:2048 sc1 lds\n"
        "buffer_load_dwordx4 v11, s[44:47], s65 offen offset:3072 sc1 lds\n"
        "s_add_u32 s65, s65, s66\n"
        "s_add_u32 m0, s85, 4096\n"
        "s_nop 0\n"
        "buffer_load_dwordx4 v10, s[44:47], s65 offen offset:0 sc1 lds\n"
        "buffer_load_dwordx4 v11, s[44:47], s65 offen offset:1024 sc1 lds\n"
        "buffer_load_dwordx4 v10, s[44:47], s65 offen offset:2048 sc1 lds\n"
        "buffer_load_dwordx4 v11, s[44:47], s65 offen offset:3072 sc1 lds\n"
        "s_add_u32 s65, s65, s66\n"
        "ds_read_b128 a[160:163], v22 offset:20480\n"
        "ds_read_b128 a[164:167], v22 offset:51200\n"
        "ds_read_b128 a[168:171], v23 offset:20480\n"
        "ds_read_b128 a[172:175], v23 offset:51200\n"
        "s_waitcnt lgkmcnt(5)\n"
        "v_add_u32 v47, v46, v47\n"
        "v_add_u32 v48, v47, v48\n"
        "v_add_u32 v49, v48, v49\n"
        "v_mov_b32 v50, v49\n"
        "s_nop 1\n"
        "v_add_u32_dpp v50, v50, v50 row_shr:1 row_mask:0xf bank_mask:0xf bound_ctrl:0\n"
        "v_add_u32 v24, 133760, v249\n"
        "s_mul_i32 s77, s58, 10368\n"
        "v_add_u32_dpp v50, v50, v50 row_shr:2 row_mask:0xf bank_mask:0xf bound_ctrl:0\n"
        "s_add_u32 s77, s77, 1842944\n"
        "s_or_b32 s77, s77, s69\n"
        "v_add_u32_dpp v50, v50, v50 row_shr:4 row_mask:0xf bank_mask:0xf bound_ctrl:0\n"
        "v_add_u32 v25, s77, v249\n"
        "v_cmp_gt_u32 vcc, 136, v8\n"
        "v_add_u32_dpp v50, v50, v50 row_shr:8 row_mask:0xf bank_mask:0xf bound_ctrl:0\n"
        "s_nop 0\n"
        "v_cndmask_b32 v26, v235, v25, vcc\n"
        "s_mul_i32 s77, s58, 15360\n"
        "v_add_u32_dpp v50, v50, v50 row_bcast:15 row_mask:0xa bank_mask:0xf\n"
        "s_add_u32 s77, s77, 4434944\n"
        "s_or_b32 s78, s77, s69\n"
        "v_add_u32_dpp v50, v50, v50 row_bcast:31 row_mask:0xc bank_mask:0xf\n"
        "v_add_u32 v27, s78, v249\n"
        "v_cmp_gt_u32 vcc, 192, v8\n"
        "v_sub_u32 v50, v50, v49\n"
        "v_add_u32 v50, 133760, v50\n"
        "v_add_u32 v51, v50, v46\n"
        "v_add_u32 v52, v50, v47\n"
        "v_add_u32 v53, v50, v48\n"
        "ds_write_b128 v234, v[50:53] offset:1024\n"
        "ds_read_b32 v84, v84 offset:1024\n"
        "ds_read_b32 v85, v85 offset:1024\n"
        "ds_read_b32 v86, v86 offset:1024\n"
        "ds_read_b32 v87, v87 offset:1024\n"
        "ds_read_b32 v88, v88 offset:1024\n"
        "ds_read_b32 v89, v89 offset:1024\n"
        "ds_read_b32 v90, v90 offset:1024\n"
        "ds_read_b32 v91, v91 offset:1024\n"
        "ds_read_b32 v92, v92 offset:1024\n"
        "ds_read_b32 v93, v93 offset:1024\n"
        "ds_read2st64_b32 v[228:229], v233 offset0:0 offset1:4\n"
        "ds_read_b128 a[176:179], v22 offset:21504\n"
        "ds_read_b128 a[180:183], v22 offset:52224\n"
        "ds_read_b128 a[184:187], v23 offset:21504\n"
        "ds_read_b128 a[188:191], v23 offset:52224\n"
        "v_cndmask_b32 v28, v235, v27, vcc\n"
        "s_lshl_b32 s77, s58, 11\n"
        "s_add_u32 s77, s77, 1330944\n"
        "s_or_b32 s77, s77, s69\n"
        "v_lshl_add_u32 v29, v8, 3, s77\n"
        "v_lshl_or_b32 v74, v74, 16, v64\n"
        "v_lshl_or_b32 v75, v75, 16, v65\n"
        "v_lshl_or_b32 v76, v76, 16, v66\n"
        "v_lshl_or_b32 v77, v77, 16, v67\n"
        "v_lshl_or_b32 v78, v78, 16, v68\n"
        "v_lshl_or_b32 v79, v79, 16, v69\n"
        "v_lshl_or_b32 v80, v80, 16, v70\n"
        "v_lshl_or_b32 v81, v81, 16, v71\n"
        "v_lshl_or_b32 v82, v82, 16, v72\n"
        "v_lshl_or_b32 v83, v83, 16, v73\n"
        "s_waitcnt lgkmcnt(4)\n"
        "v_add_u32 v84, v84, v94\n"
        "v_add_u32 v85, v85, v95\n"
        "v_add_u32 v86, v86, v96\n"
        "v_add_u32 v87, v87, v97\n"
        "v_add_u32 v88, v88, v98\n"
        "v_add_u32 v89, v89, v99\n"
        "v_add_u32 v90, v90, v100\n"
        "v_add_u32 v91, v91, v101\n"
        "v_add_u32 v92, v92, v102\n"
        "v_add_u32 v93, v93, v103\n"
        "v_subrev_u32 v229, 133760, v229\n"
        "v_lshrrev_b32 v229, 2, v229\n"
        "v_lshl_or_b32 v228, v228, 10, v229\n"
        "v_or_b32 v30, s56, v228\n"
        "v_cmp_ne_u32 vcc, 0, v214\n"
        "ds_write_b32 v84, v74\n"
        "ds_write_b32 v85, v75\n"
        "ds_write_b32 v86, v76\n"
        "ds_write_b32 v87, v77\n"
        "ds_write_b32 v88, v78\n"
        "ds_write_b32 v89, v79\n"
        "ds_write_b32 v90, v80\n"
        "ds_write_b32 v91, v81\n"
        "ds_write_b32 v92, v82\n"
        "ds_write_b32 v93, v83\n"
        "s_cmp_lt_u32 s60, 2\n"
        "s_cbranch_vccnz 30f\n"
        "s_cbranch_scc1 33f\n"
        "s_waitcnt vmcnt(4)\n"
        "ds_read_b128 v[128:131], v12 offset:4096\n"
        "ds_read_b128 v[132:135], v13 offset:4096\n"
        "ds_read_b128 v[136:139], v14 offset:4096\n"
        "ds_read_b128 v[140:143], v15 offset:4096\n"
        "s_waitcnt lgkmcnt(3)\n"
        "v_mfma_f32_32x32x16_bf16 v[80:95], v[128:131], a[160:163], 0\n"
        "ds_read_b64_tr_b16 v[160:161], v16 offset:4096\n"
        "ds_read_b64_tr_b16 v[162:163], v17 offset:4096\n"
        "ds_read_b64_tr_b16 v[164:165], v18 offset:4096\n"
        "ds_read_b64_tr_b16 v[166:167], v19 offset:4096\n"
        "s_waitcnt lgkmcnt(6)\n"
        "v_mfma_f32_32x32x16_bf16 v[80:95], v[132:135], a[164:167], v[80:95]\n"
        "ds_read_b64_tr_b16 v[168:169], v16 offset:6144\n"
        "ds_read_b64_tr_b16 v[170:171], v17 offset:6144\n"
        "ds_read_b64_tr_b16 v[172:173], v18 offset:6144\n"
        "ds_read_b64_tr_b16 v[174:175], v19 offset:6144\n"
        "s_waitcnt lgkmcnt(9)\n"
        "v_mfma_f32_32x32x16_bf16 v[80:95], v[136:139], a[168:171], v[80:95]\n"
        "ds_read_b128 a[192:195], v22 offset:22528\n"
        "ds_read_b128 a[196:199], v22 offset:53248\n"
        "s_waitcnt lgkmcnt(10)\n"
        "v_mfma_f32_32x32x16_bf16 v[80:95], v[140:143], a[172:175], v[80:95]\n"
        "ds_read_b128 a[200:203], v23 offset:22528\n"
        "ds_read_b128 a[204:207], v23 offset:53248\n"
        "v_mfma_f32_32x32x16_bf16 v[64:79], v[128:131], a[176:179], 0\n"
        "s_add_u32 m0, s62, 12288\n"
        "ds_read_b128 a[208:211], v22 offset:23552\n"
        "ds_read_b128 a[212:215], v22 offset:54272\n"
        "ds_read_b128 a[216:219], v23 offset:23552\n"
        "v_mfma_f32_32x32x16_bf16 v[64:79], v[132:135], a[180:183], v[64:79]\n"
        "ds_read_b128 a[220:223], v23 offset:54272\n"
        "ds_read_b128 a[224:227], v22 offset:24576\n"
        "ds_read_b128 a[228:231], v22 offset:55296\n"
        "v_mfma_f32_32x32x16_bf16 v[64:79], v[136:139], a[184:187], v[64:79]\n"
        "ds_read_b128 a[232:235], v23 offset:24576\n"
        "ds_read_b128 a[236:239], v23 offset:55296\n"
        "buffer_load_dwordx4 v10, s[44:47], s65 offen offset:0 sc1 lds\n"
        "v_exp_f32 v80, v80\n"
        "v_exp_f32 v81, v81\n"
        "v_mfma_f32_32x32x16_bf16 v[64:79], v[140:143], a[188:191], v[64:79]\n"
        "buffer_load_dwordx4 v11, s[44:47], s65 offen offset:1024 sc1 lds\n"
        "buffer_load_dwordx4 v10, s[44:47], s65 offen offset:2048 sc1 lds\n"
        "buffer_load_dwordx4 v11, s[44:47], s65 offen offset:3072 sc1 lds\n"
        "s_add_u32 s65, s65, s66\n"
        "v_exp_f32 v82, v82\n"
        "v_exp_f32 v83, v83\n"
        "v_cvt_pk_bf16_f32 v104, v80, v81\n"
        "v_exp_f32 v84, v84\n"
        "v_exp_f32 v85, v85\n"
        "v_cvt_pk_bf16_f32 v105, v82, v83\n"
        "v_exp_f32 v86, v86\n"
        "v_exp_f32 v87, v87\n"
        "v_cvt_pk_bf16_f32 v106, v84, v85\n"
        "v_exp_f32 v88, v88\n"
        "v_exp_f32 v89, v89\n"
        "v_cvt_pk_bf16_f32 v107, v86, v87\n"
        "v_exp_f32 v90, v90\n"
        "v_exp_f32 v91, v91\n"
        "v_cvt_pk_bf16_f32 v108, v88, v89\n"
        "v_exp_f32 v92, v92\n"
        "v_exp_f32 v93, v93\n"
        "v_cvt_pk_bf16_f32 v109, v90, v91\n"
        "v_exp_f32 v94, v94\n"
        "v_exp_f32 v95, v95\n"
        "v_cvt_pk_bf16_f32 v110, v92, v93\n"
        "v_exp_f32 v64, v64\n"
        "v_exp_f32 v65, v65\n"
        "v_cvt_pk_bf16_f32 v111, v94, v95\n"
        "s_waitcnt lgkmcnt(0)\n"
        "v_mfma_f32_32x32x16_bf16 v[80:95], v[128:131], a[192:195], 0\n"
        "s_waitcnt vmcnt(4)\n"
        "ds_read_b128 v[112:115], v12 offset:8192\n"
        "v_exp_f32 v66, v66\n"
        "v_exp_f32 v67, v67\n"
        "v_cvt_pk_bf16_f32 v96, v64, v65\n"
        "v_mfma_f32_32x32x16_bf16 v[80:95], v[132:135], a[196:199], v[80:95]\n"
        "s_min_u32 s77, s58, 249\n"
        "ds_read_b128 v[116:119], v13 offset:8192\n"
        "v_exp_f32 v68, v68\n"
        "v_exp_f32 v69, v69\n"
        "v_cvt_pk_bf16_f32 v97, v66, v67\n"
        "v_mfma_f32_32x32x16_bf16 v[80:95], v[136:139], a[200:203], v[80:95]\n"
        "s_lshl_b32 s77, s77, 3\n"
        "ds_read_b128 v[120:123], v14 offset:8192\n"
        "v_exp_f32 v70, v70\n"
        "v_exp_f32 v71, v71\n"
        "v_cvt_pk_bf16_f32 v98, v68, v69\n"
        "v_mfma_f32_32x32x16_bf16 v[80:95], v[140:143], a[204:207], v[80:95]\n"
        "s_add_u32 s77, s77, 1330944\n"
        "ds_read_b128 v[124:127], v15 offset:8192\n"
        "v_exp_f32 v72, v72\n"
        "v_exp_f32 v73, v73\n"
        "v_cvt_pk_bf16_f32 v99, v70, v71\n"
        "v_mfma_f32_32x32x16_bf16 a[0:15], v[104:107], v[160:163], 0\n"
        "ds_read_b64_tr_b16 v[144:145], v16 offset:8192\n"
        "v_exp_f32 v74, v74\n"
        "v_exp_f32 v75, v75\n"
        "v_cvt_pk_bf16_f32 v100, v72, v73\n"
        "v_mfma_f32_32x32x16_bf16 a[16:31], v[104:107], v[164:167], 0\n"
        "ds_read_b64_tr_b16 v[146:147], v17 offset:8192\n"
        "v_exp_f32 v76, v76\n"
        "v_exp_f32 v77, v77\n"
        "v_cvt_pk_bf16_f32 v101, v74, v75\n"
        "v_mfma_f32_32x32x16_bf16 a[0:15], v[108:111], v[168:171], a[0:15]\n"
        "ds_read_b64_tr_b16 v[148:149], v18 offset:8192\n"
        "v_exp_f32 v78, v78\n"
        "v_exp_f32 v79, v79\n"
        "v_cvt_pk_bf16_f32 v102, v76, v77\n"
        "v_mfma_f32_32x32x16_bf16 a[16:31], v[108:111], v[172:175], a[16:31]\n"
        "ds_read_b64_tr_b16 v[150:151], v19 offset:8192\n"
        "v_exp_f32 v80, v80\n"
        "v_exp_f32 v81, v81\n"
        "v_cvt_pk_bf16_f32 v103, v78, v79\n"
        "v_mfma_f32_32x32x16_bf16 v[64:79], v[128:131], a[208:211], 0\n"
        "ds_read_b64_tr_b16 v[152:153], v16 offset:10240\n"
        "v_exp_f32 v82, v82\n"
        "v_exp_f32 v83, v83\n"
        "v_cvt_pk_bf16_f32 v104, v80, v81\n"
        "v_mfma_f32_32x32x16_bf16 v[64:79], v[132:135], a[212:215], v[64:79]\n"
        "ds_read_b64_tr_b16 v[154:155], v17 offset:10240\n"
        "v_exp_f32 v84, v84\n"
        "v_exp_f32 v85, v85\n"
        "v_cvt_pk_bf16_f32 v105, v82, v83\n"
        "v_mfma_f32_32x32x16_bf16 v[64:79], v[136:139], a[216:219], v[64:79]\n"
        "ds_read_b64_tr_b16 v[156:157], v18 offset:10240\n"
        "v_exp_f32 v86, v86\n"
        "v_exp_f32 v87, v87\n"
        "v_cvt_pk_bf16_f32 v106, v84, v85\n"
        "v_mfma_f32_32x32x16_bf16 v[64:79], v[140:143], a[220:223], v[64:79]\n"
        "s_add_u32 m0, s62, 0\n"
        "ds_read_b64_tr_b16 v[158:159], v19 offset:10240\n"
        "v_exp_f32 v88, v88\n"
        "v_exp_f32 v89, v89\n"
        "v_cvt_pk_bf16_f32 v107, v86, v87\n"
        "v_mfma_f32_32x32x16_bf16 a[32:47], v[96:99], v[160:163], 0\n"
        "buffer_load_dwordx4 v10, s[44:47], s65 offen offset:0 sc1 lds\n"
        "v_exp_f32 v90, v90\n"
        "v_exp_f32 v91, v91\n"
        "v_cvt_pk_bf16_f32 v108, v88, v89\n"
        "v_mfma_f32_32x32x16_bf16 a[48:63], v[96:99], v[164:167], 0\n"
        "buffer_load_dwordx4 v11, s[44:47], s65 offen offset:1024 sc1 lds\n"
        "v_exp_f32 v92, v92\n"
        "v_exp_f32 v93, v93\n"
        "v_cvt_pk_bf16_f32 v109, v90, v91\n"
        "v_mfma_f32_32x32x16_bf16 a[32:47], v[100:103], v[168:171], a[32:47]\n"
        "s_barrier\n"
        "buffer_load_dwordx4 v10, s[44:47], s65 offen offset:2048 sc1 lds\n"
        "v_exp_f32 v94, v94\n"
        "v_exp_f32 v95, v95\n"
        "v_cvt_pk_bf16_f32 v110, v92, v93\n"
        "v_mfma_f32_32x32x16_bf16 a[48:63], v[100:103], v[172:175], a[48:63]\n"
        "buffer_load_dwordx4 v11, s[44:47], s65 offen offset:3072 sc1 lds\n"
        "v_exp_f32 v64, v64\n"
        "v_exp_f32 v65, v65\n"
        "v_cvt_pk_bf16_f32 v111, v94, v95\n"
        "v_mfma_f32_32x32x16_bf16 v[80:95], v[128:131], a[224:227], 0\n"
        "s_add_u32 s65, s65, s66\n"
        "ds_read_b128 v[176:179], v24 offset:0\n"
        "v_exp_f32 v66, v66\n"
        "v_exp_f32 v67, v67\n"
        "v_cvt_pk_bf16_f32 v96, v64, v65\n"
        "v_mfma_f32_32x32x16_bf16 v[80:95], v[132:135], a[228:231], v[80:95]\n"
        "s_add_u32 m0, s62, 4096\n"
        "ds_read_b128 v[180:183], v24 offset:4096\n"
        "v_exp_f32 v68, v68\n"
        "v_exp_f32 v69, v69\n"
        "v_cvt_pk_bf16_f32 v97, v66, v67\n"
        "v_mfma_f32_32x32x16_bf16 v[80:95], v[136:139], a[232:235], v[80:95]\n"
        "buffer_load_dwordx4 v10, s[44:47], s65 offen offset:0 sc1 lds\n"
        "v_exp_f32 v70, v70\n"
        "v_exp_f32 v71, v71\n"
        "v_cvt_pk_bf16_f32 v98, v68, v69\n"
        "v_mfma_f32_32x32x16_bf16 v[80:95], v[140:143], a[236:239], v[80:95]\n"
        "buffer_load_dwordx4 v11, s[44:47], s65 offen offset:1024 sc1 lds\n"
        "v_exp_f32 v72, v72\n"
        "v_exp_f32 v73, v73\n"
        "v_cvt_pk_bf16_f32 v99, v70, v71\n"
        "v_mfma_f32_32x32x16_bf16 a[64:79], v[104:107], v[160:163], 0\n"
        "buffer_load_dwordx4 v10, s[44:47], s65 offen offset:2048 sc1 lds\n"
        "v_exp_f32 v74, v74\n"
        "v_exp_f32 v75, v75\n"
        "v_cvt_pk_bf16_f32 v100, v72, v73\n"
        "v_mfma_f32_32x32x16_bf16 a[80:95], v[104:107], v[164:167], 0\n"
        "buffer_load_dwordx4 v11, s[44:47], s65 offen offset:3072 sc1 lds\n"
        "v_exp_f32 v76, v76\n"
        "v_exp_f32 v77, v77\n"
        "v_cvt_pk_bf16_f32 v101, v74, v75\n"
        "v_mfma_f32_32x32x16_bf16 a[64:79], v[108:111], v[168:171], a[64:79]\n"
        "s_add_u32 s65, s65, s66\n"
        "s_waitcnt lgkmcnt(2)\n"
        "ds_read_b128 v[184:187], v24 offset:8192\n"
        "v_exp_f32 v78, v78\n"
        "v_exp_f32 v79, v79\n"
        "v_cvt_pk_bf16_f32 v102, v76, v77\n"
        "v_mfma_f32_32x32x16_bf16 a[80:95], v[108:111], v[172:175], a[80:95]\n"
        "s_add_u32 m0, s62, 8192\n"
        "v_exp_f32 v80, v80\n"
        "v_exp_f32 v81, v81\n"
        "v_cvt_pk_bf16_f32 v103, v78, v79\n"
        "v_lshl_add_u32 v232, v8, 11, s77\n"
        "v_mfma_f32_32x32x16_bf16 v[64:79], v[112:115], a[160:163], 0\n"
        "buffer_load_dwordx4 v10, s[44:47], s65 offen offset:0 sc1 lds\n"
        "v_exp_f32 v82, v82\n"
        "v_exp_f32 v83, v83\n"
        "v_cvt_pk_bf16_f32 v104, v80, v81\n"
        "v_mfma_f32_32x32x16_bf16 v[64:79], v[116:119], a[164:167], v[64:79]\n"
        "buffer_load_dwordx4 v11, s[44:47], s65 offen offset:1024 sc1 lds\n"
        "v_exp_f32 v84, v84\n"
        "v_exp_f32 v85, v85\n"
        "v_cvt_pk_bf16_f32 v105, v82, v83\n"
        "v_mfma_f32_32x32x16_bf16 v[64:79], v[120:123], a[168:171], v[64:79]\n"
        "buffer_load_dwordx4 v10, s[44:47], s65 offen offset:2048 sc1 lds\n"
        "v_exp_f32 v86, v86\n"
        "v_exp_f32 v87, v87\n"
        "v_cvt_pk_bf16_f32 v106, v84, v85\n"
        "v_mfma_f32_32x32x16_bf16 v[64:79], v[124:127], a[172:175], v[64:79]\n"
        "buffer_load_dwordx4 v11, s[44:47], s65 offen offset:3072 sc1 lds\n"
        "v_exp_f32 v88, v88\n"
        "v_exp_f32 v89, v89\n"
        "v_cvt_pk_bf16_f32 v107, v86, v87\n"
        "v_mfma_f32_32x32x16_bf16 a[96:111], v[96:99], v[160:163], 0\n"
        "v_exp_f32 v90, v90\n"
        "v_exp_f32 v91, v91\n"
        "v_cvt_pk_bf16_f32 v108, v88, v89\n"
        "v_mov_b32 v233, 1312256\n"
        "v_mfma_f32_32x32x16_bf16 a[112:127], v[96:99], v[164:167], 0\n"
        "s_add_u32 s65, s65, s66\n"
        "v_exp_f32 v92, v92\n"
        "v_exp_f32 v93, v93\n"
        "v_cvt_pk_bf16_f32 v109, v90, v91\n"
        "v_cmp_gt_u32 vcc, 250, v8\n"
        "v_mfma_f32_32x32x16_bf16 a[96:111], v[100:103], v[168:171], a[96:111]\n"
        "s_waitcnt lgkmcnt(2)\n"
        "buffer_store_dwordx4 v[176:179], v25, s[36:39], 0 offen sc1\n"
        "v_exp_f32 v94, v94\n"
        "v_exp_f32 v95, v95\n"
        "v_cvt_pk_bf16_f32 v110, v92, v93\n"
        "v_mfma_f32_32x32x16_bf16 a[112:127], v[100:103], v[172:175], a[112:127]\n"
        "v_exp_f32 v64, v64\n"
        "v_exp_f32 v65, v65\n"
        "v_cvt_pk_bf16_f32 v111, v94, v95\n"
        "v_mul_u32_u24 v234, 10368, v8\n"
        "v_mfma_f32_32x32x16_bf16 v[80:95], v[112:115], a[176:179], 0\n"
        "v_exp_f32 v66, v66\n"
        "v_exp_f32 v67, v67\n"
        "v_cvt_pk_bf16_f32 v96, v64, v65\n"
        "v_cndmask_b32 v32, v233, v232, vcc\n"
        "v_mfma_f32_32x32x16_bf16 v[80:95], v[116:119], a[180:183], v[80:95]\n"
        "s_or_b32 s77, s69, 1842944\n"
        "s_waitcnt lgkmcnt(1)\n"
        "buffer_store_dwordx4 v[180:183], v25, s[36:39], s66 offen sc1\n"
        "v_exp_f32 v68, v68\n"
        "v_exp_f32 v69, v69\n"
        "v_cvt_pk_bf16_f32 v97, v66, v67\n"
        "v_mfma_f32_32x32x16_bf16 v[80:95], v[120:123], a[184:187], v[80:95]\n"
        "v_exp_f32 v70, v70\n"
        "v_exp_f32 v71, v71\n"
        "v_cvt_pk_bf16_f32 v98, v68, v69\n"
        "v_add_u32 v234, s77, v234\n"
        "v_mfma_f32_32x32x16_bf16 v[80:95], v[124:127], a[188:191], v[80:95]\n"
        "v_exp_f32 v72, v72\n"
        "v_exp_f32 v73, v73\n"
        "v_cvt_pk_bf16_f32 v99, v70, v71\n"
        "v_cndmask_b32 v34, 0, 12, vcc\n"
        "v_mfma_f32_32x32x16_bf16 a[128:143], v[104:107], v[160:163], 0\n"
        "s_waitcnt lgkmcnt(0)\n"
        "buffer_store_dwordx4 v[184:187], v26, s[36:39], s67 offen sc1\n"
        "v_exp_f32 v74, v74\n"
        "v_exp_f32 v75, v75\n"
        "v_cvt_pk_bf16_f32 v100, v72, v73\n"
        "v_mfma_f32_32x32x16_bf16 a[144:159], v[104:107], v[164:167], 0\n"
        "v_exp_f32 v76, v76\n"
        "v_exp_f32 v77, v77\n"
        "v_cvt_pk_bf16_f32 v101, v74, v75\n"
        "v_cndmask_b32 v33, v235, v234, vcc\n"
        "v_mfma_f32_32x32x16_bf16 a[128:143], v[108:111], v[168:171], a[128:143]\n"
        "v_exp_f32 v78, v78\n"
        "v_exp_f32 v79, v79\n"
        "v_cvt_pk_bf16_f32 v102, v76, v77\n"
        "v_mov_b32 v236, -1\n"
        "v_mfma_f32_32x32x16_bf16 a[144:159], v[108:111], v[172:175], a[144:159]\n"
        "v_exp_f32 v80, v80\n"
        "v_exp_f32 v81, v81\n"
        "v_cvt_pk_bf16_f32 v103, v78, v79\n"
        "v_mov_b32 v237, -1\n"
        "v_mfma_f32_32x32x16_bf16 v[64:79], v[112:115], a[192:195], 0\n"
        "s_waitcnt vmcnt(15)\n"
        "ds_read_b128 v[128:131], v12 offset:12288\n"
        "v_exp_f32 v82, v82\n"
        "v_exp_f32 v83, v83\n"
        "v_cvt_pk_bf16_f32 v104, v80, v81\n"
        "v_mfma_f32_32x32x16_bf16 v[64:79], v[116:119], a[196:199], v[64:79]\n"
        "ds_read_b128 v[132:135], v13 offset:12288\n"
        "v_exp_f32 v84, v84\n"
        "v_exp_f32 v85, v85\n"
        "v_cvt_pk_bf16_f32 v105, v82, v83\n"
        "v_mfma_f32_32x32x16_bf16 v[64:79], v[120:123], a[200:203], v[64:79]\n"
        "ds_read_b128 v[136:139], v14 offset:12288\n"
        "v_exp_f32 v86, v86\n"
        "v_exp_f32 v87, v87\n"
        "v_cvt_pk_bf16_f32 v106, v84, v85\n"
        "v_mfma_f32_32x32x16_bf16 v[64:79], v[124:127], a[204:207], v[64:79]\n"
        "ds_read_b128 v[140:143], v15 offset:12288\n"
        "v_exp_f32 v88, v88\n"
        "v_exp_f32 v89, v89\n"
        "v_cvt_pk_bf16_f32 v107, v86, v87\n"
        "v_mfma_f32_32x32x16_bf16 a[0:15], v[96:99], v[144:147], a[0:15]\n"
        "ds_read_b64_tr_b16 v[160:161], v16 offset:12288\n"
        "v_exp_f32 v90, v90\n"
        "v_exp_f32 v91, v91\n"
        "v_cvt_pk_bf16_f32 v108, v88, v89\n"
        "v_mfma_f32_32x32x16_bf16 a[16:31], v[96:99], v[148:151], a[16:31]\n"
        "ds_read_b64_tr_b16 v[162:163], v17 offset:12288\n"
        "v_exp_f32 v92, v92\n"
        "v_exp_f32 v93, v93\n"
        "v_cvt_pk_bf16_f32 v109, v90, v91\n"
        "v_mfma_f32_32x32x16_bf16 a[0:15], v[100:103], v[152:155], a[0:15]\n"
        "ds_read_b64_tr_b16 v[164:165], v18 offset:12288\n"
        "v_exp_f32 v94, v94\n"
        "v_exp_f32 v95, v95\n"
        "v_cvt_pk_bf16_f32 v110, v92, v93\n"
        "v_mfma_f32_32x32x16_bf16 a[16:31], v[100:103], v[156:159], a[16:31]\n"
        "ds_read_b64_tr_b16 v[166:167], v19 offset:12288\n"
        "v_exp_f32 v64, v64\n"
        "v_exp_f32 v65, v65\n"
        "v_cvt_pk_bf16_f32 v111, v94, v95\n"
        "v_mfma_f32_32x32x16_bf16 v[80:95], v[112:115], a[208:211], 0\n"
        "ds_read_b64_tr_b16 v[168:169], v16 offset:14336\n"
        "v_exp_f32 v66, v66\n"
        "v_exp_f32 v67, v67\n"
        "v_cvt_pk_bf16_f32 v96, v64, v65\n"
        "v_mfma_f32_32x32x16_bf16 v[80:95], v[116:119], a[212:215], v[80:95]\n"
        "ds_read_b64_tr_b16 v[170:171], v17 offset:14336\n"
        "v_exp_f32 v68, v68\n"
        "v_exp_f32 v69, v69\n"
        "v_cvt_pk_bf16_f32 v97, v66, v67\n"
        "v_mfma_f32_32x32x16_bf16 v[80:95], v[120:123], a[216:219], v[80:95]\n"
        "ds_read_b64_tr_b16 v[172:173], v18 offset:14336\n"
        "v_exp_f32 v70, v70\n"
        "v_exp_f32 v71, v71\n"
        "v_cvt_pk_bf16_f32 v98, v68, v69\n"
        "v_mfma_f32_32x32x16_bf16 v[80:95], v[124:127], a[220:223], v[80:95]\n"
        "ds_read_b64_tr_b16 v[174:175], v19 offset:14336\n"
        "v_exp_f32 v72, v72\n"
        "v_exp_f32 v73, v73\n"
        "v_cvt_pk_bf16_f32 v99, v70, v71\n"
        "v_mfma_f32_32x32x16_bf16 a[32:47], v[104:107], v[144:147], a[32:47]\n"
        "v_exp_f32 v74, v74\n"
        "v_exp_f32 v75, v75\n"
        "v_cvt_pk_bf16_f32 v100, v72, v73\n"
        "v_mov_b32 v238, -1\n"
        "v_mfma_f32_32x32x16_bf16 a[48:63], v[104:107], v[148:151], a[48:63]\n"
        "v_exp_f32 v76, v76\n"
        "v_exp_f32 v77, v77\n"
        "v_cvt_pk_bf16_f32 v101, v74, v75\n"
        "v_mov_b32 v239, -1\n"
        "v_mfma_f32_32x32x16_bf16 a[32:47], v[108:111], v[152:155], a[32:47]\n"
        "s_lshl_b32 s75, s61, 2\n"
        "v_exp_f32 v78, v78\n"
        "v_exp_f32 v79, v79\n"
        "v_cvt_pk_bf16_f32 v102, v76, v77\n"
        "v_mfma_f32_32x32x16_bf16 a[48:63], v[108:111], v[156:159], a[48:63]\n"
        "s_mul_i32 s77, s59, 2720\n"
        "v_exp_f32 v80, v80\n"
        "v_exp_f32 v81, v81\n"
        "v_cvt_pk_bf16_f32 v103, v78, v79\n"
        "v_mfma_f32_32x32x16_bf16 v[64:79], v[112:115], a[224:227], 0\n"
        "s_add_u32 s77, s77, 122880\n"
        "v_exp_f32 v82, v82\n"
        "v_exp_f32 v83, v83\n"
        "v_cvt_pk_bf16_f32 v104, v80, v81\n"
        "v_and_b32 v232, 1, v246\n"
        "v_mfma_f32_32x32x16_bf16 v[64:79], v[116:119], a[228:231], v[64:79]\n"
        "v_exp_f32 v84, v84\n"
        "v_exp_f32 v85, v85\n"
        "v_cvt_pk_bf16_f32 v105, v82, v83\n"
        "v_mul_u32_u24 v232, 272, v232\n"
        "v_mfma_f32_32x32x16_bf16 v[64:79], v[120:123], a[232:235], v[64:79]\n"
        "v_exp_f32 v86, v86\n"
        "v_exp_f32 v87, v87\n"
        "v_cvt_pk_bf16_f32 v106, v84, v85\n"
        "v_add_u32 v232, s77, v232\n"
        "v_mfma_f32_32x32x16_bf16 v[64:79], v[124:127], a[236:239], v[64:79]\n"
        "v_exp_f32 v88, v88\n"
        "v_exp_f32 v89, v89\n"
        "v_cvt_pk_bf16_f32 v107, v86, v87\n"
        "v_lshl_add_u32 v35, v60, 5, v232\n"
        "v_mfma_f32_32x32x16_bf16 a[64:79], v[96:99], v[144:147], a[64:79]\n"
        "v_exp_f32 v90, v90\n"
        "v_exp_f32 v91, v91\n"
        "v_cvt_pk_bf16_f32 v108, v88, v89\n"
        "v_lshl_add_u32 v36, v60, 3, v232\n"
        "v_mfma_f32_32x32x16_bf16 a[80:95], v[96:99], v[148:151], a[80:95]\n"
        "v_exp_f32 v92, v92\n"
        "v_exp_f32 v93, v93\n"
        "v_cvt_pk_bf16_f32 v109, v90, v91\n"
        "v_lshl_or_b32 v37, v247, 4, s69\n"
        "v_mfma_f32_32x32x16_bf16 a[64:79], v[100:103], v[152:155], a[64:79]\n"
        "s_waitcnt lgkmcnt(0)\n"
        "v_exp_f32 v94, v94\n"
        "v_exp_f32 v95, v95\n"
        "v_cvt_pk_bf16_f32 v110, v92, v93\n"
        "v_and_b32 v39, 48, v248\n"
        "v_mfma_f32_32x32x16_bf16 a[80:95], v[100:103], v[156:159], a[80:95]\n"
        "s_add_u32 m0, s62, 12288\n"
        "v_exp_f32 v64, v64\n"
        "v_exp_f32 v65, v65\n"
        "v_cvt_pk_bf16_f32 v111, v94, v95\n"
        "v_mfma_f32_32x32x16_bf16 v[80:95], v[128:131], a[160:163], 0\n"
        "s_xor_b32 s77, s59, 1\n"
        "buffer_load_dwordx4 v10, s[44:47], s65 offen offset:0 sc1 lds\n"
        "v_exp_f32 v66, v66\n"
        "v_exp_f32 v67, v67\n"
        "v_cvt_pk_bf16_f32 v96, v64, v65\n"
        "v_mfma_f32_32x32x16_bf16 v[80:95], v[132:135], a[164:167], v[80:95]\n"
        "s_mul_i32 s77, s77, 30720\n"
        "buffer_load_dwordx4 v11, s[44:47], s65 offen offset:1024 sc1 lds\n"
        "v_exp_f32 v68, v68\n"
        "v_exp_f32 v69, v69\n"
        "v_cvt_pk_bf16_f32 v97, v66, v67\n"
        "v_mfma_f32_32x32x16_bf16 v[80:95], v[136:139], a[168:171], v[80:95]\n"
        "buffer_load_dwordx4 v10, s[44:47], s65 offen offset:2048 sc1 lds\n"
        "v_exp_f32 v70, v70\n"
        "v_exp_f32 v71, v71\n"
        "v_cvt_pk_bf16_f32 v98, v68, v69\n"
        "v_mfma_f32_32x32x16_bf16 v[80:95], v[140:143], a[172:175], v[80:95]\n"
        "buffer_load_dwordx4 v11, s[44:47], s65 offen offset:3072 sc1 lds\n"
        "v_exp_f32 v72, v72\n"
        "v_exp_f32 v73, v73\n"
        "v_cvt_pk_bf16_f32 v99, v70, v71\n"
        "v_mfma_f32_32x32x16_bf16 a[96:111], v[104:107], v[144:147], a[96:111]\n"
        "v_exp_f32 v74, v74\n"
        "v_exp_f32 v75, v75\n"
        "v_cvt_pk_bf16_f32 v100, v72, v73\n"
        "v_add_u32 v40, s77, v248\n"
        "v_mfma_f32_32x32x16_bf16 a[112:127], v[104:107], v[148:151], a[112:127]\n"
        "s_add_u32 s65, s65, s66\n"
        "v_exp_f32 v76, v76\n"
        "v_exp_f32 v77, v77\n"
        "v_cvt_pk_bf16_f32 v101, v74, v75\n"
        "v_mfma_f32_32x32x16_bf16 a[96:111], v[108:111], v[152:155], a[96:111]\n"
        "s_xor_b32 s77, s59, 2\n"
        "v_exp_f32 v78, v78\n"
        "v_exp_f32 v79, v79\n"
        "v_cvt_pk_bf16_f32 v102, v76, v77\n"
        "v_mfma_f32_32x32x16_bf16 a[112:127], v[108:111], v[156:159], a[112:127]\n"
        "s_mul_i32 s77, s77, 30720\n"
        "v_exp_f32 v80, v80\n"
        "v_exp_f32 v81, v81\n"
        "v_cvt_pk_bf16_f32 v103, v78, v79\n"
        "v_add_u32 v41, s77, v248\n"
        "v_mfma_f32_32x32x16_bf16 v[64:79], v[128:131], a[176:179], 0\n"
        "s_xor_b32 s77, s59, 3\n"
        "v_exp_f32 v82, v82\n"
        "v_exp_f32 v83, v83\n"
        "v_cvt_pk_bf16_f32 v104, v80, v81\n"
        "v_mfma_f32_32x32x16_bf16 v[64:79], v[132:135], a[180:183], v[64:79]\n"
        "s_mul_i32 s77, s77, 30720\n"
        "v_exp_f32 v84, v84\n"
        "v_exp_f32 v85, v85\n"
        "v_cvt_pk_bf16_f32 v105, v82, v83\n"
        "v_add_u32 v42, s77, v248\n"
        "v_mfma_f32_32x32x16_bf16 v[64:79], v[136:139], a[184:187], v[64:79]\n"
        "v_exp_f32 v86, v86\n"
        "v_exp_f32 v87, v87\n"
        "v_cvt_pk_bf16_f32 v106, v84, v85\n"
        "v_and_b32 v232, 31, v9\n"
        "v_mfma_f32_32x32x16_bf16 v[64:79], v[140:143], a[188:191], v[64:79]\n"
        "v_exp_f32 v88, v88\n"
        "v_exp_f32 v89, v89\n"
        "v_cvt_pk_bf16_f32 v107, v86, v87\n"
        "v_mul_u32_u24 v232, 144, v232\n"
        "v_mfma_f32_32x32x16_bf16 a[128:143], v[96:99], v[144:147], a[128:143]\n"
        "v_exp_f32 v90, v90\n"
        "v_exp_f32 v91, v91\n"
        "v_cvt_pk_bf16_f32 v108, v88, v89\n"
        "v_lshrrev_b32 v233, 5, v9\n"
        "v_mfma_f32_32x32x16_bf16 a[144:159], v[96:99], v[148:151], a[144:159]\n"
        "v_exp_f32 v92, v92\n"
        "v_exp_f32 v93, v93\n"
        "v_cvt_pk_bf16_f32 v109, v90, v91\n"
        "v_lshl_add_u32 v232, v233, 4, v232\n"
        "v_mfma_f32_32x32x16_bf16 a[128:143], v[100:103], v[152:155], a[128:143]\n"
        "s_lshl_b32 s77, s59, 5\n"
        "v_exp_f32 v94, v94\n"
        "v_exp_f32 v95, v95\n"
        "v_cvt_pk_bf16_f32 v110, v92, v93\n"
        "v_mfma_f32_32x32x16_bf16 a[144:159], v[100:103], v[156:159], a[144:159]\n"
        "s_add_u32 s77, s77, 154384\n"
        "v_exp_f32 v64, v64\n"
        "v_exp_f32 v65, v65\n"
        "v_cvt_pk_bf16_f32 v111, v94, v95\n"
        "v_add_u32 v55, s77, v232\n"
        "v_mfma_f32_32x32x16_bf16 v[80:95], v[128:131], a[192:195], 0\n"
        "s_waitcnt vmcnt(15)\n"
        "ds_read_b128 v[112:115], v12 offset:0\n"
        "v_exp_f32 v66, v66\n"
        "v_exp_f32 v67, v67\n"
        "v_cvt_pk_bf16_f32 v96, v64, v65\n"
        "v_mfma_f32_32x32x16_bf16 v[80:95], v[132:135], a[196:199], v[80:95]\n"
        "ds_read_b128 v[116:119], v13 offset:0\n"
        "v_exp_f32 v68, v68\n"
        "v_exp_f32 v69, v69\n"
        "v_cvt_pk_bf16_f32 v97, v66, v67\n"
        "v_mfma_f32_32x32x16_bf16 v[80:95], v[136:139], a[200:203], v[80:95]\n"
        "ds_read_b128 v[120:123], v14 offset:0\n"
        "v_exp_f32 v70, v70\n"
        "v_exp_f32 v71, v71\n"
        "v_cvt_pk_bf16_f32 v98, v68, v69\n"
        "v_mfma_f32_32x32x16_bf16 v[80:95], v[140:143], a[204:207], v[80:95]\n"
        "ds_read_b128 v[124:127], v15 offset:0\n"
        "v_exp_f32 v72, v72\n"
        "v_exp_f32 v73, v73\n"
        "v_cvt_pk_bf16_f32 v99, v70, v71\n"
        "v_mfma_f32_32x32x16_bf16 a[0:15], v[104:107], v[160:163], a[0:15]\n"
        "ds_read_b64_tr_b16 v[144:145], v16 offset:0\n"
        "v_exp_f32 v74, v74\n"
        "v_exp_f32 v75, v75\n"
        "v_cvt_pk_bf16_f32 v100, v72, v73\n"
        "v_mfma_f32_32x32x16_bf16 a[16:31], v[104:107], v[164:167], a[16:31]\n"
        "ds_read_b64_tr_b16 v[146:147], v17 offset:0\n"
        "v_exp_f32 v76, v76\n"
        "v_exp_f32 v77, v77\n"
        "v_cvt_pk_bf16_f32 v101, v74, v75\n"
        "v_mfma_f32_32x32x16_bf16 a[0:15], v[108:111], v[168:171], a[0:15]\n"
        "ds_read_b64_tr_b16 v[148:149], v18 offset:0\n"
        "v_exp_f32 v78, v78\n"
        "v_exp_f32 v79, v79\n"
        "v_cvt_pk_bf16_f32 v102, v76, v77\n"
        "v_mfma_f32_32x32x16_bf16 a[16:31], v[108:111], v[172:175], a[16:31]\n"
        "ds_read_b64_tr_b16 v[150:151], v19 offset:0\n"
        "v_exp_f32 v80, v80\n"
        "v_exp_f32 v81, v81\n"
        "v_cvt_pk_bf16_f32 v103, v78, v79\n"
        "v_mfma_f32_32x32x16_bf16 v[64:79], v[128:131], a[208:211], 0\n"
        "ds_read_b64_tr_b16 v[152:153], v16 offset:2048\n"
        "v_exp_f32 v82, v82\n"
        "v_exp_f32 v83, v83\n"
        "v_cvt_pk_bf16_f32 v104, v80, v81\n"
        "v_mfma_f32_32x32x16_bf16 v[64:79], v[132:135], a[212:215], v[64:79]\n"
        "ds_read_b64_tr_b16 v[154:155], v17 offset:2048\n"
        "v_exp_f32 v84, v84\n"
        "v_exp_f32 v85, v85\n"
        "v_cvt_pk_bf16_f32 v105, v82, v83\n"
        "v_mfma_f32_32x32x16_bf16 v[64:79], v[136:139], a[216:219], v[64:79]\n"
        "ds_read_b64_tr_b16 v[156:157], v18 offset:2048\n"
        "v_exp_f32 v86, v86\n"
        "v_exp_f32 v87, v87\n"
        "v_cvt_pk_bf16_f32 v106, v84, v85\n"
        "v_mfma_f32_32x32x16_bf16 v[64:79], v[140:143], a[220:223], v[64:79]\n"
        "ds_read_b64_tr_b16 v[158:159], v19 offset:2048\n"
        "v_exp_f32 v88, v88\n"
        "v_exp_f32 v89, v89\n"
        "v_cvt_pk_bf16_f32 v107, v86, v87\n"
        "v_mfma_f32_32x32x16_bf16 a[32:47], v[96:99], v[160:163], a[32:47]\n"
        "s_waitcnt vmcnt(4)\n"
        "v_exp_f32 v90, v90\n"
        "v_exp_f32 v91, v91\n"
        "v_cvt_pk_bf16_f32 v108, v88, v89\n"
        "v_and_b32 v232, 3, v9\n"
        "v_mfma_f32_32x32x16_bf16 a[48:63], v[96:99], v[164:167], a[48:63]\n"
        "s_barrier\n"
        "buffer_store_dwordx2 v[30:31], v29, s[36:39], 0 offen sc1\n"
        "v_exp_f32 v92, v92\n"
        "v_exp_f32 v93, v93\n"
        "v_cvt_pk_bf16_f32 v109, v90, v91\n"
        "v_mfma_f32_32x32x16_bf16 a[32:47], v[100:103], v[168:171], a[32:47]\n"
        "v_exp_f32 v94, v94\n"
        "v_exp_f32 v95, v95\n"
        "v_cvt_pk_bf16_f32 v110, v92, v93\n"
        "v_min_u32 v54, 2, v232\n"
        "v_mfma_f32_32x32x16_bf16 a[48:63], v[100:103], v[172:175], a[48:63]\n"
        "v_exp_f32 v64, v64\n"
        "v_exp_f32 v65, v65\n"
        "v_cvt_pk_bf16_f32 v111, v94, v95\n"
        "v_sub_u32 v232, v232, v54\n"
        "v_mfma_f32_32x32x16_bf16 v[80:95], v[128:131], a[224:227], 0\n"
        "buffer_store_dwordx4 v[236:239], v27, s[36:39], 0 offen sc1\n"
        "v_exp_f32 v66, v66\n"
        "v_exp_f32 v67, v67\n"
        "v_cvt_pk_bf16_f32 v96, v64, v65\n"
        "v_mfma_f32_32x32x16_bf16 v[80:95], v[132:135], a[228:231], v[80:95]\n"
        "v_exp_f32 v68, v68\n"
        "v_exp_f32 v69, v69\n"
        "v_cvt_pk_bf16_f32 v97, v66, v67\n"
        "v_lshlrev_b32 v232, 4, v232\n"
        "v_mfma_f32_32x32x16_bf16 v[80:95], v[136:139], a[232:235], v[80:95]\n"
        "v_exp_f32 v70, v70\n"
        "v_exp_f32 v71, v71\n"
        "v_cvt_pk_bf16_f32 v98, v68, v69\n"
        "v_mov_b32 v234, 0x3f80\n"
        "v_mfma_f32_32x32x16_bf16 v[80:95], v[140:143], a[236:239], v[80:95]\n"
        "v_exp_f32 v72, v72\n"
        "v_exp_f32 v73, v73\n"
        "v_cvt_pk_bf16_f32 v99, v70, v71\n"
        "v_lshlrev_b32 v232, v232, v234\n"
        "v_mfma_f32_32x32x16_bf16 a[64:79], v[104:107], v[160:163], a[64:79]\n"
        "buffer_store_dwordx4 v[236:239], v27, s[36:39], s66 offen sc1\n"
        "v_exp_f32 v74, v74\n"
        "v_exp_f32 v75, v75\n"
        "v_cvt_pk_bf16_f32 v100, v72, v73\n"
        "v_mfma_f32_32x32x16_bf16 a[80:95], v[104:107], v[164:167], a[80:95]\n"
        "v_exp_f32 v76, v76\n"
        "v_exp_f32 v77, v77\n"
        "v_cvt_pk_bf16_f32 v101, v74, v75\n"
        "v_or_b32 v54, 12, v54\n"
        "v_mfma_f32_32x32x16_bf16 a[64:79], v[108:111], v[168:171], a[64:79]\n"
        "s_waitcnt lgkmcnt(0)\n"
        "v_exp_f32 v78, v78\n"
        "v_exp_f32 v79, v79\n"
        "v_cvt_pk_bf16_f32 v102, v76, v77\n"
        "v_mul_u32_u24 v54, 0x1111, v54\n"
        "v_mfma_f32_32x32x16_bf16 a[80:95], v[108:111], v[172:175], a[80:95]\n"
        "s_add_u32 m0, s62, 0\n"
        "v_exp_f32 v80, v80\n"
        "v_exp_f32 v81, v81\n"
        "v_cvt_pk_bf16_f32 v103, v78, v79\n"
        "v_lshrrev_b32 v233, 2, v59\n"
        "v_mfma_f32_32x32x16_bf16 v[64:79], v[112:115], a[160:163], 0\n"
        "buffer_load_dwordx4 v10, s[44:47], s65 offen offset:0 sc1 lds\n"
        "v_exp_f32 v82, v82\n"
        "v_exp_f32 v83, v83\n"
        "v_cvt_pk_bf16_f32 v104, v80, v81\n"
        "v_mfma_f32_32x32x16_bf16 v[64:79], v[116:119], a[164:167], v[64:79]\n"
        "buffer_load_dwordx4 v11, s[44:47], s65 offen offset:1024 sc1 lds\n"
        "v_exp_f32 v84, v84\n"
        "v_exp_f32 v85, v85\n"
        "v_cvt_pk_bf16_f32 v105, v82, v83\n"
        "v_mfma_f32_32x32x16_bf16 v[64:79], v[120:123], a[168:171], v[64:79]\n"
        "buffer_load_dwordx4 v10, s[44:47], s65 offen offset:2048 sc1 lds\n"
        "v_exp_f32 v86, v86\n"
        "v_exp_f32 v87, v87\n"
        "v_cvt_pk_bf16_f32 v106, v84, v85\n"
        "v_mfma_f32_32x32x16_bf16 v[64:79], v[124:127], a[172:175], v[64:79]\n"
        "buffer_load_dwordx4 v11, s[44:47], s65 offen offset:3072 sc1 lds\n"
        "v_exp_f32 v88, v88\n"
        "v_exp_f32 v89, v89\n"
        "v_cvt_pk_bf16_f32 v107, v86, v87\n"
        "v_mfma_f32_32x32x16_bf16 a[96:111], v[96:99], v[160:163], a[96:111]\n"
        "buffer_store_dwordx4 v[236:239], v27, s[36:39], s67 offen sc1\n"
        "v_exp_f32 v90, v90\n"
        "v_exp_f32 v91, v91\n"
        "v_cvt_pk_bf16_f32 v108, v88, v89\n"
        "v_mfma_f32_32x32x16_bf16 a[112:127], v[96:99], v[164:167], a[112:127]\n"
        "s_add_u32 s65, s65, s66\n"
        "v_exp_f32 v92, v92\n"
        "v_exp_f32 v93, v93\n"
        "v_cvt_pk_bf16_f32 v109, v90, v91\n"
        "v_cmp_eq_u32 vcc, 0, v233\n"
        "v_mfma_f32_32x32x16_bf16 a[96:111], v[100:103], v[168:171], a[96:111]\n"
        "v_exp_f32 v94, v94\n"
        "v_exp_f32 v95, v95\n"
        "v_cvt_pk_bf16_f32 v110, v92, v93\n"
        "v_cndmask_b32 v250, 0, v232, vcc\n"
        "v_mfma_f32_32x32x16_bf16 a[112:127], v[100:103], v[172:175], a[112:127]\n"
        "v_exp_f32 v64, v64\n"
        "v_exp_f32 v65, v65\n"
        "v_cvt_pk_bf16_f32 v111, v94, v95\n"
        "v_accvgpr_write_b32 a240, v250\n"
        "v_mfma_f32_32x32x16_bf16 v[80:95], v[112:115], a[176:179], 0\n"
        "buffer_store_dwordx4 v[236:239], v28, s[36:39], s68 offen sc1\n"
        "v_exp_f32 v66, v66\n"
        "v_exp_f32 v67, v67\n"
        "v_cvt_pk_bf16_f32 v96, v64, v65\n"
        "v_mfma_f32_32x32x16_bf16 v[80:95], v[116:119], a[180:183], v[80:95]\n"
        "v_exp_f32 v68, v68\n"
        "v_exp_f32 v69, v69\n"
        "v_cvt_pk_bf16_f32 v97, v66, v67\n"
        "v_accvgpr_write_b32 a242, v250\n"
        "v_mfma_f32_32x32x16_bf16 v[80:95], v[120:123], a[184:187], v[80:95]\n"
        "v_exp_f32 v70, v70\n"
        "v_exp_f32 v71, v71\n"
        "v_cvt_pk_bf16_f32 v98, v68, v69\n"
        "v_cmp_eq_u32 vcc, 1, v233\n"
        "v_mfma_f32_32x32x16_bf16 v[80:95], v[124:127], a[188:191], v[80:95]\n"
        "v_exp_f32 v72, v72\n"
        "v_exp_f32 v73, v73\n"
        "v_cvt_pk_bf16_f32 v99, v70, v71\n"
        "v_cndmask_b32 v250, 0, v232, vcc\n"
        "v_mfma_f32_32x32x16_bf16 a[128:143], v[104:107], v[160:163], a[128:143]\n"
        "v_exp_f32 v74, v74\n"
        "v_exp_f32 v75, v75\n"
        "v_cvt_pk_bf16_f32 v100, v72, v73\n"
        "v_accvgpr_write_b32 a241, v250\n"
        "v_mfma_f32_32x32x16_bf16 a[144:159], v[104:107], v[164:167], a[144:159]\n"
        "v_exp_f32 v76, v76\n"
        "v_exp_f32 v77, v77\n"
        "v_cvt_pk_bf16_f32 v101, v74, v75\n"
        "v_accvgpr_write_b32 a243, v250\n"
        "v_mfma_f32_32x32x16_bf16 a[128:143], v[108:111], v[168:171], a[128:143]\n"
        "v_exp_f32 v78, v78\n"
        "v_exp_f32 v79, v79\n"
        "v_cvt_pk_bf16_f32 v102, v76, v77\n"
        "v_cmp_eq_u32 vcc, 2, v233\n"
        "v_mfma_f32_32x32x16_bf16 a[144:159], v[108:111], v[172:175], a[144:159]\n"
        "v_exp_f32 v80, v80\n"
        "v_exp_f32 v81, v81\n"
        "v_cvt_pk_bf16_f32 v103, v78, v79\n"
        "v_cndmask_b32 v250, 0, v232, vcc\n"
        "v_mfma_f32_32x32x16_bf16 v[64:79], v[112:115], a[192:195], 0\n"
        "ds_read_b128 v[128:131], v12 offset:4096\n"
        "v_exp_f32 v82, v82\n"
        "v_exp_f32 v83, v83\n"
        "v_cvt_pk_bf16_f32 v104, v80, v81\n"
        "v_mfma_f32_32x32x16_bf16 v[64:79], v[116:119], a[196:199], v[64:79]\n"
        "ds_read_b128 v[132:135], v13 offset:4096\n"
        "v_exp_f32 v84, v84\n"
        "v_exp_f32 v85, v85\n"
        "v_cvt_pk_bf16_f32 v105, v82, v83\n"
        "v_mfma_f32_32x32x16_bf16 v[64:79], v[120:123], a[200:203], v[64:79]\n"
        "ds_read_b128 v[136:139], v14 offset:4096\n"
        "v_exp_f32 v86, v86\n"
        "v_exp_f32 v87, v87\n"
        "v_cvt_pk_bf16_f32 v106, v84, v85\n"
        "v_mfma_f32_32x32x16_bf16 v[64:79], v[124:127], a[204:207], v[64:79]\n"
        "ds_read_b128 v[140:143], v15 offset:4096\n"
        "v_exp_f32 v88, v88\n"
        "v_exp_f32 v89, v89\n"
        "v_cvt_pk_bf16_f32 v107, v86, v87\n"
        "v_mfma_f32_32x32x16_bf16 a[0:15], v[96:99], v[144:147], a[0:15]\n"
        "ds_read_b64_tr_b16 v[160:161], v16 offset:4096\n"
        "v_exp_f32 v90, v90\n"
        "v_exp_f32 v91, v91\n"
        "v_cvt_pk_bf16_f32 v108, v88, v89\n"
        "v_mfma_f32_32x32x16_bf16 a[16:31], v[96:99], v[148:151], a[16:31]\n"
        "ds_read_b64_tr_b16 v[162:163], v17 offset:4096\n"
        "v_exp_f32 v92, v92\n"
        "v_exp_f32 v93, v93\n"
        "v_cvt_pk_bf16_f32 v109, v90, v91\n"
        "v_mfma_f32_32x32x16_bf16 a[0:15], v[100:103], v[152:155], a[0:15]\n"
        "ds_read_b64_tr_b16 v[164:165], v18 offset:4096\n"
        "v_exp_f32 v94, v94\n"
        "v_exp_f32 v95, v95\n"
        "v_cvt_pk_bf16_f32 v110, v92, v93\n"
        "v_mfma_f32_32x32x16_bf16 a[16:31], v[100:103], v[156:159], a[16:31]\n"
        "ds_read_b64_tr_b16 v[166:167], v19 offset:4096\n"
        "v_exp_f32 v64, v64\n"
        "v_exp_f32 v65, v65\n"
        "v_cvt_pk_bf16_f32 v111, v94, v95\n"
        "v_mfma_f32_32x32x16_bf16 v[80:95], v[112:115], a[208:211], 0\n"
        "ds_read_b64_tr_b16 v[168:169], v16 offset:6144\n"
        "v_exp_f32 v66, v66\n"
        "v_exp_f32 v67, v67\n"
        "v_cvt_pk_bf16_f32 v96, v64, v65\n"
        "v_mfma_f32_32x32x16_bf16 v[80:95], v[116:119], a[212:215], v[80:95]\n"
        "ds_read_b64_tr_b16 v[170:171], v17 offset:6144\n"
        "v_exp_f32 v68, v68\n"
        "v_exp_f32 v69, v69\n"
        "v_cvt_pk_bf16_f32 v97, v66, v67\n"
        "v_mfma_f32_32x32x16_bf16 v[80:95], v[120:123], a[216:219], v[80:95]\n"
        "ds_read_b64_tr_b16 v[172:173], v18 offset:6144\n"
        "v_exp_f32 v70, v70\n"
        "v_exp_f32 v71, v71\n"
        "v_cvt_pk_bf16_f32 v98, v68, v69\n"
        "v_mfma_f32_32x32x16_bf16 v[80:95], v[124:127], a[220:223], v[80:95]\n"
        "ds_read_b64_tr_b16 v[174:175], v19 offset:6144\n"
        "v_exp_f32 v72, v72\n"
        "v_exp_f32 v73, v73\n"
        "v_cvt_pk_bf16_f32 v99, v70, v71\n"
        "v_mfma_f32_32x32x16_bf16 a[32:47], v[104:107], v[144:147], a[32:47]\n"
        "v_exp_f32 v74, v74\n"
        "v_exp_f32 v75, v75\n"
        "v_cvt_pk_bf16_f32 v100, v72, v73\n"
        "v_accvgpr_write_b32 a244, v250\n"
        "v_mfma_f32_32x32x16_bf16 a[48:63], v[104:107], v[148:151], a[48:63]\n"
        "v_exp_f32 v76, v76\n"
        "v_exp_f32 v77, v77\n"
        "v_cvt_pk_bf16_f32 v101, v74, v75\n"
        "v_accvgpr_write_b32 a246, v250\n"
        "v_mfma_f32_32x32x16_bf16 a[32:47], v[108:111], v[152:155], a[32:47]\n"
        "v_exp_f32 v78, v78\n"
        "v_exp_f32 v79, v79\n"
        "v_cvt_pk_bf16_f32 v102, v76, v77\n"
        "v_cmp_eq_u32 vcc, 3, v233\n"
        "v_mfma_f32_32x32x16_bf16 a[48:63], v[108:111], v[156:159], a[48:63]\n"
        "v_exp_f32 v80, v80\n"
        "v_exp_f32 v81, v81\n"
        "v_cvt_pk_bf16_f32 v103, v78, v79\n"
        "v_cndmask_b32 v250, 0, v232, vcc\n"
        "v_mfma_f32_32x32x16_bf16 v[64:79], v[112:115], a[224:227], 0\n"
        "v_exp_f32 v82, v82\n"
        "v_exp_f32 v83, v83\n"
        "v_cvt_pk_bf16_f32 v104, v80, v81\n"
        "v_accvgpr_write_b32 a245, v250\n"
        "v_mfma_f32_32x32x16_bf16 v[64:79], v[116:119], a[228:231], v[64:79]\n"
        "v_exp_f32 v84, v84\n"
        "v_exp_f32 v85, v85\n"
        "v_cvt_pk_bf16_f32 v105, v82, v83\n"
        "v_accvgpr_write_b32 a247, v250\n"
        "v_mfma_f32_32x32x16_bf16 v[64:79], v[120:123], a[232:235], v[64:79]\n"
        "v_exp_f32 v86, v86\n"
        "v_exp_f32 v87, v87\n"
        "v_cvt_pk_bf16_f32 v106, v84, v85\n"
        "v_and_b32 v232, 1, v9\n"
        "v_mfma_f32_32x32x16_bf16 v[64:79], v[124:127], a[236:239], v[64:79]\n"
        "v_exp_f32 v88, v88\n"
        "v_exp_f32 v89, v89\n"
        "v_cvt_pk_bf16_f32 v107, v86, v87\n"
        "v_lshlrev_b32 v232, 4, v232\n"
        "v_mfma_f32_32x32x16_bf16 a[64:79], v[96:99], v[144:147], a[64:79]\n"
        "v_exp_f32 v90, v90\n"
        "v_exp_f32 v91, v91\n"
        "v_cvt_pk_bf16_f32 v108, v88, v89\n"
        "v_lshlrev_b32 v232, v232, v234\n"
        "v_mfma_f32_32x32x16_bf16 a[80:95], v[96:99], v[148:151], a[80:95]\n"
        "v_exp_f32 v92, v92\n"
        "v_exp_f32 v93, v93\n"
        "v_cvt_pk_bf16_f32 v109, v90, v91\n"
        "v_and_b32 v233, 2, v9\n"
        "v_mfma_f32_32x32x16_bf16 a[64:79], v[100:103], v[152:155], a[64:79]\n"
        "s_waitcnt lgkmcnt(0)\n"
        "v_exp_f32 v94, v94\n"
        "v_exp_f32 v95, v95\n"
        "v_cvt_pk_bf16_f32 v110, v92, v93\n"
        "v_cmp_eq_u32 vcc, 0, v233\n"
        "v_mfma_f32_32x32x16_bf16 a[80:95], v[100:103], v[156:159], a[80:95]\n"
        "s_add_u32 m0, s62, 4096\n"
        "v_exp_f32 v64, v64\n"
        "v_exp_f32 v65, v65\n"
        "v_cvt_pk_bf16_f32 v111, v94, v95\n"
        "v_cndmask_b32 v250, 0, v232, vcc\n"
        "v_mfma_f32_32x32x16_bf16 v[80:95], v[128:131], a[160:163], 0\n"
        "buffer_load_dwordx4 v10, s[44:47], s65 offen offset:0 sc1 lds\n"
        "v_exp_f32 v66, v66\n"
        "v_exp_f32 v67, v67\n"
        "v_cvt_pk_bf16_f32 v96, v64, v65\n"
        "v_mfma_f32_32x32x16_bf16 v[80:95], v[132:135], a[164:167], v[80:95]\n"
        "buffer_load_dwordx4 v11, s[44:47], s65 offen offset:1024 sc1 lds\n"
        "v_exp_f32 v68, v68\n"
        "v_exp_f32 v69, v69\n"
        "v_cvt_pk_bf16_f32 v97, v66, v67\n"
        "v_mfma_f32_32x32x16_bf16 v[80:95], v[136:139], a[168:171], v[80:95]\n"
        "buffer_load_dwordx4 v10, s[44:47], s65 offen offset:2048 sc1 lds\n"
        "v_exp_f32 v70, v70\n"
        "v_exp_f32 v71, v71\n"
        "v_cvt_pk_bf16_f32 v98, v68, v69\n"
        "v_mfma_f32_32x32x16_bf16 v[80:95], v[140:143], a[172:175], v[80:95]\n"
        "buffer_load_dwordx4 v11, s[44:47], s65 offen offset:3072 sc1 lds\n"
        "v_exp_f32 v72, v72\n"
        "v_exp_f32 v73, v73\n"
        "v_cvt_pk_bf16_f32 v99, v70, v71\n"
        "v_mfma_f32_32x32x16_bf16 a[96:111], v[104:107], v[144:147], a[96:111]\n"
        "v_exp_f32 v74, v74\n"
        "v_exp_f32 v75, v75\n"
        "v_cvt_pk_bf16_f32 v100, v72, v73\n"
        "v_accvgpr_write_b32 a252, v250\n"
        "v_mfma_f32_32x32x16_bf16 a[112:127], v[104:107], v[148:151], a[112:127]\n"
        "s_add_u32 s65, s65, s66\n"
        "v_exp_f32 v76, v76\n"
        "v_exp_f32 v77, v77\n"
        "v_cvt_pk_bf16_f32 v101, v74, v75\n"
        "v_xor_b32 v250, v250, v232\n"
        "v_mfma_f32_32x32x16_bf16 a[96:111], v[108:111], v[152:155], a[96:111]\n"
        "v_exp_f32 v78, v78\n"
        "v_exp_f32 v79, v79\n"
        "v_cvt_pk_bf16_f32 v102, v76, v77\n"
        "v_accvgpr_write_b32 a253, v250\n"
        "v_mfma_f32_32x32x16_bf16 a[112:127], v[108:111], v[156:159], a[112:127]\n"
        "s_lshl_b32 s77, s58, 7\n"
        "v_exp_f32 v80, v80\n"
        "v_exp_f32 v81, v81\n"
        "v_cvt_pk_bf16_f32 v103, v78, v79\n"
        "v_mfma_f32_32x32x16_bf16 v[64:79], v[128:131], a[176:179], 0\n"
        "s_add_u32 s77, s77, 1280064\n"
        "buffer_load_dwordx2 v[208:209], v32, s[36:39], 0 offen sc1\n"
        "v_exp_f32 v82, v82\n"
        "v_exp_f32 v83, v83\n"
        "v_cvt_pk_bf16_f32 v104, v80, v81\n"
        "v_mfma_f32_32x32x16_bf16 v[64:79], v[132:135], a[180:183], v[64:79]\n"
        "v_exp_f32 v84, v84\n"
        "v_exp_f32 v85, v85\n"
        "v_cvt_pk_bf16_f32 v105, v82, v83\n"
        "v_mov_b32 v232, s77\n"
        "v_mfma_f32_32x32x16_bf16 v[64:79], v[136:139], a[184:187], v[64:79]\n"
        "v_exp_f32 v86, v86\n"
        "v_exp_f32 v87, v87\n"
        "v_cvt_pk_bf16_f32 v106, v84, v85\n"
        "v_cmp_eq_u32 vcc, 0, v8\n"
        "v_mfma_f32_32x32x16_bf16 v[64:79], v[140:143], a[188:191], v[64:79]\n"
        "v_exp_f32 v88, v88\n"
        "v_exp_f32 v89, v89\n"
        "v_cvt_pk_bf16_f32 v107, v86, v87\n"
        "v_cndmask_b32 v21, v235, v232, vcc\n"
        "v_mfma_f32_32x32x16_bf16 a[128:143], v[96:99], v[144:147], a[128:143]\n"
        "v_exp_f32 v90, v90\n"
        "v_exp_f32 v91, v91\n"
        "v_cvt_pk_bf16_f32 v108, v88, v89\n"
        "v_and_or_b32 v232, v9, 3, s75\n"
        "v_mfma_f32_32x32x16_bf16 a[144:159], v[96:99], v[148:151], a[144:159]\n"
        "v_exp_f32 v92, v92\n"
        "v_exp_f32 v93, v93\n"
        "v_cvt_pk_bf16_f32 v109, v90, v91\n"
        "v_lshlrev_b32 v232, 7, v232\n"
        "v_mfma_f32_32x32x16_bf16 a[128:143], v[100:103], v[152:155], a[128:143]\n"
        "v_exp_f32 v94, v94\n"
        "v_exp_f32 v95, v95\n"
        "v_cvt_pk_bf16_f32 v110, v92, v93\n"
        "v_add_u32 v45, 1280064, v232\n"
        "v_mfma_f32_32x32x16_bf16 a[144:159], v[100:103], v[156:159], a[144:159]\n"
        "s_lshl_b32 s77, s59, 12\n"
        "v_exp_f32 v64, v64\n"
        "v_exp_f32 v65, v65\n"
        "v_cvt_pk_bf16_f32 v111, v94, v95\n"
        "v_mfma_f32_32x32x16_bf16 v[80:95], v[128:131], a[192:195], 0\n"
        "ds_read_b128 v[112:115], v12 offset:8192\n"
        "v_exp_f32 v66, v66\n"
        "v_exp_f32 v67, v67\n"
        "v_cvt_pk_bf16_f32 v96, v64, v65\n"
        "v_mfma_f32_32x32x16_bf16 v[80:95], v[132:135], a[196:199], v[80:95]\n"
        "s_add_u32 s77, s77, 1312512\n"
        "ds_read_b128 v[116:119], v13 offset:8192\n"
        "v_exp_f32 v68, v68\n"
        "v_exp_f32 v69, v69\n"
        "v_cvt_pk_bf16_f32 v97, v66, v67\n"
        "v_mfma_f32_32x32x16_bf16 v[80:95], v[136:139], a[200:203], v[80:95]\n"
        "ds_read_b128 v[120:123], v14 offset:8192\n"
        "v_exp_f32 v70, v70\n"
        "v_exp_f32 v71, v71\n"
        "v_cvt_pk_bf16_f32 v98, v68, v69\n"
        "v_mfma_f32_32x32x16_bf16 v[80:95], v[140:143], a[204:207], v[80:95]\n"
        "ds_read_b128 v[124:127], v15 offset:8192\n"
        "v_exp_f32 v72, v72\n"
        "v_exp_f32 v73, v73\n"
        "v_cvt_pk_bf16_f32 v99, v70, v71\n"
        "v_mfma_f32_32x32x16_bf16 a[0:15], v[104:107], v[160:163], a[0:15]\n"
        "ds_read_b64_tr_b16 v[144:145], v16 offset:8192\n"
        "v_exp_f32 v74, v74\n"
        "v_exp_f32 v75, v75\n"
        "v_cvt_pk_bf16_f32 v100, v72, v73\n"
        "v_mfma_f32_32x32x16_bf16 a[16:31], v[104:107], v[164:167], a[16:31]\n"
        "ds_read_b64_tr_b16 v[146:147], v17 offset:8192\n"
        "v_exp_f32 v76, v76\n"
        "v_exp_f32 v77, v77\n"
        "v_cvt_pk_bf16_f32 v101, v74, v75\n"
        "v_mfma_f32_32x32x16_bf16 a[0:15], v[108:111], v[168:171], a[0:15]\n"
        "ds_read_b64_tr_b16 v[148:149], v18 offset:8192\n"
        "v_exp_f32 v78, v78\n"
        "v_exp_f32 v79, v79\n"
        "v_cvt_pk_bf16_f32 v102, v76, v77\n"
        "v_mfma_f32_32x32x16_bf16 a[16:31], v[108:111], v[172:175], a[16:31]\n"
        "ds_read_b64_tr_b16 v[150:151], v19 offset:8192\n"
        "v_exp_f32 v80, v80\n"
        "v_exp_f32 v81, v81\n"
        "v_cvt_pk_bf16_f32 v103, v78, v79\n"
        "v_mfma_f32_32x32x16_bf16 v[64:79], v[128:131], a[208:211], 0\n"
        "ds_read_b64_tr_b16 v[152:153], v16 offset:10240\n"
        "v_exp_f32 v82, v82\n"
        "v_exp_f32 v83, v83\n"
        "v_cvt_pk_bf16_f32 v104, v80, v81\n"
        "v_mfma_f32_32x32x16_bf16 v[64:79], v[132:135], a[212:215], v[64:79]\n"
        "ds_read_b64_tr_b16 v[154:155], v17 offset:10240\n"
        "v_exp_f32 v84, v84\n"
        "v_exp_f32 v85, v85\n"
        "v_cvt_pk_bf16_f32 v105, v82, v83\n"
        "v_mfma_f32_32x32x16_bf16 v[64:79], v[136:139], a[216:219], v[64:79]\n"
        "ds_read_b64_tr_b16 v[156:157], v18 offset:10240\n"
        "v_exp_f32 v86, v86\n"
        "v_exp_f32 v87, v87\n"
        "v_cvt_pk_bf16_f32 v106, v84, v85\n"
        "v_mfma_f32_32x32x16_bf16 v[64:79], v[140:143], a[220:223], v[64:79]\n"
        "ds_read_b64_tr_b16 v[158:159], v19 offset:10240\n"
        "v_exp_f32 v88, v88\n"
        "v_exp_f32 v89, v89\n"
        "v_cvt_pk_bf16_f32 v107, v86, v87\n"
        "v_mfma_f32_32x32x16_bf16 a[32:47], v[96:99], v[160:163], a[32:47]\n"
        "v_exp_f32 v90, v90\n"
        "v_exp_f32 v91, v91\n"
        "v_cvt_pk_bf16_f32 v108, v88, v89\n"
        "v_add_u32 v38, s77, v248\n"
        "v_mfma_f32_32x32x16_bf16 a[48:63], v[96:99], v[164:167], a[48:63]\n"
        "v_exp_f32 v92, v92\n"
        "v_exp_f32 v93, v93\n"
        "v_cvt_pk_bf16_f32 v109, v90, v91\n"
        "v_xor_b32 v232, v247, v60\n"
        "v_mfma_f32_32x32x16_bf16 a[32:47], v[100:103], v[168:171], a[32:47]\n"
        "v_exp_f32 v94, v94\n"
        "v_exp_f32 v95, v95\n"
        "v_cvt_pk_bf16_f32 v110, v92, v93\n"
        "v_lshlrev_b32 v232, 4, v232\n"
        "v_mfma_f32_32x32x16_bf16 a[48:63], v[100:103], v[172:175], a[48:63]\n"
        "v_exp_f32 v64, v64\n"
        "v_exp_f32 v65, v65\n"
        "v_cvt_pk_bf16_f32 v111, v94, v95\n"
        "v_lshl_add_u32 v232, v246, 7, v232\n"
        "v_mfma_f32_32x32x16_bf16 v[80:95], v[128:131], a[224:227], 0\n"
        "s_mul_i32 s77, s59, 1088\n"
        "v_exp_f32 v66, v66\n"
        "v_exp_f32 v67, v67\n"
        "v_cvt_pk_bf16_f32 v96, v64, v65\n"
        "v_xor_b32 v232, s77, v232\n"
        "v_mfma_f32_32x32x16_bf16 v[80:95], v[132:135], a[228:231], v[80:95]\n"
        "v_exp_f32 v68, v68\n"
        "v_exp_f32 v69, v69\n"
        "v_cvt_pk_bf16_f32 v97, v66, v67\n"
        "v_add_u32 v232, 1277952, v232\n"
        "v_mfma_f32_32x32x16_bf16 v[80:95], v[136:139], a[232:235], v[80:95]\n"
        "v_exp_f32 v70, v70\n"
        "v_exp_f32 v71, v71\n"
        "v_cvt_pk_bf16_f32 v98, v68, v69\n"
        "v_cmp_gt_u32 vcc, 128, v8\n"
        "v_mfma_f32_32x32x16_bf16 v[80:95], v[140:143], a[236:239], v[80:95]\n"
        "v_exp_f32 v72, v72\n"
        "v_exp_f32 v73, v73\n"
        "v_cvt_pk_bf16_f32 v99, v70, v71\n"
        "v_add_u32 v233, 1326848, v249\n"
        "v_mfma_f32_32x32x16_bf16 a[64:79], v[104:107], v[160:163], a[64:79]\n"
        "v_exp_f32 v74, v74\n"
        "v_exp_f32 v75, v75\n"
        "v_cvt_pk_bf16_f32 v100, v72, v73\n"
        "v_cndmask_b32 v56, v233, v232, vcc\n"
        "v_mfma_f32_32x32x16_bf16 a[80:95], v[104:107], v[164:167], a[80:95]\n"
        "ds_read_b128 v[46:49], v20 offset:24576\n"
        "v_exp_f32 v76, v76\n"
        "v_exp_f32 v77, v77\n"
        "v_cvt_pk_bf16_f32 v101, v74, v75\n"
        "v_mfma_f32_32x32x16_bf16 a[64:79], v[108:111], v[168:171], a[64:79]\n"
        "s_waitcnt lgkmcnt(1)\n"
        "ds_read_b128 v[50:53], v20 offset:20480\n"
        "v_exp_f32 v78, v78\n"
        "v_exp_f32 v79, v79\n"
        "v_cvt_pk_bf16_f32 v102, v76, v77\n"
        "v_mfma_f32_32x32x16_bf16 a[80:95], v[108:111], v[172:175], a[80:95]\n"
        "s_add_u32 m0, s62, 8192\n"
        "v_exp_f32 v80, v80\n"
        "v_exp_f32 v81, v81\n"
        "v_cvt_pk_bf16_f32 v103, v78, v79\n"
        "v_mfma_f32_32x32x16_bf16 v[64:79], v[112:115], a[160:163], 0\n"
        "buffer_load_dwordx4 v10, s[44:47], s65 offen offset:0 sc1 lds\n"
        "v_exp_f32 v82, v82\n"
        "v_exp_f32 v83, v83\n"
        "v_cvt_pk_bf16_f32 v104, v80, v81\n"
        "v_mfma_f32_32x32x16_bf16 v[64:79], v[116:119], a[164:167], v[64:79]\n"
        "buffer_load_dwordx4 v11, s[44:47], s65 offen offset:1024 sc1 lds\n"
        "v_exp_f32 v84, v84\n"
        "v_exp_f32 v85, v85\n"
        "v_cvt_pk_bf16_f32 v105, v82, v83\n"
        "v_mfma_f32_32x32x16_bf16 v[64:79], v[120:123], a[168:171], v[64:79]\n"
        "buffer_load_dwordx4 v10, s[44:47], s65 offen offset:2048 sc1 lds\n"
        "v_exp_f32 v86, v86\n"
        "v_exp_f32 v87, v87\n"
        "v_cvt_pk_bf16_f32 v106, v84, v85\n"
        "v_mfma_f32_32x32x16_bf16 v[64:79], v[124:127], a[172:175], v[64:79]\n"
        "buffer_load_dwordx4 v11, s[44:47], s65 offen offset:3072 sc1 lds\n"
        "v_exp_f32 v88, v88\n"
        "v_exp_f32 v89, v89\n"
        "v_cvt_pk_bf16_f32 v107, v86, v87\n"
        "v_mfma_f32_32x32x16_bf16 a[96:111], v[96:99], v[160:163], a[96:111]\n"
        "v_exp_f32 v90, v90\n"
        "v_exp_f32 v91, v91\n"
        "v_cvt_pk_bf16_f32 v108, v88, v89\n"
        "v_mfma_f32_32x32x16_bf16 a[112:127], v[96:99], v[164:167], a[112:127]\n"
        "s_add_u32 s65, s65, s66\n"
        "s_waitcnt lgkmcnt(1)\n"
        "ds_write_b128 v55, v[46:49]\n"
        "v_exp_f32 v92, v92\n"
        "v_exp_f32 v93, v93\n"
        "v_cvt_pk_bf16_f32 v109, v90, v91\n"
        "v_mfma_f32_32x32x16_bf16 a[96:111], v[100:103], v[168:171], a[96:111]\n"
        "s_waitcnt vmcnt(4)\n"
        "v_exp_f32 v94, v94\n"
        "v_exp_f32 v95, v95\n"
        "v_cvt_pk_bf16_f32 v110, v92, v93\n"
        "v_lshrrev_b64 v[210:211], 24, v[208:209]\n"
        "v_mfma_f32_32x32x16_bf16 a[112:127], v[100:103], v[172:175], a[112:127]\n"
        "v_exp_f32 v64, v64\n"
        "v_exp_f32 v65, v65\n"
        "v_cvt_pk_bf16_f32 v111, v94, v95\n"
        "v_cmp_ne_u64 vcc, s[54:55], v[210:211]\n"
        "v_mfma_f32_32x32x16_bf16 v[80:95], v[112:115], a[176:179], 0\n"
        "s_cbranch_vccnz 107f\n"
        "108:\n"
        "v_exp_f32 v66, v66\n"
        "v_exp_f32 v67, v67\n"
        "v_cvt_pk_bf16_f32 v96, v64, v65\n"
        "v_and_b32 v210, 0xfff, v208\n"
        "v_mfma_f32_32x32x16_bf16 v[80:95], v[116:119], a[180:183], v[80:95]\n"
        "v_exp_f32 v68, v68\n"
        "v_exp_f32 v69, v69\n"
        "v_cvt_pk_bf16_f32 v97, v66, v67\n"
        "v_lshl_add_u32 v210, v210, 2, v33\n"
        "v_mfma_f32_32x32x16_bf16 v[80:95], v[120:123], a[184:187], v[80:95]\n"
        "v_exp_f32 v70, v70\n"
        "v_exp_f32 v71, v71\n"
        "v_cvt_pk_bf16_f32 v98, v68, v69\n"
        "v_add_u32_dpp v212, v210, v39 quad_perm:[0,0,0,0] row_mask:0xf bank_mask:0xf\n"
        "v_mfma_f32_32x32x16_bf16 v[80:95], v[124:127], a[188:191], v[80:95]\n"
        "buffer_load_dwordx4 v[176:179], v212, s[36:39], 0 offen sc1\n"
        "v_exp_f32 v72, v72\n"
        "v_exp_f32 v73, v73\n"
        "v_cvt_pk_bf16_f32 v99, v70, v71\n"
        "v_mfma_f32_32x32x16_bf16 a[128:143], v[104:107], v[160:163], a[128:143]\n"
        "v_exp_f32 v74, v74\n"
        "v_exp_f32 v75, v75\n"
        "v_cvt_pk_bf16_f32 v100, v72, v73\n"
        "v_bfe_u32 v62, v208, 12, v34\n"
        "v_mfma_f32_32x32x16_bf16 a[144:159], v[104:107], v[164:167], a[144:159]\n"
        "v_exp_f32 v76, v76\n"
        "v_exp_f32 v77, v77\n"
        "v_cvt_pk_bf16_f32 v101, v74, v75\n"
        "v_min_u32 v211, 64, v62\n"
        "v_mfma_f32_32x32x16_bf16 a[128:143], v[108:111], v[168:171], a[128:143]\n"
        "s_waitcnt lgkmcnt(1)\n"
        "ds_write_b128 v55, v[50:53] offset:4608\n"
        "v_exp_f32 v78, v78\n"
        "v_exp_f32 v79, v79\n"
        "v_cvt_pk_bf16_f32 v102, v76, v77\n"
        "v_mfma_f32_32x32x16_bf16 a[144:159], v[108:111], v[172:175], a[144:159]\n"
        "s_mul_i32 s78, s59, 10\n"
        "v_exp_f32 v80, v80\n"
        "v_exp_f32 v81, v81\n"
        "v_cvt_pk_bf16_f32 v103, v78, v79\n"
        "v_mfma_f32_32x32x16_bf16 v[64:79], v[112:115], a[192:195], 0\n"
        "ds_read_b128 v[128:131], v12 offset:12288\n"
        "v_exp_f32 v82, v82\n"
        "v_exp_f32 v83, v83\n"
        "v_cvt_pk_bf16_f32 v104, v80, v81\n"
        "v_mfma_f32_32x32x16_bf16 v[64:79], v[116:119], a[196:199], v[64:79]\n"
        "s_and_b32 s84, s78, -4\n"
        "ds_read_b128 v[132:135], v13 offset:12288\n"
        "v_exp_f32 v84, v84\n"
        "v_exp_f32 v85, v85\n"
        "v_cvt_pk_bf16_f32 v105, v82, v83\n"
        "v_mfma_f32_32x32x16_bf16 v[64:79], v[120:123], a[200:203], v[64:79]\n"
        "s_and_b32 s79, s78, 2\n"
        "ds_read_b128 v[136:139], v14 offset:12288\n"
        "v_exp_f32 v86, v86\n"
        "v_exp_f32 v87, v87\n"
        "v_cvt_pk_bf16_f32 v106, v84, v85\n"
        "v_mfma_f32_32x32x16_bf16 v[64:79], v[124:127], a[204:207], v[64:79]\n"
        "s_mul_i32 s77, s58, 40\n"
        "ds_read_b128 v[140:143], v15 offset:12288\n"
        "v_exp_f32 v88, v88\n"
        "v_exp_f32 v89, v89\n"
        "v_cvt_pk_bf16_f32 v107, v86, v87\n"
        "v_mfma_f32_32x32x16_bf16 a[0:15], v[96:99], v[144:147], a[0:15]\n"
        "s_add_u32 s77, s77, s84\n"
        "ds_read_b64_tr_b16 v[160:161], v16 offset:12288\n"
        "v_exp_f32 v90, v90\n"
        "v_exp_f32 v91, v91\n"
        "v_cvt_pk_bf16_f32 v108, v88, v89\n"
        "v_mfma_f32_32x32x16_bf16 a[16:31], v[96:99], v[148:151], a[16:31]\n"
        "ds_read_b64_tr_b16 v[162:163], v17 offset:12288\n"
        "v_exp_f32 v92, v92\n"
        "v_exp_f32 v93, v93\n"
        "v_cvt_pk_bf16_f32 v109, v90, v91\n"
        "v_mfma_f32_32x32x16_bf16 a[0:15], v[100:103], v[152:155], a[0:15]\n"
        "ds_read_b64_tr_b16 v[164:165], v18 offset:12288\n"
        "v_exp_f32 v94, v94\n"
        "v_exp_f32 v95, v95\n"
        "v_cvt_pk_bf16_f32 v110, v92, v93\n"
        "v_mfma_f32_32x32x16_bf16 a[16:31], v[100:103], v[156:159], a[16:31]\n"
        "ds_read_b64_tr_b16 v[166:167], v19 offset:12288\n"
        "v_exp_f32 v64, v64\n"
        "v_exp_f32 v65, v65\n"
        "v_cvt_pk_bf16_f32 v111, v94, v95\n"
        "v_mfma_f32_32x32x16_bf16 v[80:95], v[112:115], a[208:211], 0\n"
        "ds_read_b64_tr_b16 v[168:169], v16 offset:14336\n"
        "v_exp_f32 v66, v66\n"
        "v_exp_f32 v67, v67\n"
        "v_cvt_pk_bf16_f32 v96, v64, v65\n"
        "v_mfma_f32_32x32x16_bf16 v[80:95], v[116:119], a[212:215], v[80:95]\n"
        "ds_read_b64_tr_b16 v[170:171], v17 offset:14336\n"
        "v_exp_f32 v68, v68\n"
        "v_exp_f32 v69, v69\n"
        "v_cvt_pk_bf16_f32 v97, v66, v67\n"
        "v_mfma_f32_32x32x16_bf16 v[80:95], v[120:123], a[216:219], v[80:95]\n"
        "ds_read_b64_tr_b16 v[172:173], v18 offset:14336\n"
        "v_exp_f32 v70, v70\n"
        "v_exp_f32 v71, v71\n"
        "v_cvt_pk_bf16_f32 v98, v68, v69\n"
        "v_mfma_f32_32x32x16_bf16 v[80:95], v[124:127], a[220:223], v[80:95]\n"
        "ds_read_b64_tr_b16 v[174:175], v19 offset:14336\n"
        "v_exp_f32 v72, v72\n"
        "v_exp_f32 v73, v73\n"
        "v_cvt_pk_bf16_f32 v99, v70, v71\n"
        "v_mfma_f32_32x32x16_bf16 a[32:47], v[104:107], v[144:147], a[32:47]\n"
        "v_exp_f32 v74, v74\n"
        "v_exp_f32 v75, v75\n"
        "v_cvt_pk_bf16_f32 v100, v72, v73\n"
        "v_add_u32_dpp v213, v210, v39 quad_perm:[1,1,1,1] row_mask:0xf bank_mask:0xf\n"
        "v_mfma_f32_32x32x16_bf16 a[48:63], v[104:107], v[148:151], a[48:63]\n"
        "buffer_load_dwordx4 v[180:183], v213, s[36:39], 0 offen sc1\n"
        "v_exp_f32 v76, v76\n"
        "v_exp_f32 v77, v77\n"
        "v_cvt_pk_bf16_f32 v101, v74, v75\n"
        "v_mfma_f32_32x32x16_bf16 a[32:47], v[108:111], v[152:155], a[32:47]\n"
        "v_exp_f32 v78, v78\n"
        "v_exp_f32 v79, v79\n"
        "v_cvt_pk_bf16_f32 v102, v76, v77\n"
        "v_add_u32 v211, 3, v211\n"
        "v_mfma_f32_32x32x16_bf16 a[48:63], v[108:111], v[156:159], a[48:63]\n"
        "v_exp_f32 v80, v80\n"
        "v_exp_f32 v81, v81\n"
        "v_cvt_pk_bf16_f32 v103, v78, v79\n"
        "v_lshrrev_b32 v211, 2, v211\n"
        "v_mfma_f32_32x32x16_bf16 v[64:79], v[112:115], a[224:227], 0\n"
        "v_exp_f32 v82, v82\n"
        "v_exp_f32 v83, v83\n"
        "v_cvt_pk_bf16_f32 v104, v80, v81\n"
        "v_lshl_add_u32 v232, v60, 2, s77\n"
        "v_mfma_f32_32x32x16_bf16 v[64:79], v[116:119], a[228:231], v[64:79]\n"
        "v_exp_f32 v84, v84\n"
        "v_exp_f32 v85, v85\n"
        "v_cvt_pk_bf16_f32 v105, v82, v83\n"
        "v_min_u32 v48, 9996, v232\n"
        "v_mfma_f32_32x32x16_bf16 v[64:79], v[120:123], a[232:235], v[64:79]\n"
        "v_exp_f32 v86, v86\n"
        "v_exp_f32 v87, v87\n"
        "v_cvt_pk_bf16_f32 v106, v84, v85\n"
        "v_add_u32_dpp v212, v210, v39 quad_perm:[2,2,2,2] row_mask:0xf bank_mask:0xf\n"
        "v_mfma_f32_32x32x16_bf16 v[64:79], v[124:127], a[236:239], v[64:79]\n"
        "buffer_load_dwordx4 v[184:187], v212, s[36:39], 0 offen sc1\n"
        "v_exp_f32 v88, v88\n"
        "v_exp_f32 v89, v89\n"
        "v_cvt_pk_bf16_f32 v107, v86, v87\n"
        "v_mfma_f32_32x32x16_bf16 a[64:79], v[96:99], v[144:147], a[64:79]\n"
        "v_exp_f32 v90, v90\n"
        "v_exp_f32 v91, v91\n"
        "v_cvt_pk_bf16_f32 v108, v88, v89\n"
        "v_lshlrev_b32 v211, v211, -1\n"
        "v_mfma_f32_32x32x16_bf16 a[80:95], v[96:99], v[148:151], a[80:95]\n"
        "v_exp_f32 v92, v92\n"
        "v_exp_f32 v93, v93\n"
        "v_cvt_pk_bf16_f32 v109, v90, v91\n"
        "v_lshlrev_b32 v48, 1, v48\n"
        "v_mfma_f32_32x32x16_bf16 a[64:79], v[100:103], v[152:155], a[64:79]\n"
        "s_waitcnt lgkmcnt(0)\n"
        "v_exp_f32 v94, v94\n"
        "v_exp_f32 v95, v95\n"
        "v_cvt_pk_bf16_f32 v110, v92, v93\n"
        "v_lshlrev_b32 v232, 7, v232\n"
        "v_mfma_f32_32x32x16_bf16 a[80:95], v[100:103], v[156:159], a[80:95]\n"
        "s_add_u32 m0, s62, 12288\n"
        "v_exp_f32 v64, v64\n"
        "v_exp_f32 v65, v65\n"
        "v_cvt_pk_bf16_f32 v111, v94, v95\n"
        "v_lshl_add_u32 v232, v59, 3, v232\n"
        "v_mfma_f32_32x32x16_bf16 v[80:95], v[128:131], a[160:163], 0\n"
        "buffer_load_dwordx4 v10, s[44:47], s65 offen offset:0 sc1 lds\n"
        "v_exp_f32 v66, v66\n"
        "v_exp_f32 v67, v67\n"
        "v_cvt_pk_bf16_f32 v96, v64, v65\n"
        "v_mfma_f32_32x32x16_bf16 v[80:95], v[132:135], a[164:167], v[80:95]\n"
        "buffer_load_dwordx4 v11, s[44:47], s65 offen offset:1024 sc1 lds\n"
        "v_exp_f32 v68, v68\n"
        "v_exp_f32 v69, v69\n"
        "v_cvt_pk_bf16_f32 v97, v66, v67\n"
        "v_mfma_f32_32x32x16_bf16 v[80:95], v[136:139], a[168:171], v[80:95]\n"
        "buffer_load_dwordx4 v10, s[44:47], s65 offen offset:2048 sc1 lds\n"
        "v_exp_f32 v70, v70\n"
        "v_exp_f32 v71, v71\n"
        "v_cvt_pk_bf16_f32 v98, v68, v69\n"
        "v_mfma_f32_32x32x16_bf16 v[80:95], v[140:143], a[172:175], v[80:95]\n"
        "buffer_load_dwordx4 v11, s[44:47], s65 offen offset:3072 sc1 lds\n"
        "v_exp_f32 v72, v72\n"
        "v_exp_f32 v73, v73\n"
        "v_cvt_pk_bf16_f32 v99, v70, v71\n"
        "v_mfma_f32_32x32x16_bf16 a[96:111], v[104:107], v[144:147], a[96:111]\n"
        "v_exp_f32 v74, v74\n"
        "v_exp_f32 v75, v75\n"
        "v_cvt_pk_bf16_f32 v100, v72, v73\n"
        "v_add_u32_dpp v213, v210, v39 quad_perm:[3,3,3,3] row_mask:0xf bank_mask:0xf\n"
        "v_mfma_f32_32x32x16_bf16 a[112:127], v[104:107], v[148:151], a[112:127]\n"
        "s_add_u32 s65, s65, s66\n"
        "buffer_load_dwordx4 v[188:191], v213, s[36:39], 0 offen sc1\n"
        "v_exp_f32 v76, v76\n"
        "v_exp_f32 v77, v77\n"
        "v_cvt_pk_bf16_f32 v101, v74, v75\n"
        "v_mfma_f32_32x32x16_bf16 a[96:111], v[108:111], v[152:155], a[96:111]\n"
        "v_exp_f32 v78, v78\n"
        "v_exp_f32 v79, v79\n"
        "v_cvt_pk_bf16_f32 v102, v76, v77\n"
        "v_lshlrev_b32 v233, 2, v60\n"
        "v_mfma_f32_32x32x16_bf16 a[112:127], v[108:111], v[156:159], a[112:127]\n"
        "v_exp_f32 v80, v80\n"
        "v_exp_f32 v81, v81\n"
        "v_cvt_pk_bf16_f32 v103, v78, v79\n"
        "v_subrev_u32 v233, s79, v233\n"
        "v_mfma_f32_32x32x16_bf16 v[64:79], v[128:131], a[176:179], 0\n"
        "v_exp_f32 v82, v82\n"
        "v_exp_f32 v83, v83\n"
        "v_cvt_pk_bf16_f32 v104, v80, v81\n"
        "v_cmp_gt_u32 s[86:87], 10, v233\n"
        "v_mfma_f32_32x32x16_bf16 v[64:79], v[132:135], a[180:183], v[64:79]\n"
        "v_exp_f32 v84, v84\n"
        "v_exp_f32 v85, v85\n"
        "v_cvt_pk_bf16_f32 v105, v82, v83\n"
        "v_add_u32 v233, 2, v233\n"
        "v_mfma_f32_32x32x16_bf16 v[64:79], v[136:139], a[184:187], v[64:79]\n"
        "v_exp_f32 v86, v86\n"
        "v_exp_f32 v87, v87\n"
        "v_cvt_pk_bf16_f32 v106, v84, v85\n"
        "v_lshl_or_b32 v212, v211, 27, v210\n"
        "v_mfma_f32_32x32x16_bf16 v[64:79], v[140:143], a[188:191], v[64:79]\n"
        "buffer_load_dwordx4 v[192:195], v212, s[36:39], 0 offen offset:64 sc1\n"
        "v_exp_f32 v88, v88\n"
        "v_exp_f32 v89, v89\n"
        "v_cvt_pk_bf16_f32 v107, v86, v87\n"
        "v_mfma_f32_32x32x16_bf16 a[128:143], v[96:99], v[144:147], a[128:143]\n"
        "v_exp_f32 v90, v90\n"
        "v_exp_f32 v91, v91\n"
        "v_cvt_pk_bf16_f32 v108, v88, v89\n"
        "v_cndmask_b32 v46, v235, v232, s[86:87]\n"
        "v_mfma_f32_32x32x16_bf16 a[144:159], v[96:99], v[148:151], a[144:159]\n"
        "v_exp_f32 v92, v92\n"
        "v_exp_f32 v93, v93\n"
        "v_cvt_pk_bf16_f32 v109, v90, v91\n"
        "v_lshl_or_b32 v213, v211, 26, v210\n"
        "v_mfma_f32_32x32x16_bf16 a[128:143], v[100:103], v[152:155], a[128:143]\n"
        "buffer_load_dwordx4 v[196:199], v213, s[36:39], 0 offen offset:80 sc1\n"
        "v_exp_f32 v94, v94\n"
        "v_exp_f32 v95, v95\n"
        "v_cvt_pk_bf16_f32 v110, v92, v93\n"
        "v_mfma_f32_32x32x16_bf16 a[144:159], v[100:103], v[156:159], a[144:159]\n"
        "v_exp_f32 v64, v64\n"
        "v_exp_f32 v65, v65\n"
        "v_cvt_pk_bf16_f32 v111, v94, v95\n"
        "v_cmp_gt_u32 s[86:87], 10, v233\n"
        "v_mfma_f32_32x32x16_bf16 v[80:95], v[128:131], a[192:195], 0\n"
        "ds_read_b128 v[112:115], v12 offset:0\n"
        "v_exp_f32 v66, v66\n"
        "v_exp_f32 v67, v67\n"
        "v_cvt_pk_bf16_f32 v96, v64, v65\n"
        "v_mfma_f32_32x32x16_bf16 v[80:95], v[132:135], a[196:199], v[80:95]\n"
        "ds_read_b128 v[116:119], v13 offset:0\n"
        "v_exp_f32 v68, v68\n"
        "v_exp_f32 v69, v69\n"
        "v_cvt_pk_bf16_f32 v97, v66, v67\n"
        "v_mfma_f32_32x32x16_bf16 v[80:95], v[136:139], a[200:203], v[80:95]\n"
        "ds_read_b128 v[120:123], v14 offset:0\n"
        "v_exp_f32 v70, v70\n"
        "v_exp_f32 v71, v71\n"
        "v_cvt_pk_bf16_f32 v98, v68, v69\n"
        "v_mfma_f32_32x32x16_bf16 v[80:95], v[140:143], a[204:207], v[80:95]\n"
        "ds_read_b128 v[124:127], v15 offset:0\n"
        "v_exp_f32 v72, v72\n"
        "v_exp_f32 v73, v73\n"
        "v_cvt_pk_bf16_f32 v99, v70, v71\n"
        "v_mfma_f32_32x32x16_bf16 a[0:15], v[104:107], v[160:163], a[0:15]\n"
        "ds_read_b64_tr_b16 v[144:145], v16 offset:0\n"
        "v_exp_f32 v74, v74\n"
        "v_exp_f32 v75, v75\n"
        "v_cvt_pk_bf16_f32 v100, v72, v73\n"
        "v_mfma_f32_32x32x16_bf16 a[16:31], v[104:107], v[164:167], a[16:31]\n"
        "ds_read_b64_tr_b16 v[146:147], v17 offset:0\n"
        "v_exp_f32 v76, v76\n"
        "v_exp_f32 v77, v77\n"
        "v_cvt_pk_bf16_f32 v101, v74, v75\n"
        "v_mfma_f32_32x32x16_bf16 a[0:15], v[108:111], v[168:171], a[0:15]\n"
        "ds_read_b64_tr_b16 v[148:149], v18 offset:0\n"
        "v_exp_f32 v78, v78\n"
        "v_exp_f32 v79, v79\n"
        "v_cvt_pk_bf16_f32 v102, v76, v77\n"
        "v_mfma_f32_32x32x16_bf16 a[16:31], v[108:111], v[172:175], a[16:31]\n"
        "ds_read_b64_tr_b16 v[150:151], v19 offset:0\n"
        "v_exp_f32 v80, v80\n"
        "v_exp_f32 v81, v81\n"
        "v_cvt_pk_bf16_f32 v103, v78, v79\n"
        "v_mfma_f32_32x32x16_bf16 v[64:79], v[128:131], a[208:211], 0\n"
        "ds_read_b64_tr_b16 v[152:153], v16 offset:2048\n"
        "v_exp_f32 v82, v82\n"
        "v_exp_f32 v83, v83\n"
        "v_cvt_pk_bf16_f32 v104, v80, v81\n"
        "v_mfma_f32_32x32x16_bf16 v[64:79], v[132:135], a[212:215], v[64:79]\n"
        "ds_read_b64_tr_b16 v[154:155], v17 offset:2048\n"
        "v_exp_f32 v84, v84\n"
        "v_exp_f32 v85, v85\n"
        "v_cvt_pk_bf16_f32 v105, v82, v83\n"
        "v_mfma_f32_32x32x16_bf16 v[64:79], v[136:139], a[216:219], v[64:79]\n"
        "ds_read_b64_tr_b16 v[156:157], v18 offset:2048\n"
        "v_exp_f32 v86, v86\n"
        "v_exp_f32 v87, v87\n"
        "v_cvt_pk_bf16_f32 v106, v84, v85\n"
        "v_mfma_f32_32x32x16_bf16 v[64:79], v[140:143], a[220:223], v[64:79]\n"
        "ds_read_b64_tr_b16 v[158:159], v19 offset:2048\n"
        "v_exp_f32 v88, v88\n"
        "v_exp_f32 v89, v89\n"
        "v_cvt_pk_bf16_f32 v107, v86, v87\n"
        "v_mfma_f32_32x32x16_bf16 a[32:47], v[96:99], v[160:163], a[32:47]\n"
        "v_exp_f32 v90, v90\n"
        "v_exp_f32 v91, v91\n"
        "v_cvt_pk_bf16_f32 v108, v88, v89\n"
        "v_lshl_or_b32 v212, v211, 25, v210\n"
        "v_mfma_f32_32x32x16_bf16 a[48:63], v[96:99], v[164:167], a[48:63]\n"
        "buffer_load_dwordx4 v[200:203], v212, s[36:39], 0 offen offset:96 sc1\n"
        "v_exp_f32 v92, v92\n"
        "v_exp_f32 v93, v93\n"
        "v_cvt_pk_bf16_f32 v109, v90, v91\n"
        "v_mfma_f32_32x32x16_bf16 a[32:47], v[100:103], v[168:171], a[32:47]\n"
        "v_exp_f32 v94, v94\n"
        "v_exp_f32 v95, v95\n"
        "v_cvt_pk_bf16_f32 v110, v92, v93\n"
        "v_cndmask_b32 v47, v235, v232, s[86:87]\n"
        "v_mfma_f32_32x32x16_bf16 a[48:63], v[100:103], v[172:175], a[48:63]\n"
        "v_exp_f32 v64, v64\n"
        "v_exp_f32 v65, v65\n"
        "v_cvt_pk_bf16_f32 v111, v94, v95\n"
        "v_lshl_or_b32 v213, v211, 24, v210\n"
        "v_mfma_f32_32x32x16_bf16 v[80:95], v[128:131], a[224:227], 0\n"
        "buffer_load_dwordx4 v[204:207], v213, s[36:39], 0 offen offset:112 sc1\n"
        "v_exp_f32 v66, v66\n"
        "v_exp_f32 v67, v67\n"
        "v_cvt_pk_bf16_f32 v96, v64, v65\n"
        "v_mfma_f32_32x32x16_bf16 v[80:95], v[132:135], a[228:231], v[80:95]\n"
        "v_exp_f32 v68, v68\n"
        "v_exp_f32 v69, v69\n"
        "v_cvt_pk_bf16_f32 v97, v66, v67\n"
        "v_bfe_u32 v232, v9, 3, 2\n"
        "v_mfma_f32_32x32x16_bf16 v[80:95], v[136:139], a[232:235], v[80:95]\n"
        "v_exp_f32 v70, v70\n"
        "v_exp_f32 v71, v71\n"
        "v_cvt_pk_bf16_f32 v98, v68, v69\n"
        "v_and_b32 v233, 3, v9\n"
        "v_mfma_f32_32x32x16_bf16 v[80:95], v[140:143], a[236:239], v[80:95]\n"
        "v_exp_f32 v72, v72\n"
        "v_exp_f32 v73, v73\n"
        "v_cvt_pk_bf16_f32 v99, v70, v71\n"
        "v_lshl_add_u32 v232, v232, 2, v233\n"
        "v_mfma_f32_32x32x16_bf16 a[64:79], v[104:107], v[160:163], a[64:79]\n"
        "v_exp_f32 v74, v74\n"
        "v_exp_f32 v75, v75\n"
        "v_cvt_pk_bf16_f32 v100, v72, v73\n"
        "v_bfe_u32 v234, v9, 2, 1\n"
        "v_mfma_f32_32x32x16_bf16 a[80:95], v[104:107], v[164:167], a[80:95]\n"
        "v_exp_f32 v76, v76\n"
        "v_exp_f32 v77, v77\n"
        "v_cvt_pk_bf16_f32 v101, v74, v75\n"
        "v_lshl_add_u32 v232, v234, 4, v232\n"
        "v_mfma_f32_32x32x16_bf16 a[64:79], v[108:111], v[168:171], a[64:79]\n"
        "s_waitcnt lgkmcnt(0)\n"
        "v_exp_f32 v78, v78\n"
        "v_exp_f32 v79, v79\n"
        "v_cvt_pk_bf16_f32 v102, v76, v77\n"
        "v_mul_u32_u24 v232, 96, v232\n"
        "v_mfma_f32_32x32x16_bf16 a[80:95], v[108:111], v[172:175], a[80:95]\n"
        "s_add_u32 m0, s62, 0\n"
        "v_exp_f32 v80, v80\n"
        "v_exp_f32 v81, v81\n"
        "v_cvt_pk_bf16_f32 v103, v78, v79\n"
        "v_lshrrev_b32 v234, 5, v9\n"
        "v_mfma_f32_32x32x16_bf16 v[64:79], v[112:115], a[160:163], 0\n"
        "s_waitcnt vmcnt(11)\n"
        "buffer_load_dwordx4 v10, s[44:47], s65 offen offset:0 sc1 lds\n"
        "v_exp_f32 v82, v82\n"
        "v_exp_f32 v83, v83\n"
        "v_cvt_pk_bf16_f32 v104, v80, v81\n"
        "v_mfma_f32_32x32x16_bf16 v[64:79], v[116:119], a[164:167], v[64:79]\n"
        "buffer_load_dwordx4 v11, s[44:47], s65 offen offset:1024 sc1 lds\n"
        "v_exp_f32 v84, v84\n"
        "v_exp_f32 v85, v85\n"
        "v_cvt_pk_bf16_f32 v105, v82, v83\n"
        "v_mfma_f32_32x32x16_bf16 v[64:79], v[120:123], a[168:171], v[64:79]\n"
        "buffer_load_dwordx4 v10, s[44:47], s65 offen offset:2048 sc1 lds\n"
        "v_exp_f32 v86, v86\n"
        "v_exp_f32 v87, v87\n"
        "v_cvt_pk_bf16_f32 v106, v84, v85\n"
        "v_mfma_f32_32x32x16_bf16 v[64:79], v[124:127], a[172:175], v[64:79]\n"
        "buffer_load_dwordx4 v11, s[44:47], s65 offen offset:3072 sc1 lds\n"
        "v_exp_f32 v88, v88\n"
        "v_exp_f32 v89, v89\n"
        "v_cvt_pk_bf16_f32 v107, v86, v87\n"
        "v_mfma_f32_32x32x16_bf16 a[96:111], v[96:99], v[160:163], a[96:111]\n"
        "v_exp_f32 v90, v90\n"
        "v_exp_f32 v91, v91\n"
        "v_cvt_pk_bf16_f32 v108, v88, v89\n"
        "v_sub_u32_sdwa v208, s63, v176 dst_sel:DWORD dst_unused:UNUSED_PAD src0_sel:DWORD src1_sel:WORD_0\n"
        "v_mfma_f32_32x32x16_bf16 a[112:127], v[96:99], v[164:167], a[112:127]\n"
        "s_add_u32 s65, s65, s66\n"
        "ds_add_rtn_u32 v216, v208, v61\n"
        "v_exp_f32 v92, v92\n"
        "v_exp_f32 v93, v93\n"
        "v_cvt_pk_bf16_f32 v109, v90, v91\n"
        "v_mfma_f32_32x32x16_bf16 a[96:111], v[100:103], v[168:171], a[96:111]\n"
        "v_exp_f32 v94, v94\n"
        "v_exp_f32 v95, v95\n"
        "v_cvt_pk_bf16_f32 v110, v92, v93\n"
        "v_sub_u32_sdwa v209, s63, v177 dst_sel:DWORD dst_unused:UNUSED_PAD src0_sel:DWORD src1_sel:WORD_0\n"
        "v_mfma_f32_32x32x16_bf16 a[112:127], v[100:103], v[172:175], a[112:127]\n"
        "ds_add_rtn_u32 v217, v209, v61\n"
        "v_exp_f32 v64, v64\n"
        "v_exp_f32 v65, v65\n"
        "v_cvt_pk_bf16_f32 v111, v94, v95\n"
        "v_mfma_f32_32x32x16_bf16 v[80:95], v[112:115], a[176:179], 0\n"
        "v_exp_f32 v66, v66\n"
        "v_exp_f32 v67, v67\n"
        "v_cvt_pk_bf16_f32 v96, v64, v65\n"
        "v_sub_u32_sdwa v210, s63, v178 dst_sel:DWORD dst_unused:UNUSED_PAD src0_sel:DWORD src1_sel:WORD_0\n"
        "v_mfma_f32_32x32x16_bf16 v[80:95], v[116:119], a[180:183], v[80:95]\n"
        "ds_add_rtn_u32 v218, v210, v61\n"
        "v_exp_f32 v68, v68\n"
        "v_exp_f32 v69, v69\n"
        "v_cvt_pk_bf16_f32 v97, v66, v67\n"
        "v_mfma_f32_32x32x16_bf16 v[80:95], v[120:123], a[184:187], v[80:95]\n"
        "v_exp_f32 v70, v70\n"
        "v_exp_f32 v71, v71\n"
        "v_cvt_pk_bf16_f32 v98, v68, v69\n"
        "v_sub_u32_sdwa v211, s63, v179 dst_sel:DWORD dst_unused:UNUSED_PAD src0_sel:DWORD src1_sel:WORD_0\n"
        "v_mfma_f32_32x32x16_bf16 v[80:95], v[124:127], a[188:191], v[80:95]\n"
        "ds_add_rtn_u32 v219, v211, v61\n"
        "v_exp_f32 v72, v72\n"
        "v_exp_f32 v73, v73\n"
        "v_cvt_pk_bf16_f32 v99, v70, v71\n"
        "v_mfma_f32_32x32x16_bf16 a[128:143], v[104:107], v[160:163], a[128:143]\n"
        "s_waitcnt lgkmcnt(3)\n"
        "ds_write_b16_d16_hi v216, v176 offset:16384\n"
        "v_exp_f32 v74, v74\n"
        "v_exp_f32 v75, v75\n"
        "v_cvt_pk_bf16_f32 v100, v72, v73\n"
        "v_mfma_f32_32x32x16_bf16 a[144:159], v[104:107], v[164:167], a[144:159]\n"
        "s_waitcnt vmcnt(14)\n"
        "v_exp_f32 v76, v76\n"
        "v_exp_f32 v77, v77\n"
        "v_cvt_pk_bf16_f32 v101, v74, v75\n"
        "v_sub_u32_sdwa v212, s63, v180 dst_sel:DWORD dst_unused:UNUSED_PAD src0_sel:DWORD src1_sel:WORD_0\n"
        "v_mfma_f32_32x32x16_bf16 a[128:143], v[108:111], v[168:171], a[128:143]\n"
        "ds_add_rtn_u32 v220, v212, v61\n"
        "v_exp_f32 v78, v78\n"
        "v_exp_f32 v79, v79\n"
        "v_cvt_pk_bf16_f32 v102, v76, v77\n"
        "v_mfma_f32_32x32x16_bf16 a[144:159], v[108:111], v[172:175], a[144:159]\n"
        "s_waitcnt lgkmcnt(4)\n"
        "ds_write_b16_d16_hi v217, v177 offset:16384\n"
        "v_exp_f32 v80, v80\n"
        "v_exp_f32 v81, v81\n"
        "v_cvt_pk_bf16_f32 v103, v78, v79\n"
        "v_mfma_f32_32x32x16_bf16 v[64:79], v[112:115], a[192:195], 0\n"
        "ds_read_b128 v[128:131], v12 offset:4096\n"
        "v_exp_f32 v82, v82\n"
        "v_exp_f32 v83, v83\n"
        "v_cvt_pk_bf16_f32 v104, v80, v81\n"
        "v_mfma_f32_32x32x16_bf16 v[64:79], v[116:119], a[196:199], v[64:79]\n"
        "ds_read_b128 v[132:135], v13 offset:4096\n"
        "v_exp_f32 v84, v84\n"
        "v_exp_f32 v85, v85\n"
        "v_cvt_pk_bf16_f32 v105, v82, v83\n"
        "v_mfma_f32_32x32x16_bf16 v[64:79], v[120:123], a[200:203], v[64:79]\n"
        "ds_read_b128 v[136:139], v14 offset:4096\n"
        "v_exp_f32 v86, v86\n"
        "v_exp_f32 v87, v87\n"
        "v_cvt_pk_bf16_f32 v106, v84, v85\n"
        "v_mfma_f32_32x32x16_bf16 v[64:79], v[124:127], a[204:207], v[64:79]\n"
        "ds_read_b128 v[140:143], v15 offset:4096\n"
        "v_exp_f32 v88, v88\n"
        "v_exp_f32 v89, v89\n"
        "v_cvt_pk_bf16_f32 v107, v86, v87\n"
        "v_mfma_f32_32x32x16_bf16 a[0:15], v[96:99], v[144:147], a[0:15]\n"
        "ds_read_b64_tr_b16 v[160:161], v16 offset:4096\n"
        "v_exp_f32 v90, v90\n"
        "v_exp_f32 v91, v91\n"
        "v_cvt_pk_bf16_f32 v108, v88, v89\n"
        "v_mfma_f32_32x32x16_bf16 a[16:31], v[96:99], v[148:151], a[16:31]\n"
        "ds_read_b64_tr_b16 v[162:163], v17 offset:4096\n"
        "v_exp_f32 v92, v92\n"
        "v_exp_f32 v93, v93\n"
        "v_cvt_pk_bf16_f32 v109, v90, v91\n"
        "v_mfma_f32_32x32x16_bf16 a[0:15], v[100:103], v[152:155], a[0:15]\n"
        "ds_read_b64_tr_b16 v[164:165], v18 offset:4096\n"
        "v_exp_f32 v94, v94\n"
        "v_exp_f32 v95, v95\n"
        "v_cvt_pk_bf16_f32 v110, v92, v93\n"
        "v_mfma_f32_32x32x16_bf16 a[16:31], v[100:103], v[156:159], a[16:31]\n"
        "ds_read_b64_tr_b16 v[166:167], v19 offset:4096\n"
        "v_exp_f32 v64, v64\n"
        "v_exp_f32 v65, v65\n"
        "v_cvt_pk_bf16_f32 v111, v94, v95\n"
        "v_mfma_f32_32x32x16_bf16 v[80:95], v[112:115], a[208:211], 0\n"
        "ds_read_b64_tr_b16 v[168:169], v16 offset:6144\n"
        "v_exp_f32 v66, v66\n"
        "v_exp_f32 v67, v67\n"
        "v_cvt_pk_bf16_f32 v96, v64, v65\n"
        "v_mfma_f32_32x32x16_bf16 v[80:95], v[116:119], a[212:215], v[80:95]\n"
        "ds_read_b64_tr_b16 v[170:171], v17 offset:6144\n"
        "v_exp_f32 v68, v68\n"
        "v_exp_f32 v69, v69\n"
        "v_cvt_pk_bf16_f32 v97, v66, v67\n"
        "v_mfma_f32_32x32x16_bf16 v[80:95], v[120:123], a[216:219], v[80:95]\n"
        "ds_read_b64_tr_b16 v[172:173], v18 offset:6144\n"
        "v_exp_f32 v70, v70\n"
        "v_exp_f32 v71, v71\n"
        "v_cvt_pk_bf16_f32 v98, v68, v69\n"
        "v_mfma_f32_32x32x16_bf16 v[80:95], v[124:127], a[220:223], v[80:95]\n"
        "ds_read_b64_tr_b16 v[174:175], v19 offset:6144\n"
        "v_exp_f32 v72, v72\n"
        "v_exp_f32 v73, v73\n"
        "v_cvt_pk_bf16_f32 v99, v70, v71\n"
        "v_mfma_f32_32x32x16_bf16 a[32:47], v[104:107], v[144:147], a[32:47]\n"
        "v_exp_f32 v74, v74\n"
        "v_exp_f32 v75, v75\n"
        "v_cvt_pk_bf16_f32 v100, v72, v73\n"
        "v_sub_u32_sdwa v213, s63, v181 dst_sel:DWORD dst_unused:UNUSED_PAD src0_sel:DWORD src1_sel:WORD_0\n"
        "v_mfma_f32_32x32x16_bf16 a[48:63], v[104:107], v[148:151], a[48:63]\n"
        "ds_add_rtn_u32 v221, v213, v61\n"
        "v_exp_f32 v76, v76\n"
        "v_exp_f32 v77, v77\n"
        "v_cvt_pk_bf16_f32 v101, v74, v75\n"
        "v_mfma_f32_32x32x16_bf16 a[32:47], v[108:111], v[152:155], a[32:47]\n"
        "s_waitcnt lgkmcnt(14)\n"
        "ds_write_b16_d16_hi v218, v178 offset:16384\n"
        "v_exp_f32 v78, v78\n"
        "v_exp_f32 v79, v79\n"
        "v_cvt_pk_bf16_f32 v102, v76, v77\n"
        "v_mfma_f32_32x32x16_bf16 a[48:63], v[108:111], v[156:159], a[48:63]\n"
        "v_exp_f32 v80, v80\n"
        "v_exp_f32 v81, v81\n"
        "v_cvt_pk_bf16_f32 v103, v78, v79\n"
        "v_sub_u32_sdwa v214, s63, v182 dst_sel:DWORD dst_unused:UNUSED_PAD src0_sel:DWORD src1_sel:WORD_0\n"
        "v_mfma_f32_32x32x16_bf16 v[64:79], v[112:115], a[224:227], 0\n"
        "ds_add_rtn_u32 v222, v214, v61\n"
        "v_exp_f32 v82, v82\n"
        "v_exp_f32 v83, v83\n"
        "v_cvt_pk_bf16_f32 v104, v80, v81\n"
        "v_mfma_f32_32x32x16_bf16 v[64:79], v[116:119], a[228:231], v[64:79]\n"
        "ds_write_b16_d16_hi v219, v179 offset:16384\n"
        "v_exp_f32 v84, v84\n"
        "v_exp_f32 v85, v85\n"
        "v_cvt_pk_bf16_f32 v105, v82, v83\n"
        "v_mfma_f32_32x32x16_bf16 v[64:79], v[120:123], a[232:235], v[64:79]\n"
        "v_exp_f32 v86, v86\n"
        "v_exp_f32 v87, v87\n"
        "v_cvt_pk_bf16_f32 v106, v84, v85\n"
        "v_sub_u32_sdwa v215, s63, v183 dst_sel:DWORD dst_unused:UNUSED_PAD src0_sel:DWORD src1_sel:WORD_0\n"
        "v_mfma_f32_32x32x16_bf16 v[64:79], v[124:127], a[236:239], v[64:79]\n"
        "ds_add_rtn_u32 v223, v215, v61\n"
        "v_exp_f32 v88, v88\n"
        "v_exp_f32 v89, v89\n"
        "v_cvt_pk_bf16_f32 v107, v86, v87\n"
        "v_mfma_f32_32x32x16_bf16 a[64:79], v[96:99], v[144:147], a[64:79]\n"
        "ds_write_b16_d16_hi v220, v180 offset:16384\n"
        "v_exp_f32 v90, v90\n"
        "v_exp_f32 v91, v91\n"
        "v_cvt_pk_bf16_f32 v108, v88, v89\n"
        "v_mfma_f32_32x32x16_bf16 a[80:95], v[96:99], v[148:151], a[80:95]\n"
        "s_waitcnt vmcnt(13)\n"
        "v_exp_f32 v92, v92\n"
        "v_exp_f32 v93, v93\n"
        "v_cvt_pk_bf16_f32 v109, v90, v91\n"
        "v_sub_u32_sdwa v208, s63, v184 dst_sel:DWORD dst_unused:UNUSED_PAD src0_sel:DWORD src1_sel:WORD_0\n"
        "v_mfma_f32_32x32x16_bf16 a[64:79], v[100:103], v[152:155], a[64:79]\n"
        "s_waitcnt lgkmcnt(6)\n"
        "ds_add_rtn_u32 v216, v208, v61\n"
        "v_exp_f32 v94, v94\n"
        "v_exp_f32 v95, v95\n"
        "v_cvt_pk_bf16_f32 v110, v92, v93\n"
        "v_mfma_f32_32x32x16_bf16 a[80:95], v[100:103], v[156:159], a[80:95]\n"
        "s_add_u32 m0, s62, 4096\n"
        "v_exp_f32 v64, v64\n"
        "v_exp_f32 v65, v65\n"
        "v_cvt_pk_bf16_f32 v111, v94, v95\n"
        "v_lshl_add_u32 v232, v234, 3, v232\n"
        "v_mfma_f32_32x32x16_bf16 v[80:95], v[128:131], a[160:163], 0\n"
        "s_waitcnt lgkmcnt(6)\n"
        "buffer_load_dwordx4 v10, s[44:47], s65 offen offset:0 sc1 lds\n"
        "v_exp_f32 v66, v66\n"
        "v_exp_f32 v67, v67\n"
        "v_cvt_pk_bf16_f32 v96, v64, v65\n"
        "v_mfma_f32_32x32x16_bf16 v[80:95], v[132:135], a[164:167], v[80:95]\n"
        "s_lshl_b32 s77, s59, 4\n"
        "buffer_load_dwordx4 v11, s[44:47], s65 offen offset:1024 sc1 lds\n"
        "v_exp_f32 v68, v68\n"
        "v_exp_f32 v69, v69\n"
        "v_cvt_pk_bf16_f32 v97, v66, v67\n"
        "v_mfma_f32_32x32x16_bf16 v[80:95], v[136:139], a[168:171], v[80:95]\n"
        "s_add_u32 s77, s77, 20480\n"
        "buffer_load_dwordx4 v10, s[44:47], s65 offen offset:2048 sc1 lds\n"
        "v_exp_f32 v70, v70\n"
        "v_exp_f32 v71, v71\n"
        "v_cvt_pk_bf16_f32 v98, v68, v69\n"
        "v_mfma_f32_32x32x16_bf16 v[80:95], v[140:143], a[172:175], v[80:95]\n"
        "buffer_load_dwordx4 v11, s[44:47], s65 offen offset:3072 sc1 lds\n"
        "v_exp_f32 v72, v72\n"
        "v_exp_f32 v73, v73\n"
        "v_cvt_pk_bf16_f32 v99, v70, v71\n"
        "v_mfma_f32_32x32x16_bf16 a[96:111], v[104:107], v[144:147], a[96:111]\n"
        "ds_write_b16_d16_hi v221, v181 offset:16384\n"
        "v_exp_f32 v74, v74\n"
        "v_exp_f32 v75, v75\n"
        "v_cvt_pk_bf16_f32 v100, v72, v73\n"
        "v_mfma_f32_32x32x16_bf16 a[112:127], v[104:107], v[148:151], a[112:127]\n"
        "s_add_u32 s65, s65, s66\n"
        "v_exp_f32 v76, v76\n"
        "v_exp_f32 v77, v77\n"
        "v_cvt_pk_bf16_f32 v101, v74, v75\n"
        "v_sub_u32_sdwa v209, s63, v185 dst_sel:DWORD dst_unused:UNUSED_PAD src0_sel:DWORD src1_sel:WORD_0\n"
        "v_mfma_f32_32x32x16_bf16 a[96:111], v[108:111], v[152:155], a[96:111]\n"
        "ds_add_rtn_u32 v217, v209, v61\n"
        "v_exp_f32 v78, v78\n"
        "v_exp_f32 v79, v79\n"
        "v_cvt_pk_bf16_f32 v102, v76, v77\n"
        "v_mfma_f32_32x32x16_bf16 a[112:127], v[108:111], v[156:159], a[112:127]\n"
        "s_waitcnt lgkmcnt(6)\n"
        "ds_write_b16_d16_hi v222, v182 offset:16384\n"
        "v_exp_f32 v80, v80\n"
        "v_exp_f32 v81, v81\n"
        "v_cvt_pk_bf16_f32 v103, v78, v79\n"
        "v_mfma_f32_32x32x16_bf16 v[64:79], v[128:131], a[176:179], 0\n"
        "v_exp_f32 v82, v82\n"
        "v_exp_f32 v83, v83\n"
        "v_cvt_pk_bf16_f32 v104, v80, v81\n"
        "v_sub_u32_sdwa v210, s63, v186 dst_sel:DWORD dst_unused:UNUSED_PAD src0_sel:DWORD src1_sel:WORD_0\n"
        "v_mfma_f32_32x32x16_bf16 v[64:79], v[132:135], a[180:183], v[64:79]\n"
        "ds_add_rtn_u32 v218, v210, v61\n"
        "v_exp_f32 v84, v84\n"
        "v_exp_f32 v85, v85\n"
        "v_cvt_pk_bf16_f32 v105, v82, v83\n"
        "v_mfma_f32_32x32x16_bf16 v[64:79], v[136:139], a[184:187], v[64:79]\n"
        "s_waitcnt lgkmcnt(6)\n"
        "ds_write_b16_d16_hi v223, v183 offset:16384\n"
        "v_exp_f32 v86, v86\n"
        "v_exp_f32 v87, v87\n"
        "v_cvt_pk_bf16_f32 v106, v84, v85\n"
        "v_mfma_f32_32x32x16_bf16 v[64:79], v[140:143], a[188:191], v[64:79]\n"
        "v_exp_f32 v88, v88\n"
        "v_exp_f32 v89, v89\n"
        "v_cvt_pk_bf16_f32 v107, v86, v87\n"
        "v_sub_u32_sdwa v211, s63, v187 dst_sel:DWORD dst_unused:UNUSED_PAD src0_sel:DWORD src1_sel:WORD_0\n"
        "v_mfma_f32_32x32x16_bf16 a[128:143], v[96:99], v[144:147], a[128:143]\n"
        "ds_add_rtn_u32 v219, v211, v61\n"
        "v_exp_f32 v90, v90\n"
        "v_exp_f32 v91, v91\n"
        "v_cvt_pk_bf16_f32 v108, v88, v89\n"
        "v_mfma_f32_32x32x16_bf16 a[144:159], v[96:99], v[148:151], a[144:159]\n"
        "s_waitcnt lgkmcnt(6)\n"
        "ds_write_b16_d16_hi v216, v184 offset:16384\n"
        "v_exp_f32 v92, v92\n"
        "v_exp_f32 v93, v93\n"
        "v_cvt_pk_bf16_f32 v109, v90, v91\n"
        "v_mfma_f32_32x32x16_bf16 a[128:143], v[100:103], v[152:155], a[128:143]\n"
        "s_waitcnt vmcnt(12)\n"
        "v_exp_f32 v94, v94\n"
        "v_exp_f32 v95, v95\n"
        "v_cvt_pk_bf16_f32 v110, v92, v93\n"
        "v_sub_u32_sdwa v212, s63, v188 dst_sel:DWORD dst_unused:UNUSED_PAD src0_sel:DWORD src1_sel:WORD_0\n"
        "v_mfma_f32_32x32x16_bf16 a[144:159], v[100:103], v[156:159], a[144:159]\n"
        "ds_add_rtn_u32 v220, v212, v61\n"
        "v_exp_f32 v64, v64\n"
        "v_exp_f32 v65, v65\n"
        "v_cvt_pk_bf16_f32 v111, v94, v95\n"
        "v_mfma_f32_32x32x16_bf16 v[80:95], v[128:131], a[192:195], 0\n"
        "ds_read_b128 v[112:115], v12 offset:8192\n"
        "v_exp_f32 v66, v66\n"
        "v_exp_f32 v67, v67\n"
        "v_cvt_pk_bf16_f32 v96, v64, v65\n"
        "v_mfma_f32_32x32x16_bf16 v[80:95], v[132:135], a[196:199], v[80:95]\n"
        "s_waitcnt lgkmcnt(7)\n"
        "ds_read_b128 v[116:119], v13 offset:8192\n"
        "v_exp_f32 v68, v68\n"
        "v_exp_f32 v69, v69\n"
        "v_cvt_pk_bf16_f32 v97, v66, v67\n"
        "v_mfma_f32_32x32x16_bf16 v[80:95], v[136:139], a[200:203], v[80:95]\n"
        "ds_read_b128 v[120:123], v14 offset:8192\n"
        "v_exp_f32 v70, v70\n"
        "v_exp_f32 v71, v71\n"
        "v_cvt_pk_bf16_f32 v98, v68, v69\n"
        "v_mfma_f32_32x32x16_bf16 v[80:95], v[140:143], a[204:207], v[80:95]\n"
        "ds_read_b128 v[124:127], v15 offset:8192\n"
        "v_exp_f32 v72, v72\n"
        "v_exp_f32 v73, v73\n"
        "v_cvt_pk_bf16_f32 v99, v70, v71\n"
        "v_mfma_f32_32x32x16_bf16 a[0:15], v[104:107], v[160:163], a[0:15]\n"
        "ds_read_b64_tr_b16 v[144:145], v16 offset:8192\n"
        "v_exp_f32 v74, v74\n"
        "v_exp_f32 v75, v75\n"
        "v_cvt_pk_bf16_f32 v100, v72, v73\n"
        "v_mfma_f32_32x32x16_bf16 a[16:31], v[104:107], v[164:167], a[16:31]\n"
        "ds_read_b64_tr_b16 v[146:147], v17 offset:8192\n"
        "v_exp_f32 v76, v76\n"
        "v_exp_f32 v77, v77\n"
        "v_cvt_pk_bf16_f32 v101, v74, v75\n"
        "v_mfma_f32_32x32x16_bf16 a[0:15], v[108:111], v[168:171], a[0:15]\n"
        "ds_read_b64_tr_b16 v[148:149], v18 offset:8192\n"
        "v_exp_f32 v78, v78\n"
        "v_exp_f32 v79, v79\n"
        "v_cvt_pk_bf16_f32 v102, v76, v77\n"
        "v_mfma_f32_32x32x16_bf16 a[16:31], v[108:111], v[172:175], a[16:31]\n"
        "ds_read_b64_tr_b16 v[150:151], v19 offset:8192\n"
        "v_exp_f32 v80, v80\n"
        "v_exp_f32 v81, v81\n"
        "v_cvt_pk_bf16_f32 v103, v78, v79\n"
        "v_mfma_f32_32x32x16_bf16 v[64:79], v[128:131], a[208:211], 0\n"
        "ds_read_b64_tr_b16 v[152:153], v16 offset:10240\n"
        "v_exp_f32 v82, v82\n"
        "v_exp_f32 v83, v83\n"
        "v_cvt_pk_bf16_f32 v104, v80, v81\n"
        "v_mfma_f32_32x32x16_bf16 v[64:79], v[132:135], a[212:215], v[64:79]\n"
        "ds_read_b64_tr_b16 v[154:155], v17 offset:10240\n"
        "v_exp_f32 v84, v84\n"
        "v_exp_f32 v85, v85\n"
        "v_cvt_pk_bf16_f32 v105, v82, v83\n"
        "v_mfma_f32_32x32x16_bf16 v[64:79], v[136:139], a[216:219], v[64:79]\n"
        "ds_read_b64_tr_b16 v[156:157], v18 offset:10240\n"
        "v_exp_f32 v86, v86\n"
        "v_exp_f32 v87, v87\n"
        "v_cvt_pk_bf16_f32 v106, v84, v85\n"
        "v_mfma_f32_32x32x16_bf16 v[64:79], v[140:143], a[220:223], v[64:79]\n"
        "ds_read_b64_tr_b16 v[158:159], v19 offset:10240\n"
        "v_exp_f32 v88, v88\n"
        "v_exp_f32 v89, v89\n"
        "v_cvt_pk_bf16_f32 v107, v86, v87\n"
        "v_mfma_f32_32x32x16_bf16 a[32:47], v[96:99], v[160:163], a[32:47]\n"
        "ds_write_b16_d16_hi v217, v185 offset:16384\n"
        "v_exp_f32 v90, v90\n"
        "v_exp_f32 v91, v91\n"
        "v_cvt_pk_bf16_f32 v108, v88, v89\n"
        "v_mfma_f32_32x32x16_bf16 a[48:63], v[96:99], v[164:167], a[48:63]\n"
        "v_exp_f32 v92, v92\n"
        "v_exp_f32 v93, v93\n"
        "v_cvt_pk_bf16_f32 v109, v90, v91\n"
        "v_sub_u32_sdwa v213, s63, v189 dst_sel:DWORD dst_unused:UNUSED_PAD src0_sel:DWORD src1_sel:WORD_0\n"
        "v_mfma_f32_32x32x16_bf16 a[32:47], v[100:103], v[168:171], a[32:47]\n"
        "ds_add_rtn_u32 v221, v213, v61\n"
        "v_exp_f32 v94, v94\n"
        "v_exp_f32 v95, v95\n"
        "v_cvt_pk_bf16_f32 v110, v92, v93\n"
        "v_mfma_f32_32x32x16_bf16 a[48:63], v[100:103], v[172:175], a[48:63]\n"
        "s_waitcnt lgkmcnt(14)\n"
        "ds_write_b16_d16_hi v218, v186 offset:16384\n"
        "v_exp_f32 v64, v64\n"
        "v_exp_f32 v65, v65\n"
        "v_cvt_pk_bf16_f32 v111, v94, v95\n"
        "v_mfma_f32_32x32x16_bf16 v[80:95], v[128:131], a[224:227], 0\n"
        "v_exp_f32 v66, v66\n"
        "v_exp_f32 v67, v67\n"
        "v_cvt_pk_bf16_f32 v96, v64, v65\n"
        "v_sub_u32_sdwa v214, s63, v190 dst_sel:DWORD dst_unused:UNUSED_PAD src0_sel:DWORD src1_sel:WORD_0\n"
        "v_mfma_f32_32x32x16_bf16 v[80:95], v[132:135], a[228:231], v[80:95]\n"
        "ds_add_rtn_u32 v222, v214, v61\n"
        "v_exp_f32 v68, v68\n"
        "v_exp_f32 v69, v69\n"
        "v_cvt_pk_bf16_f32 v97, v66, v67\n"
        "v_mfma_f32_32x32x16_bf16 v[80:95], v[136:139], a[232:235], v[80:95]\n"
        "ds_write_b16_d16_hi v219, v187 offset:16384\n"
        "v_exp_f32 v70, v70\n"
        "v_exp_f32 v71, v71\n"
        "v_cvt_pk_bf16_f32 v98, v68, v69\n"
        "v_mfma_f32_32x32x16_bf16 v[80:95], v[140:143], a[236:239], v[80:95]\n"
        "v_exp_f32 v72, v72\n"
        "v_exp_f32 v73, v73\n"
        "v_cvt_pk_bf16_f32 v99, v70, v71\n"
        "v_sub_u32_sdwa v215, s63, v191 dst_sel:DWORD dst_unused:UNUSED_PAD src0_sel:DWORD src1_sel:WORD_0\n"
        "v_mfma_f32_32x32x16_bf16 a[64:79], v[104:107], v[160:163], a[64:79]\n"
        "ds_add_rtn_u32 v223, v215, v61\n"
        "v_exp_f32 v74, v74\n"
        "v_exp_f32 v75, v75\n"
        "v_cvt_pk_bf16_f32 v100, v72, v73\n"
        "v_mfma_f32_32x32x16_bf16 a[80:95], v[104:107], v[164:167], a[80:95]\n"
        "ds_write_b16_d16_hi v220, v188 offset:16384\n"
        "v_exp_f32 v76, v76\n"
        "v_exp_f32 v77, v77\n"
        "v_cvt_pk_bf16_f32 v101, v74, v75\n"
        "v_mfma_f32_32x32x16_bf16 a[64:79], v[108:111], v[168:171], a[64:79]\n"
        "s_waitcnt lgkmcnt(7)\n"
        "v_exp_f32 v78, v78\n"
        "v_exp_f32 v79, v79\n"
        "v_cvt_pk_bf16_f32 v102, v76, v77\n"
        "v_add_u32 v4, s77, v232\n"
        "v_mfma_f32_32x32x16_bf16 a[80:95], v[108:111], v[172:175], a[80:95]\n"
        "s_add_u32 m0, s62, 8192\n"
        "v_exp_f32 v80, v80\n"
        "v_exp_f32 v81, v81\n"
        "v_cvt_pk_bf16_f32 v103, v78, v79\n"
        "v_mfma_f32_32x32x16_bf16 v[64:79], v[112:115], a[160:163], 0\n"
        "s_waitcnt vmcnt(11)\n"
        "buffer_load_dwordx4 v10, s[44:47], s65 offen offset:0 sc1 lds\n"
        "v_exp_f32 v82, v82\n"
        "v_exp_f32 v83, v83\n"
        "v_cvt_pk_bf16_f32 v104, v80, v81\n"
        "v_mfma_f32_32x32x16_bf16 v[64:79], v[116:119], a[164:167], v[64:79]\n"
        "buffer_load_dwordx4 v11, s[44:47], s65 offen offset:1024 sc1 lds\n"
        "v_exp_f32 v84, v84\n"
        "v_exp_f32 v85, v85\n"
        "v_cvt_pk_bf16_f32 v105, v82, v83\n"
        "v_mfma_f32_32x32x16_bf16 v[64:79], v[120:123], a[168:171], v[64:79]\n"
        "buffer_load_dwordx4 v10, s[44:47], s65 offen offset:2048 sc1 lds\n"
        "v_exp_f32 v86, v86\n"
        "v_exp_f32 v87, v87\n"
        "v_cvt_pk_bf16_f32 v106, v84, v85\n"
        "v_mfma_f32_32x32x16_bf16 v[64:79], v[124:127], a[172:175], v[64:79]\n"
        "buffer_load_dwordx4 v11, s[44:47], s65 offen offset:3072 sc1 lds\n"
        "v_exp_f32 v88, v88\n"
        "v_exp_f32 v89, v89\n"
        "v_cvt_pk_bf16_f32 v107, v86, v87\n"
        "v_mfma_f32_32x32x16_bf16 a[96:111], v[96:99], v[160:163], a[96:111]\n"
        "s_cmp_eq_u32 s59, 0\n"
        "s_cselect_b32 s77, 0, s83\n"
        "v_exp_f32 v90, v90\n"
        "v_exp_f32 v91, v91\n"
        "v_cvt_pk_bf16_f32 v108, v88, v89\n"
        "v_sub_u32_sdwa v208, s63, v192 dst_sel:DWORD dst_unused:UNUSED_PAD src0_sel:DWORD src1_sel:WORD_0\n"
        "v_mfma_f32_32x32x16_bf16 a[112:127], v[96:99], v[164:167], a[112:127]\n"
        "s_add_u32 s65, s65, s66\n"
        "ds_add_rtn_u32 v216, v208, v61\n"
        "v_exp_f32 v92, v92\n"
        "v_exp_f32 v93, v93\n"
        "v_cvt_pk_bf16_f32 v109, v90, v91\n"
        "v_mfma_f32_32x32x16_bf16 a[96:111], v[100:103], v[168:171], a[96:111]\n"
        "s_waitcnt lgkmcnt(6)\n"
        "ds_write_b16_d16_hi v221, v189 offset:16384\n"
        "v_exp_f32 v94, v94\n"
        "v_exp_f32 v95, v95\n"
        "v_cvt_pk_bf16_f32 v110, v92, v93\n"
        "v_mfma_f32_32x32x16_bf16 a[112:127], v[100:103], v[172:175], a[112:127]\n"
        "v_exp_f32 v64, v64\n"
        "v_exp_f32 v65, v65\n"
        "v_cvt_pk_bf16_f32 v111, v94, v95\n"
        "v_sub_u32_sdwa v209, s63, v193 dst_sel:DWORD dst_unused:UNUSED_PAD src0_sel:DWORD src1_sel:WORD_0\n"
        "v_mfma_f32_32x32x16_bf16 v[80:95], v[112:115], a[176:179], 0\n"
        "ds_add_rtn_u32 v217, v209, v61\n"
        "v_exp_f32 v66, v66\n"
        "v_exp_f32 v67, v67\n"
        "v_cvt_pk_bf16_f32 v96, v64, v65\n"
        "v_mfma_f32_32x32x16_bf16 v[80:95], v[116:119], a[180:183], v[80:95]\n"
        "s_waitcnt lgkmcnt(6)\n"
        "ds_write_b16_d16_hi v222, v190 offset:16384\n"
        "v_exp_f32 v68, v68\n"
        "v_exp_f32 v69, v69\n"
        "v_cvt_pk_bf16_f32 v97, v66, v67\n"
        "v_mfma_f32_32x32x16_bf16 v[80:95], v[120:123], a[184:187], v[80:95]\n"
        "v_exp_f32 v70, v70\n"
        "v_exp_f32 v71, v71\n"
        "v_cvt_pk_bf16_f32 v98, v68, v69\n"
        "v_sub_u32_sdwa v210, s63, v194 dst_sel:DWORD dst_unused:UNUSED_PAD src0_sel:DWORD src1_sel:WORD_0\n"
        "v_mfma_f32_32x32x16_bf16 v[80:95], v[124:127], a[188:191], v[80:95]\n"
        "ds_add_rtn_u32 v218, v210, v61\n"
        "v_exp_f32 v72, v72\n"
        "v_exp_f32 v73, v73\n"
        "v_cvt_pk_bf16_f32 v99, v70, v71\n"
        "v_mfma_f32_32x32x16_bf16 a[128:143], v[104:107], v[160:163], a[128:143]\n"
        "s_waitcnt lgkmcnt(6)\n"
        "ds_write_b16_d16_hi v223, v191 offset:16384\n"
        "v_exp_f32 v74, v74\n"
        "v_exp_f32 v75, v75\n"
        "v_cvt_pk_bf16_f32 v100, v72, v73\n"
        "v_mfma_f32_32x32x16_bf16 a[144:159], v[104:107], v[164:167], a[144:159]\n"
        "v_exp_f32 v76, v76\n"
        "v_exp_f32 v77, v77\n"
        "v_cvt_pk_bf16_f32 v101, v74, v75\n"
        "v_sub_u32_sdwa v211, s63, v195 dst_sel:DWORD dst_unused:UNUSED_PAD src0_sel:DWORD src1_sel:WORD_0\n"
        "v_mfma_f32_32x32x16_bf16 a[128:143], v[108:111], v[168:171], a[128:143]\n"
        "ds_add_rtn_u32 v219, v211, v61\n"
        "v_exp_f32 v78, v78\n"
        "v_exp_f32 v79, v79\n"
        "v_cvt_pk_bf16_f32 v102, v76, v77\n"
        "v_mfma_f32_32x32x16_bf16 a[144:159], v[108:111], v[172:175], a[144:159]\n"
        "s_waitcnt lgkmcnt(6)\n"
        "ds_write_b16_d16_hi v216, v192 offset:16384\n"
        "v_exp_f32 v80, v80\n"
        "v_exp_f32 v81, v81\n"
        "v_cvt_pk_bf16_f32 v103, v78, v79\n"
        "v_mfma_f32_32x32x16_bf16 v[64:79], v[112:115], a[192:195], 0\n"
        "ds_read_b128 v[128:131], v12 offset:12288\n"
        "v_exp_f32 v82, v82\n"
        "v_exp_f32 v83, v83\n"
        "v_cvt_pk_bf16_f32 v104, v80, v81\n"
        "v_mfma_f32_32x32x16_bf16 v[64:79], v[116:119], a[196:199], v[64:79]\n"
        "s_waitcnt vmcnt(12)\n"
        "ds_read_b128 v[132:135], v13 offset:12288\n"
        "v_exp_f32 v84, v84\n"
        "v_exp_f32 v85, v85\n"
        "v_cvt_pk_bf16_f32 v105, v82, v83\n"
        "v_mfma_f32_32x32x16_bf16 v[64:79], v[120:123], a[200:203], v[64:79]\n"
        "ds_read_b128 v[136:139], v14 offset:12288\n"
        "v_exp_f32 v86, v86\n"
        "v_exp_f32 v87, v87\n"
        "v_cvt_pk_bf16_f32 v106, v84, v85\n"
        "v_mfma_f32_32x32x16_bf16 v[64:79], v[124:127], a[204:207], v[64:79]\n"
        "ds_read_b128 v[140:143], v15 offset:12288\n"
        "v_exp_f32 v88, v88\n"
        "v_exp_f32 v89, v89\n"
        "v_cvt_pk_bf16_f32 v107, v86, v87\n"
        "v_mfma_f32_32x32x16_bf16 a[0:15], v[96:99], v[144:147], a[0:15]\n"
        "ds_read_b64_tr_b16 v[160:161], v16 offset:12288\n"
        "v_exp_f32 v90, v90\n"
        "v_exp_f32 v91, v91\n"
        "v_cvt_pk_bf16_f32 v108, v88, v89\n"
        "v_mfma_f32_32x32x16_bf16 a[16:31], v[96:99], v[148:151], a[16:31]\n"
        "ds_read_b64_tr_b16 v[162:163], v17 offset:12288\n"
        "v_exp_f32 v92, v92\n"
        "v_exp_f32 v93, v93\n"
        "v_cvt_pk_bf16_f32 v109, v90, v91\n"
        "v_mfma_f32_32x32x16_bf16 a[0:15], v[100:103], v[152:155], a[0:15]\n"
        "ds_read_b64_tr_b16 v[164:165], v18 offset:12288\n"
        "v_exp_f32 v94, v94\n"
        "v_exp_f32 v95, v95\n"
        "v_cvt_pk_bf16_f32 v110, v92, v93\n"
        "v_mfma_f32_32x32x16_bf16 a[16:31], v[100:103], v[156:159], a[16:31]\n"
        "ds_read_b64_tr_b16 v[166:167], v19 offset:12288\n"
        "v_exp_f32 v64, v64\n"
        "v_exp_f32 v65, v65\n"
        "v_cvt_pk_bf16_f32 v111, v94, v95\n"
        "v_mfma_f32_32x32x16_bf16 v[80:95], v[112:115], a[208:211], 0\n"
        "ds_read_b64_tr_b16 v[168:169], v16 offset:14336\n"
        "v_exp_f32 v66, v66\n"
        "v_exp_f32 v67, v67\n"
        "v_cvt_pk_bf16_f32 v96, v64, v65\n"
        "v_mfma_f32_32x32x16_bf16 v[80:95], v[116:119], a[212:215], v[80:95]\n"
        "ds_read_b64_tr_b16 v[170:171], v17 offset:14336\n"
        "v_exp_f32 v68, v68\n"
        "v_exp_f32 v69, v69\n"
        "v_cvt_pk_bf16_f32 v97, v66, v67\n"
        "v_mfma_f32_32x32x16_bf16 v[80:95], v[120:123], a[216:219], v[80:95]\n"
        "ds_read_b64_tr_b16 v[172:173], v18 offset:14336\n"
        "v_exp_f32 v70, v70\n"
        "v_exp_f32 v71, v71\n"
        "v_cvt_pk_bf16_f32 v98, v68, v69\n"
        "v_mfma_f32_32x32x16_bf16 v[80:95], v[124:127], a[220:223], v[80:95]\n"
        "ds_read_b64_tr_b16 v[174:175], v19 offset:14336\n"
        "v_exp_f32 v72, v72\n"
        "v_exp_f32 v73, v73\n"
        "v_cvt_pk_bf16_f32 v99, v70, v71\n"
        "v_mfma_f32_32x32x16_bf16 a[32:47], v[104:107], v[144:147], a[32:47]\n"
        "v_exp_f32 v74, v74\n"
        "v_exp_f32 v75, v75\n"
        "v_cvt_pk_bf16_f32 v100, v72, v73\n"
        "v_sub_u32_sdwa v212, s63, v196 dst_sel:DWORD dst_unused:UNUSED_PAD src0_sel:DWORD src1_sel:WORD_0\n"
        "v_mfma_f32_32x32x16_bf16 a[48:63], v[104:107], v[148:151], a[48:63]\n"
        "ds_add_rtn_u32 v220, v212, v61\n"
        "v_exp_f32 v76, v76\n"
        "v_exp_f32 v77, v77\n"
        "v_cvt_pk_bf16_f32 v101, v74, v75\n"
        "v_mfma_f32_32x32x16_bf16 a[32:47], v[108:111], v[152:155], a[32:47]\n"
        "s_waitcnt lgkmcnt(14)\n"
        "ds_write_b16_d16_hi v217, v193 offset:16384\n"
        "v_exp_f32 v78, v78\n"
        "v_exp_f32 v79, v79\n"
        "v_cvt_pk_bf16_f32 v102, v76, v77\n"
        "v_mfma_f32_32x32x16_bf16 a[48:63], v[108:111], v[156:159], a[48:63]\n"
        "v_exp_f32 v80, v80\n"
        "v_exp_f32 v81, v81\n"
        "v_cvt_pk_bf16_f32 v103, v78, v79\n"
        "v_sub_u32_sdwa v213, s63, v197 dst_sel:DWORD dst_unused:UNUSED_PAD src0_sel:DWORD src1_sel:WORD_0\n"
        "v_mfma_f32_32x32x16_bf16 v[64:79], v[112:115], a[224:227], 0\n"
        "ds_add_rtn_u32 v221, v213, v61\n"
        "v_exp_f32 v82, v82\n"
        "v_exp_f32 v83, v83\n"
        "v_cvt_pk_bf16_f32 v104, v80, v81\n"
        "v_mfma_f32_32x32x16_bf16 v[64:79], v[116:119], a[228:231], v[64:79]\n"
        "ds_write_b16_d16_hi v218, v194 offset:16384\n"
        "v_exp_f32 v84, v84\n"
        "v_exp_f32 v85, v85\n"
        "v_cvt_pk_bf16_f32 v105, v82, v83\n"
        "v_mfma_f32_32x32x16_bf16 v[64:79], v[120:123], a[232:235], v[64:79]\n"
        "v_exp_f32 v86, v86\n"
        "v_exp_f32 v87, v87\n"
        "v_cvt_pk_bf16_f32 v106, v84, v85\n"
        "v_sub_u32_sdwa v214, s63, v198 dst_sel:DWORD dst_unused:UNUSED_PAD src0_sel:DWORD src1_sel:WORD_0\n"
        "v_mfma_f32_32x32x16_bf16 v[64:79], v[124:127], a[236:239], v[64:79]\n"
        "ds_add_rtn_u32 v222, v214, v61\n"
        "v_exp_f32 v88, v88\n"
        "v_exp_f32 v89, v89\n"
        "v_cvt_pk_bf16_f32 v107, v86, v87\n"
        "v_mfma_f32_32x32x16_bf16 a[64:79], v[96:99], v[144:147], a[64:79]\n"
        "ds_write_b16_d16_hi v219, v195 offset:16384\n"
        "v_exp_f32 v90, v90\n"
        "v_exp_f32 v91, v91\n"
        "v_cvt_pk_bf16_f32 v108, v88, v89\n"
        "v_mfma_f32_32x32x16_bf16 a[80:95], v[96:99], v[148:151], a[80:95]\n"
        "v_exp_f32 v92, v92\n"
        "v_exp_f32 v93, v93\n"
        "v_cvt_pk_bf16_f32 v109, v90, v91\n"
        "v_sub_u32_sdwa v215, s63, v199 dst_sel:DWORD dst_unused:UNUSED_PAD src0_sel:DWORD src1_sel:WORD_0\n"
        "v_mfma_f32_32x32x16_bf16 a[64:79], v[100:103], v[152:155], a[64:79]\n"
        "s_waitcnt lgkmcnt(6)\n"
        "ds_add_rtn_u32 v223, v215, v61\n"
        "v_exp_f32 v94, v94\n"
        "v_exp_f32 v95, v95\n"
        "v_cvt_pk_bf16_f32 v110, v92, v93\n"
        "v_mfma_f32_32x32x16_bf16 a[80:95], v[100:103], v[156:159], a[80:95]\n"
        "s_add_u32 m0, s62, 12288\n"
        "v_exp_f32 v64, v64\n"
        "v_exp_f32 v65, v65\n"
        "v_cvt_pk_bf16_f32 v111, v94, v95\n"
        "v_or_b32 v2, s77, v4\n"
        "v_mfma_f32_32x32x16_bf16 v[80:95], v[128:131], a[160:163], 0\n"
        "s_waitcnt lgkmcnt(6)\n"
        "buffer_load_dwordx4 v10, s[44:47], s65 offen offset:0 sc1 lds\n"
        "v_exp_f32 v66, v66\n"
        "v_exp_f32 v67, v67\n"
        "v_cvt_pk_bf16_f32 v96, v64, v65\n"
        "v_mfma_f32_32x32x16_bf16 v[80:95], v[132:135], a[164:167], v[80:95]\n"
        "buffer_load_dwordx4 v11, s[44:47], s65 offen offset:1024 sc1 lds\n"
        "v_exp_f32 v68, v68\n"
        "v_exp_f32 v69, v69\n"
        "v_cvt_pk_bf16_f32 v97, v66, v67\n"
        "v_mfma_f32_32x32x16_bf16 v[80:95], v[136:139], a[168:171], v[80:95]\n"
        "buffer_load_dwordx4 v10, s[44:47], s65 offen offset:2048 sc1 lds\n"
        "v_exp_f32 v70, v70\n"
        "v_exp_f32 v71, v71\n"
        "v_cvt_pk_bf16_f32 v98, v68, v69\n"
        "v_mfma_f32_32x32x16_bf16 v[80:95], v[140:143], a[172:175], v[80:95]\n"
        "buffer_load_dwordx4 v11, s[44:47], s65 offen offset:3072 sc1 lds\n"
        "v_exp_f32 v72, v72\n"
        "v_exp_f32 v73, v73\n"
        "v_cvt_pk_bf16_f32 v99, v70, v71\n"
        "v_mfma_f32_32x32x16_bf16 a[96:111], v[104:107], v[144:147], a[96:111]\n"
        "ds_write_b16_d16_hi v220, v196 offset:16384\n"
        "v_exp_f32 v74, v74\n"
        "v_exp_f32 v75, v75\n"
        "v_cvt_pk_bf16_f32 v100, v72, v73\n"
        "v_mfma_f32_32x32x16_bf16 a[112:127], v[104:107], v[148:151], a[112:127]\n"
        "s_add_u32 s65, s65, s66\n"
        "v_exp_f32 v76, v76\n"
        "v_exp_f32 v77, v77\n"
        "v_cvt_pk_bf16_f32 v101, v74, v75\n"
        "v_lshrrev_b32 v232, 2, v9\n"
        "v_mfma_f32_32x32x16_bf16 a[96:111], v[108:111], v[152:155], a[96:111]\n"
        "s_waitcnt lgkmcnt(5)\n"
        "ds_write_b16_d16_hi v221, v197 offset:16384\n"
        "v_exp_f32 v78, v78\n"
        "v_exp_f32 v79, v79\n"
        "v_cvt_pk_bf16_f32 v102, v76, v77\n"
        "v_mfma_f32_32x32x16_bf16 a[112:127], v[108:111], v[156:159], a[112:127]\n"
        "s_waitcnt lgkmcnt(4)\n"
        "ds_write_b16_d16_hi v222, v198 offset:16384\n"
        "v_exp_f32 v80, v80\n"
        "v_exp_f32 v81, v81\n"
        "v_cvt_pk_bf16_f32 v103, v78, v79\n"
        "v_mfma_f32_32x32x16_bf16 v[64:79], v[128:131], a[176:179], 0\n"
        "s_waitcnt lgkmcnt(3)\n"
        "ds_write_b16_d16_hi v223, v199 offset:16384\n"
        "v_exp_f32 v82, v82\n"
        "v_exp_f32 v83, v83\n"
        "v_cvt_pk_bf16_f32 v104, v80, v81\n"
        "v_mfma_f32_32x32x16_bf16 v[64:79], v[132:135], a[180:183], v[64:79]\n"
        "v_exp_f32 v84, v84\n"
        "v_exp_f32 v85, v85\n"
        "v_cvt_pk_bf16_f32 v105, v82, v83\n"
        "v_cmp_lt_u32 vcc, 24, v62\n"
        "v_mfma_f32_32x32x16_bf16 v[64:79], v[136:139], a[184:187], v[64:79]\n"
        "s_cbranch_vccnz 105f\n"
        "106:\n"
        "v_exp_f32 v86, v86\n"
        "v_exp_f32 v87, v87\n"
        "v_cvt_pk_bf16_f32 v106, v84, v85\n"
        "v_cmp_lt_u32 vcc, 32, v62\n"
        "v_mfma_f32_32x32x16_bf16 v[64:79], v[140:143], a[188:191], v[64:79]\n"
        "v_exp_f32 v88, v88\n"
        "v_exp_f32 v89, v89\n"
        "v_cvt_pk_bf16_f32 v107, v86, v87\n"
        "v_mul_u32_u24 v232, 96, v232\n"
        "v_mfma_f32_32x32x16_bf16 a[128:143], v[96:99], v[144:147], a[128:143]\n"
        "v_exp_f32 v90, v90\n"
        "v_exp_f32 v91, v91\n"
        "v_cvt_pk_bf16_f32 v108, v88, v89\n"
        "v_cndmask_b32 v62, -16, v63, vcc\n"
        "v_mfma_f32_32x32x16_bf16 a[144:159], v[96:99], v[148:151], a[144:159]\n"
        "ds_write_b32 v62, v61 offset:4\n"
        "v_exp_f32 v92, v92\n"
        "v_exp_f32 v93, v93\n"
        "v_cvt_pk_bf16_f32 v109, v90, v91\n"
        "v_mfma_f32_32x32x16_bf16 a[128:143], v[100:103], v[152:155], a[128:143]\n"
        "v_exp_f32 v94, v94\n"
        "v_exp_f32 v95, v95\n"
        "v_cvt_pk_bf16_f32 v110, v92, v93\n"
        "v_lshl_add_u32 v232, v233, 3, v232\n"
        "v_mfma_f32_32x32x16_bf16 a[144:159], v[100:103], v[156:159], a[144:159]\n"
        "s_lshl1_add_u32 s77, s84, 20480\n"
        "v_exp_f32 v64, v64\n"
        "v_exp_f32 v65, v65\n"
        "v_cvt_pk_bf16_f32 v111, v94, v95\n"
        "v_add_u32 v5, s77, v232\n"
        "v_mfma_f32_32x32x16_bf16 v[80:95], v[128:131], a[192:195], 0\n"
        "s_waitcnt vmcnt(12)\n"
        "ds_read_b128 v[112:115], v12 offset:0\n"
        "v_exp_f32 v66, v66\n"
        "v_exp_f32 v67, v67\n"
        "v_cvt_pk_bf16_f32 v96, v64, v65\n"
        "v_mfma_f32_32x32x16_bf16 v[80:95], v[132:135], a[196:199], v[80:95]\n"
        "s_waitcnt lgkmcnt(1)\n"
        "ds_read_b128 v[116:119], v13 offset:0\n"
        "v_exp_f32 v68, v68\n"
        "v_exp_f32 v69, v69\n"
        "v_cvt_pk_bf16_f32 v97, v66, v67\n"
        "v_mfma_f32_32x32x16_bf16 v[80:95], v[136:139], a[200:203], v[80:95]\n"
        "s_barrier\n"
        "ds_read_b128 v[120:123], v14 offset:0\n"
        "v_exp_f32 v70, v70\n"
        "v_exp_f32 v71, v71\n"
        "v_cvt_pk_bf16_f32 v98, v68, v69\n"
        "v_mfma_f32_32x32x16_bf16 v[80:95], v[140:143], a[204:207], v[80:95]\n"
        "ds_read_b128 v[124:127], v15 offset:0\n"
        "v_exp_f32 v72, v72\n"
        "v_exp_f32 v73, v73\n"
        "v_cvt_pk_bf16_f32 v99, v70, v71\n"
        "v_mfma_f32_32x32x16_bf16 a[0:15], v[104:107], v[160:163], a[0:15]\n"
        "ds_read_b64_tr_b16 v[144:145], v16 offset:0\n"
        "v_exp_f32 v74, v74\n"
        "v_exp_f32 v75, v75\n"
        "v_cvt_pk_bf16_f32 v100, v72, v73\n"
        "v_mfma_f32_32x32x16_bf16 a[16:31], v[104:107], v[164:167], a[16:31]\n"
        "ds_read_b64_tr_b16 v[146:147], v17 offset:0\n"
        "v_exp_f32 v76, v76\n"
        "v_exp_f32 v77, v77\n"
        "v_cvt_pk_bf16_f32 v101, v74, v75\n"
        "v_mfma_f32_32x32x16_bf16 a[0:15], v[108:111], v[168:171], a[0:15]\n"
        "ds_read_b64_tr_b16 v[148:149], v18 offset:0\n"
        "v_exp_f32 v78, v78\n"
        "v_exp_f32 v79, v79\n"
        "v_cvt_pk_bf16_f32 v102, v76, v77\n"
        "v_mfma_f32_32x32x16_bf16 a[16:31], v[108:111], v[172:175], a[16:31]\n"
        "ds_read_b64_tr_b16 v[150:151], v19 offset:0\n"
        "v_exp_f32 v80, v80\n"
        "v_exp_f32 v81, v81\n"
        "v_cvt_pk_bf16_f32 v103, v78, v79\n"
        "s_branch 34f\n"
        "33:\n"
        "s_waitcnt vmcnt(4)\n"
        "ds_read_b128 v[112:115], v12 offset:0\n"
        "ds_read_b128 v[116:119], v13 offset:0\n"
        "ds_read_b128 v[120:123], v14 offset:0\n"
        "ds_read_b128 v[124:127], v15 offset:0\n"
        "s_waitcnt lgkmcnt(3)\n"
        "v_mfma_f32_32x32x16_bf16 v[64:79], v[112:115], a[160:163], 0\n"
        "ds_read_b64_tr_b16 v[144:145], v16 offset:0\n"
        "ds_read_b64_tr_b16 v[146:147], v17 offset:0\n"
        "ds_read_b64_tr_b16 v[148:149], v18 offset:0\n"
        "ds_read_b64_tr_b16 v[150:151], v19 offset:0\n"
        "s_waitcnt lgkmcnt(6)\n"
        "v_mfma_f32_32x32x16_bf16 v[64:79], v[116:119], a[164:167], v[64:79]\n"
        "ds_read_b64_tr_b16 v[152:153], v16 offset:2048\n"
        "ds_read_b64_tr_b16 v[154:155], v17 offset:2048\n"
        "ds_read_b64_tr_b16 v[156:157], v18 offset:2048\n"
        "ds_read_b64_tr_b16 v[158:159], v19 offset:2048\n"
        "s_waitcnt lgkmcnt(9)\n"
        "v_mfma_f32_32x32x16_bf16 v[64:79], v[120:123], a[168:171], v[64:79]\n"
        "ds_read_b128 a[192:195], v22 offset:22528\n"
        "ds_read_b128 a[196:199], v22 offset:53248\n"
        "s_waitcnt lgkmcnt(10)\n"
        "v_mfma_f32_32x32x16_bf16 v[64:79], v[124:127], a[172:175], v[64:79]\n"
        "ds_read_b128 a[200:203], v23 offset:22528\n"
        "ds_read_b128 a[204:207], v23 offset:53248\n"
        "v_mfma_f32_32x32x16_bf16 v[80:95], v[112:115], a[176:179], 0\n"
        "s_add_u32 m0, s62, 8192\n"
        "ds_read_b128 a[208:211], v22 offset:23552\n"
        "ds_read_b128 a[212:215], v22 offset:54272\n"
        "ds_read_b128 a[216:219], v23 offset:23552\n"
        "v_mfma_f32_32x32x16_bf16 v[80:95], v[116:119], a[180:183], v[80:95]\n"
        "ds_read_b128 a[220:223], v23 offset:54272\n"
        "ds_read_b128 a[224:227], v22 offset:24576\n"
        "ds_read_b128 a[228:231], v22 offset:55296\n"
        "v_mfma_f32_32x32x16_bf16 v[80:95], v[120:123], a[184:187], v[80:95]\n"
        "ds_read_b128 a[232:235], v23 offset:24576\n"
        "ds_read_b128 a[236:239], v23 offset:55296\n"
        "buffer_load_dwordx4 v10, s[44:47], s65 offen offset:0 sc1 lds\n"
        "v_exp_f32 v64, v64\n"
        "v_exp_f32 v65, v65\n"
        "v_mfma_f32_32x32x16_bf16 v[80:95], v[124:127], a[188:191], v[80:95]\n"
        "buffer_load_dwordx4 v11, s[44:47], s65 offen offset:1024 sc1 lds\n"
        "buffer_load_dwordx4 v10, s[44:47], s65 offen offset:2048 sc1 lds\n"
        "buffer_load_dwordx4 v11, s[44:47], s65 offen offset:3072 sc1 lds\n"
        "s_add_u32 s65, s65, s66\n"
        "v_exp_f32 v66, v66\n"
        "v_exp_f32 v67, v67\n"
        "v_cvt_pk_bf16_f32 v96, v64, v65\n"
        "v_exp_f32 v68, v68\n"
        "v_exp_f32 v69, v69\n"
        "v_cvt_pk_bf16_f32 v97, v66, v67\n"
        "v_exp_f32 v70, v70\n"
        "v_exp_f32 v71, v71\n"
        "v_cvt_pk_bf16_f32 v98, v68, v69\n"
        "v_exp_f32 v72, v72\n"
        "v_exp_f32 v73, v73\n"
        "v_cvt_pk_bf16_f32 v99, v70, v71\n"
        "v_exp_f32 v74, v74\n"
        "v_exp_f32 v75, v75\n"
        "v_cvt_pk_bf16_f32 v100, v72, v73\n"
        "v_exp_f32 v76, v76\n"
        "v_exp_f32 v77, v77\n"
        "v_cvt_pk_bf16_f32 v101, v74, v75\n"
        "v_exp_f32 v78, v78\n"
        "v_exp_f32 v79, v79\n"
        "v_cvt_pk_bf16_f32 v102, v76, v77\n"
        "v_exp_f32 v80, v80\n"
        "v_exp_f32 v81, v81\n"
        "v_cvt_pk_bf16_f32 v103, v78, v79\n"
        "s_waitcnt lgkmcnt(0)\n"
        "v_mfma_f32_32x32x16_bf16 v[64:79], v[112:115], a[192:195], 0\n"
        "s_waitcnt vmcnt(4)\n"
        "ds_read_b128 v[128:131], v12 offset:4096\n"
        "v_exp_f32 v82, v82\n"
        "v_exp_f32 v83, v83\n"
        "v_cvt_pk_bf16_f32 v104, v80, v81\n"
        "v_mfma_f32_32x32x16_bf16 v[64:79], v[116:119], a[196:199], v[64:79]\n"
        "s_min_u32 s77, s58, 249\n"
        "ds_read_b128 v[132:135], v13 offset:4096\n"
        "v_exp_f32 v84, v84\n"
        "v_exp_f32 v85, v85\n"
        "v_cvt_pk_bf16_f32 v105, v82, v83\n"
        "v_mfma_f32_32x32x16_bf16 v[64:79], v[120:123], a[200:203], v[64:79]\n"
        "s_lshl_b32 s77, s77, 3\n"
        "ds_read_b128 v[136:139], v14 offset:4096\n"
        "v_exp_f32 v86, v86\n"
        "v_exp_f32 v87, v87\n"
        "v_cvt_pk_bf16_f32 v106, v84, v85\n"
        "v_mfma_f32_32x32x16_bf16 v[64:79], v[124:127], a[204:207], v[64:79]\n"
        "s_add_u32 s77, s77, 1330944\n"
        "ds_read_b128 v[140:143], v15 offset:4096\n"
        "v_exp_f32 v88, v88\n"
        "v_exp_f32 v89, v89\n"
        "v_cvt_pk_bf16_f32 v107, v86, v87\n"
        "v_mfma_f32_32x32x16_bf16 a[0:15], v[96:99], v[144:147], 0\n"
        "ds_read_b64_tr_b16 v[160:161], v16 offset:4096\n"
        "v_exp_f32 v90, v90\n"
        "v_exp_f32 v91, v91\n"
        "v_cvt_pk_bf16_f32 v108, v88, v89\n"
        "v_mfma_f32_32x32x16_bf16 a[16:31], v[96:99], v[148:151], 0\n"
        "ds_read_b64_tr_b16 v[162:163], v17 offset:4096\n"
        "v_exp_f32 v92, v92\n"
        "v_exp_f32 v93, v93\n"
        "v_cvt_pk_bf16_f32 v109, v90, v91\n"
        "v_mfma_f32_32x32x16_bf16 a[0:15], v[100:103], v[152:155], a[0:15]\n"
        "ds_read_b64_tr_b16 v[164:165], v18 offset:4096\n"
        "v_exp_f32 v94, v94\n"
        "v_exp_f32 v95, v95\n"
        "v_cvt_pk_bf16_f32 v110, v92, v93\n"
        "v_mfma_f32_32x32x16_bf16 a[16:31], v[100:103], v[156:159], a[16:31]\n"
        "ds_read_b64_tr_b16 v[166:167], v19 offset:4096\n"
        "v_exp_f32 v64, v64\n"
        "v_exp_f32 v65, v65\n"
        "v_cvt_pk_bf16_f32 v111, v94, v95\n"
        "v_mfma_f32_32x32x16_bf16 v[80:95], v[112:115], a[208:211], 0\n"
        "ds_read_b64_tr_b16 v[168:169], v16 offset:6144\n"
        "v_exp_f32 v66, v66\n"
        "v_exp_f32 v67, v67\n"
        "v_cvt_pk_bf16_f32 v96, v64, v65\n"
        "v_mfma_f32_32x32x16_bf16 v[80:95], v[116:119], a[212:215], v[80:95]\n"
        "ds_read_b64_tr_b16 v[170:171], v17 offset:6144\n"
        "v_exp_f32 v68, v68\n"
        "v_exp_f32 v69, v69\n"
        "v_cvt_pk_bf16_f32 v97, v66, v67\n"
        "v_mfma_f32_32x32x16_bf16 v[80:95], v[120:123], a[216:219], v[80:95]\n"
        "ds_read_b64_tr_b16 v[172:173], v18 offset:6144\n"
        "v_exp_f32 v70, v70\n"
        "v_exp_f32 v71, v71\n"
        "v_cvt_pk_bf16_f32 v98, v68, v69\n"
        "v_mfma_f32_32x32x16_bf16 v[80:95], v[124:127], a[220:223], v[80:95]\n"
        "s_add_u32 m0, s62, 12288\n"
        "ds_read_b64_tr_b16 v[174:175], v19 offset:6144\n"
        "v_exp_f32 v72, v72\n"
        "v_exp_f32 v73, v73\n"
        "v_cvt_pk_bf16_f32 v99, v70, v71\n"
        "v_mfma_f32_32x32x16_bf16 a[32:47], v[104:107], v[144:147], 0\n"
        "buffer_load_dwordx4 v10, s[44:47], s65 offen offset:0 sc1 lds\n"
        "v_exp_f32 v74, v74\n"
        "v_exp_f32 v75, v75\n"
        "v_cvt_pk_bf16_f32 v100, v72, v73\n"
        "v_mfma_f32_32x32x16_bf16 a[48:63], v[104:107], v[148:151], 0\n"
        "buffer_load_dwordx4 v11, s[44:47], s65 offen offset:1024 sc1 lds\n"
        "v_exp_f32 v76, v76\n"
        "v_exp_f32 v77, v77\n"
        "v_cvt_pk_bf16_f32 v101, v74, v75\n"
        "v_mfma_f32_32x32x16_bf16 a[32:47], v[108:111], v[152:155], a[32:47]\n"
        "s_barrier\n"
        "buffer_load_dwordx4 v10, s[44:47], s65 offen offset:2048 sc1 lds\n"
        "v_exp_f32 v78, v78\n"
        "v_exp_f32 v79, v79\n"
        "v_cvt_pk_bf16_f32 v102, v76, v77\n"
        "v_mfma_f32_32x32x16_bf16 a[48:63], v[108:111], v[156:159], a[48:63]\n"
        "buffer_load_dwordx4 v11, s[44:47], s65 offen offset:3072 sc1 lds\n"
        "v_exp_f32 v80, v80\n"
        "v_exp_f32 v81, v81\n"
        "v_cvt_pk_bf16_f32 v103, v78, v79\n"
        "v_mfma_f32_32x32x16_bf16 v[64:79], v[112:115], a[224:227], 0\n"
        "s_add_u32 s65, s65, s66\n"
        "ds_read_b128 v[176:179], v24 offset:0\n"
        "v_exp_f32 v82, v82\n"
        "v_exp_f32 v83, v83\n"
        "v_cvt_pk_bf16_f32 v104, v80, v81\n"
        "v_mfma_f32_32x32x16_bf16 v[64:79], v[116:119], a[228:231], v[64:79]\n"
        "s_add_u32 m0, s62, 0\n"
        "ds_read_b128 v[180:183], v24 offset:4096\n"
        "v_exp_f32 v84, v84\n"
        "v_exp_f32 v85, v85\n"
        "v_cvt_pk_bf16_f32 v105, v82, v83\n"
        "v_mfma_f32_32x32x16_bf16 v[64:79], v[120:123], a[232:235], v[64:79]\n"
        "buffer_load_dwordx4 v10, s[44:47], s65 offen offset:0 sc1 lds\n"
        "v_exp_f32 v86, v86\n"
        "v_exp_f32 v87, v87\n"
        "v_cvt_pk_bf16_f32 v106, v84, v85\n"
        "v_mfma_f32_32x32x16_bf16 v[64:79], v[124:127], a[236:239], v[64:79]\n"
        "buffer_load_dwordx4 v11, s[44:47], s65 offen offset:1024 sc1 lds\n"
        "v_exp_f32 v88, v88\n"
        "v_exp_f32 v89, v89\n"
        "v_cvt_pk_bf16_f32 v107, v86, v87\n"
        "v_mfma_f32_32x32x16_bf16 a[64:79], v[96:99], v[144:147], 0\n"
        "buffer_load_dwordx4 v10, s[44:47], s65 offen offset:2048 sc1 lds\n"
        "v_exp_f32 v90, v90\n"
        "v_exp_f32 v91, v91\n"
        "v_cvt_pk_bf16_f32 v108, v88, v89\n"
        "v_mfma_f32_32x32x16_bf16 a[80:95], v[96:99], v[148:151], 0\n"
        "buffer_load_dwordx4 v11, s[44:47], s65 offen offset:3072 sc1 lds\n"
        "v_exp_f32 v92, v92\n"
        "v_exp_f32 v93, v93\n"
        "v_cvt_pk_bf16_f32 v109, v90, v91\n"
        "v_mfma_f32_32x32x16_bf16 a[64:79], v[100:103], v[152:155], a[64:79]\n"
        "s_add_u32 s65, s65, s66\n"
        "s_waitcnt lgkmcnt(2)\n"
        "ds_read_b128 v[184:187], v24 offset:8192\n"
        "v_exp_f32 v94, v94\n"
        "v_exp_f32 v95, v95\n"
        "v_cvt_pk_bf16_f32 v110, v92, v93\n"
        "v_mfma_f32_32x32x16_bf16 a[80:95], v[100:103], v[156:159], a[80:95]\n"
        "s_add_u32 m0, s62, 4096\n"
        "v_exp_f32 v64, v64\n"
        "v_exp_f32 v65, v65\n"
        "v_cvt_pk_bf16_f32 v111, v94, v95\n"
        "v_lshl_add_u32 v232, v8, 11, s77\n"
        "v_mfma_f32_32x32x16_bf16 v[80:95], v[128:131], a[160:163], 0\n"
        "buffer_load_dwordx4 v10, s[44:47], s65 offen offset:0 sc1 lds\n"
        "v_exp_f32 v66, v66\n"
        "v_exp_f32 v67, v67\n"
        "v_cvt_pk_bf16_f32 v96, v64, v65\n"
        "v_mfma_f32_32x32x16_bf16 v[80:95], v[132:135], a[164:167], v[80:95]\n"
        "buffer_load_dwordx4 v11, s[44:47], s65 offen offset:1024 sc1 lds\n"
        "v_exp_f32 v68, v68\n"
        "v_exp_f32 v69, v69\n"
        "v_cvt_pk_bf16_f32 v97, v66, v67\n"
        "v_mfma_f32_32x32x16_bf16 v[80:95], v[136:139], a[168:171], v[80:95]\n"
        "buffer_load_dwordx4 v10, s[44:47], s65 offen offset:2048 sc1 lds\n"
        "v_exp_f32 v70, v70\n"
        "v_exp_f32 v71, v71\n"
        "v_cvt_pk_bf16_f32 v98, v68, v69\n"
        "v_mfma_f32_32x32x16_bf16 v[80:95], v[140:143], a[172:175], v[80:95]\n"
        "buffer_load_dwordx4 v11, s[44:47], s65 offen offset:3072 sc1 lds\n"
        "v_exp_f32 v72, v72\n"
        "v_exp_f32 v73, v73\n"
        "v_cvt_pk_bf16_f32 v99, v70, v71\n"
        "v_mfma_f32_32x32x16_bf16 a[96:111], v[104:107], v[144:147], 0\n"
        "v_exp_f32 v74, v74\n"
        "v_exp_f32 v75, v75\n"
        "v_cvt_pk_bf16_f32 v100, v72, v73\n"
        "v_mov_b32 v233, 1312256\n"
        "v_mfma_f32_32x32x16_bf16 a[112:127], v[104:107], v[148:151], 0\n"
        "s_add_u32 s65, s65, s66\n"
        "v_exp_f32 v76, v76\n"
        "v_exp_f32 v77, v77\n"
        "v_cvt_pk_bf16_f32 v101, v74, v75\n"
        "v_cmp_gt_u32 vcc, 250, v8\n"
        "v_mfma_f32_32x32x16_bf16 a[96:111], v[108:111], v[152:155], a[96:111]\n"
        "s_waitcnt lgkmcnt(2)\n"
        "buffer_store_dwordx4 v[176:179], v25, s[36:39], 0 offen sc1\n"
        "v_exp_f32 v78, v78\n"
        "v_exp_f32 v79, v79\n"
        "v_cvt_pk_bf16_f32 v102, v76, v77\n"
        "v_mfma_f32_32x32x16_bf16 a[112:127], v[108:111], v[156:159], a[112:127]\n"
        "v_exp_f32 v80, v80\n"
        "v_exp_f32 v81, v81\n"
        "v_cvt_pk_bf16_f32 v103, v78, v79\n"
        "v_mul_u32_u24 v234, 10368, v8\n"
        "v_mfma_f32_32x32x16_bf16 v[64:79], v[128:131], a[176:179], 0\n"
        "v_exp_f32 v82, v82\n"
        "v_exp_f32 v83, v83\n"
        "v_cvt_pk_bf16_f32 v104, v80, v81\n"
        "v_cndmask_b32 v32, v233, v232, vcc\n"
        "v_mfma_f32_32x32x16_bf16 v[64:79], v[132:135], a[180:183], v[64:79]\n"
        "s_or_b32 s77, s69, 1842944\n"
        "s_waitcnt lgkmcnt(1)\n"
        "buffer_store_dwordx4 v[180:183], v25, s[36:39], s66 offen sc1\n"
        "v_exp_f32 v84, v84\n"
        "v_exp_f32 v85, v85\n"
        "v_cvt_pk_bf16_f32 v105, v82, v83\n"
        "v_mfma_f32_32x32x16_bf16 v[64:79], v[136:139], a[184:187], v[64:79]\n"
        "v_exp_f32 v86, v86\n"
        "v_exp_f32 v87, v87\n"
        "v_cvt_pk_bf16_f32 v106, v84, v85\n"
        "v_add_u32 v234, s77, v234\n"
        "v_mfma_f32_32x32x16_bf16 v[64:79], v[140:143], a[188:191], v[64:79]\n"
        "v_exp_f32 v88, v88\n"
        "v_exp_f32 v89, v89\n"
        "v_cvt_pk_bf16_f32 v107, v86, v87\n"
        "v_cndmask_b32 v34, 0, 12, vcc\n"
        "v_mfma_f32_32x32x16_bf16 a[128:143], v[96:99], v[144:147], 0\n"
        "s_waitcnt lgkmcnt(0)\n"
        "buffer_store_dwordx4 v[184:187], v26, s[36:39], s67 offen sc1\n"
        "v_exp_f32 v90, v90\n"
        "v_exp_f32 v91, v91\n"
        "v_cvt_pk_bf16_f32 v108, v88, v89\n"
        "v_mfma_f32_32x32x16_bf16 a[144:159], v[96:99], v[148:151], 0\n"
        "v_exp_f32 v92, v92\n"
        "v_exp_f32 v93, v93\n"
        "v_cvt_pk_bf16_f32 v109, v90, v91\n"
        "v_cndmask_b32 v33, v235, v234, vcc\n"
        "v_mfma_f32_32x32x16_bf16 a[128:143], v[100:103], v[152:155], a[128:143]\n"
        "v_exp_f32 v94, v94\n"
        "v_exp_f32 v95, v95\n"
        "v_cvt_pk_bf16_f32 v110, v92, v93\n"
        "v_mov_b32 v236, -1\n"
        "v_mfma_f32_32x32x16_bf16 a[144:159], v[100:103], v[156:159], a[144:159]\n"
        "v_exp_f32 v64, v64\n"
        "v_exp_f32 v65, v65\n"
        "v_cvt_pk_bf16_f32 v111, v94, v95\n"
        "v_mov_b32 v237, -1\n"
        "v_mfma_f32_32x32x16_bf16 v[80:95], v[128:131], a[192:195], 0\n"
        "s_waitcnt vmcnt(15)\n"
        "ds_read_b128 v[112:115], v12 offset:8192\n"
        "v_exp_f32 v66, v66\n"
        "v_exp_f32 v67, v67\n"
        "v_cvt_pk_bf16_f32 v96, v64, v65\n"
        "v_mfma_f32_32x32x16_bf16 v[80:95], v[132:135], a[196:199], v[80:95]\n"
        "ds_read_b128 v[116:119], v13 offset:8192\n"
        "v_exp_f32 v68, v68\n"
        "v_exp_f32 v69, v69\n"
        "v_cvt_pk_bf16_f32 v97, v66, v67\n"
        "v_mfma_f32_32x32x16_bf16 v[80:95], v[136:139], a[200:203], v[80:95]\n"
        "ds_read_b128 v[120:123], v14 offset:8192\n"
        "v_exp_f32 v70, v70\n"
        "v_exp_f32 v71, v71\n"
        "v_cvt_pk_bf16_f32 v98, v68, v69\n"
        "v_mfma_f32_32x32x16_bf16 v[80:95], v[140:143], a[204:207], v[80:95]\n"
        "ds_read_b128 v[124:127], v15 offset:8192\n"
        "v_exp_f32 v72, v72\n"
        "v_exp_f32 v73, v73\n"
        "v_cvt_pk_bf16_f32 v99, v70, v71\n"
        "v_mfma_f32_32x32x16_bf16 a[0:15], v[104:107], v[160:163], a[0:15]\n"
        "ds_read_b64_tr_b16 v[144:145], v16 offset:8192\n"
        "v_exp_f32 v74, v74\n"
        "v_exp_f32 v75, v75\n"
        "v_cvt_pk_bf16_f32 v100, v72, v73\n"
        "v_mfma_f32_32x32x16_bf16 a[16:31], v[104:107], v[164:167], a[16:31]\n"
        "ds_read_b64_tr_b16 v[146:147], v17 offset:8192\n"
        "v_exp_f32 v76, v76\n"
        "v_exp_f32 v77, v77\n"
        "v_cvt_pk_bf16_f32 v101, v74, v75\n"
        "v_mfma_f32_32x32x16_bf16 a[0:15], v[108:111], v[168:171], a[0:15]\n"
        "ds_read_b64_tr_b16 v[148:149], v18 offset:8192\n"
        "v_exp_f32 v78, v78\n"
        "v_exp_f32 v79, v79\n"
        "v_cvt_pk_bf16_f32 v102, v76, v77\n"
        "v_mfma_f32_32x32x16_bf16 a[16:31], v[108:111], v[172:175], a[16:31]\n"
        "ds_read_b64_tr_b16 v[150:151], v19 offset:8192\n"
        "v_exp_f32 v80, v80\n"
        "v_exp_f32 v81, v81\n"
        "v_cvt_pk_bf16_f32 v103, v78, v79\n"
        "v_mfma_f32_32x32x16_bf16 v[64:79], v[128:131], a[208:211], 0\n"
        "ds_read_b64_tr_b16 v[152:153], v16 offset:10240\n"
        "v_exp_f32 v82, v82\n"
        "v_exp_f32 v83, v83\n"
        "v_cvt_pk_bf16_f32 v104, v80, v81\n"
        "v_mfma_f32_32x32x16_bf16 v[64:79], v[132:135], a[212:215], v[64:79]\n"
        "ds_read_b64_tr_b16 v[154:155], v17 offset:10240\n"
        "v_exp_f32 v84, v84\n"
        "v_exp_f32 v85, v85\n"
        "v_cvt_pk_bf16_f32 v105, v82, v83\n"
        "v_mfma_f32_32x32x16_bf16 v[64:79], v[136:139], a[216:219], v[64:79]\n"
        "ds_read_b64_tr_b16 v[156:157], v18 offset:10240\n"
        "v_exp_f32 v86, v86\n"
        "v_exp_f32 v87, v87\n"
        "v_cvt_pk_bf16_f32 v106, v84, v85\n"
        "v_mfma_f32_32x32x16_bf16 v[64:79], v[140:143], a[220:223], v[64:79]\n"
        "ds_read_b64_tr_b16 v[158:159], v19 offset:10240\n"
        "v_exp_f32 v88, v88\n"
        "v_exp_f32 v89, v89\n"
        "v_cvt_pk_bf16_f32 v107, v86, v87\n"
        "v_mfma_f32_32x32x16_bf16 a[32:47], v[96:99], v[160:163], a[32:47]\n"
        "v_exp_f32 v90, v90\n"
        "v_exp_f32 v91, v91\n"
        "v_cvt_pk_bf16_f32 v108, v88, v89\n"
        "v_mov_b32 v238, -1\n"
        "v_mfma_f32_32x32x16_bf16 a[48:63], v[96:99], v[164:167], a[48:63]\n"
        "v_exp_f32 v92, v92\n"
        "v_exp_f32 v93, v93\n"
        "v_cvt_pk_bf16_f32 v109, v90, v91\n"
        "v_mov_b32 v239, -1\n"
        "v_mfma_f32_32x32x16_bf16 a[32:47], v[100:103], v[168:171], a[32:47]\n"
        "s_lshl_b32 s75, s61, 2\n"
        "v_exp_f32 v94, v94\n"
        "v_exp_f32 v95, v95\n"
        "v_cvt_pk_bf16_f32 v110, v92, v93\n"
        "v_mfma_f32_32x32x16_bf16 a[48:63], v[100:103], v[172:175], a[48:63]\n"
        "s_mul_i32 s77, s59, 2720\n"
        "v_exp_f32 v64, v64\n"
        "v_exp_f32 v65, v65\n"
        "v_cvt_pk_bf16_f32 v111, v94, v95\n"
        "v_mfma_f32_32x32x16_bf16 v[80:95], v[128:131], a[224:227], 0\n"
        "s_add_u32 s77, s77, 122880\n"
        "v_exp_f32 v66, v66\n"
        "v_exp_f32 v67, v67\n"
        "v_cvt_pk_bf16_f32 v96, v64, v65\n"
        "v_and_b32 v232, 1, v246\n"
        "v_mfma_f32_32x32x16_bf16 v[80:95], v[132:135], a[228:231], v[80:95]\n"
        "v_exp_f32 v68, v68\n"
        "v_exp_f32 v69, v69\n"
        "v_cvt_pk_bf16_f32 v97, v66, v67\n"
        "v_mul_u32_u24 v232, 272, v232\n"
        "v_mfma_f32_32x32x16_bf16 v[80:95], v[136:139], a[232:235], v[80:95]\n"
        "v_exp_f32 v70, v70\n"
        "v_exp_f32 v71, v71\n"
        "v_cvt_pk_bf16_f32 v98, v68, v69\n"
        "v_add_u32 v232, s77, v232\n"
        "v_mfma_f32_32x32x16_bf16 v[80:95], v[140:143], a[236:239], v[80:95]\n"
        "v_exp_f32 v72, v72\n"
        "v_exp_f32 v73, v73\n"
        "v_cvt_pk_bf16_f32 v99, v70, v71\n"
        "v_lshl_add_u32 v35, v60, 5, v232\n"
        "v_mfma_f32_32x32x16_bf16 a[64:79], v[104:107], v[160:163], a[64:79]\n"
        "v_exp_f32 v74, v74\n"
        "v_exp_f32 v75, v75\n"
        "v_cvt_pk_bf16_f32 v100, v72, v73\n"
        "v_lshl_add_u32 v36, v60, 3, v232\n"
        "v_mfma_f32_32x32x16_bf16 a[80:95], v[104:107], v[164:167], a[80:95]\n"
        "v_exp_f32 v76, v76\n"
        "v_exp_f32 v77, v77\n"
        "v_cvt_pk_bf16_f32 v101, v74, v75\n"
        "v_lshl_or_b32 v37, v247, 4, s69\n"
        "v_mfma_f32_32x32x16_bf16 a[64:79], v[108:111], v[168:171], a[64:79]\n"
        "s_waitcnt lgkmcnt(0)\n"
        "v_exp_f32 v78, v78\n"
        "v_exp_f32 v79, v79\n"
        "v_cvt_pk_bf16_f32 v102, v76, v77\n"
        "v_and_b32 v39, 48, v248\n"
        "v_mfma_f32_32x32x16_bf16 a[80:95], v[108:111], v[172:175], a[80:95]\n"
        "s_add_u32 m0, s62, 8192\n"
        "v_exp_f32 v80, v80\n"
        "v_exp_f32 v81, v81\n"
        "v_cvt_pk_bf16_f32 v103, v78, v79\n"
        "v_mfma_f32_32x32x16_bf16 v[64:79], v[112:115], a[160:163], 0\n"
        "s_xor_b32 s77, s59, 1\n"
        "buffer_load_dwordx4 v10, s[44:47], s65 offen offset:0 sc1 lds\n"
        "v_exp_f32 v82, v82\n"
        "v_exp_f32 v83, v83\n"
        "v_cvt_pk_bf16_f32 v104, v80, v81\n"
        "v_mfma_f32_32x32x16_bf16 v[64:79], v[116:119], a[164:167], v[64:79]\n"
        "s_mul_i32 s77, s77, 30720\n"
        "buffer_load_dwordx4 v11, s[44:47], s65 offen offset:1024 sc1 lds\n"
        "v_exp_f32 v84, v84\n"
        "v_exp_f32 v85, v85\n"
        "v_cvt_pk_bf16_f32 v105, v82, v83\n"
        "v_mfma_f32_32x32x16_bf16 v[64:79], v[120:123], a[168:171], v[64:79]\n"
        "buffer_load_dwordx4 v10, s[44:47], s65 offen offset:2048 sc1 lds\n"
        "v_exp_f32 v86, v86\n"
        "v_exp_f32 v87, v87\n"
        "v_cvt_pk_bf16_f32 v106, v84, v85\n"
        "v_mfma_f32_32x32x16_bf16 v[64:79], v[124:127], a[172:175], v[64:79]\n"
        "buffer_load_dwordx4 v11, s[44:47], s65 offen offset:3072 sc1 lds\n"
        "v_exp_f32 v88, v88\n"
        "v_exp_f32 v89, v89\n"
        "v_cvt_pk_bf16_f32 v107, v86, v87\n"
        "v_mfma_f32_32x32x16_bf16 a[96:111], v[96:99], v[160:163], a[96:111]\n"
        "v_exp_f32 v90, v90\n"
        "v_exp_f32 v91, v91\n"
        "v_cvt_pk_bf16_f32 v108, v88, v89\n"
        "v_add_u32 v40, s77, v248\n"
        "v_mfma_f32_32x32x16_bf16 a[112:127], v[96:99], v[164:167], a[112:127]\n"
        "s_add_u32 s65, s65, s66\n"
        "v_exp_f32 v92, v92\n"
        "v_exp_f32 v93, v93\n"
        "v_cvt_pk_bf16_f32 v109, v90, v91\n"
        "v_mfma_f32_32x32x16_bf16 a[96:111], v[100:103], v[168:171], a[96:111]\n"
        "s_xor_b32 s77, s59, 2\n"
        "v_exp_f32 v94, v94\n"
        "v_exp_f32 v95, v95\n"
        "v_cvt_pk_bf16_f32 v110, v92, v93\n"
        "v_mfma_f32_32x32x16_bf16 a[112:127], v[100:103], v[172:175], a[112:127]\n"
        "s_mul_i32 s77, s77, 30720\n"
        "v_exp_f32 v64, v64\n"
        "v_exp_f32 v65, v65\n"
        "v_cvt_pk_bf16_f32 v111, v94, v95\n"
        "v_add_u32 v41, s77, v248\n"
        "v_mfma_f32_32x32x16_bf16 v[80:95], v[112:115], a[176:179], 0\n"
        "s_xor_b32 s77, s59, 3\n"
        "v_exp_f32 v66, v66\n"
        "v_exp_f32 v67, v67\n"
        "v_cvt_pk_bf16_f32 v96, v64, v65\n"
        "v_mfma_f32_32x32x16_bf16 v[80:95], v[116:119], a[180:183], v[80:95]\n"
        "s_mul_i32 s77, s77, 30720\n"
        "v_exp_f32 v68, v68\n"
        "v_exp_f32 v69, v69\n"
        "v_cvt_pk_bf16_f32 v97, v66, v67\n"
        "v_add_u32 v42, s77, v248\n"
        "v_mfma_f32_32x32x16_bf16 v[80:95], v[120:123], a[184:187], v[80:95]\n"
        "v_exp_f32 v70, v70\n"
        "v_exp_f32 v71, v71\n"
        "v_cvt_pk_bf16_f32 v98, v68, v69\n"
        "v_and_b32 v232, 31, v9\n"
        "v_mfma_f32_32x32x16_bf16 v[80:95], v[124:127], a[188:191], v[80:95]\n"
        "v_exp_f32 v72, v72\n"
        "v_exp_f32 v73, v73\n"
        "v_cvt_pk_bf16_f32 v99, v70, v71\n"
        "v_mul_u32_u24 v232, 144, v232\n"
        "v_mfma_f32_32x32x16_bf16 a[128:143], v[104:107], v[160:163], a[128:143]\n"
        "v_exp_f32 v74, v74\n"
        "v_exp_f32 v75, v75\n"
        "v_cvt_pk_bf16_f32 v100, v72, v73\n"
        "v_lshrrev_b32 v233, 5, v9\n"
        "v_mfma_f32_32x32x16_bf16 a[144:159], v[104:107], v[164:167], a[144:159]\n"
        "v_exp_f32 v76, v76\n"
        "v_exp_f32 v77, v77\n"
        "v_cvt_pk_bf16_f32 v101, v74, v75\n"
        "v_lshl_add_u32 v232, v233, 4, v232\n"
        "v_mfma_f32_32x32x16_bf16 a[128:143], v[108:111], v[168:171], a[128:143]\n"
        "s_lshl_b32 s77, s59, 5\n"
        "v_exp_f32 v78, v78\n"
        "v_exp_f32 v79, v79\n"
        "v_cvt_pk_bf16_f32 v102, v76, v77\n"
        "v_mfma_f32_32x32x16_bf16 a[144:159], v[108:111], v[172:175], a[144:159]\n"
        "s_add_u32 s77, s77, 154384\n"
        "v_exp_f32 v80, v80\n"
        "v_exp_f32 v81, v81\n"
        "v_cvt_pk_bf16_f32 v103, v78, v79\n"
        "v_add_u32 v55, s77, v232\n"
        "v_mfma_f32_32x32x16_bf16 v[64:79], v[112:115], a[192:195], 0\n"
        "s_waitcnt vmcnt(15)\n"
        "ds_read_b128 v[128:131], v12 offset:12288\n"
        "v_exp_f32 v82, v82\n"
        "v_exp_f32 v83, v83\n"
        "v_cvt_pk_bf16_f32 v104, v80, v81\n"
        "v_mfma_f32_32x32x16_bf16 v[64:79], v[116:119], a[196:199], v[64:79]\n"
        "ds_read_b128 v[132:135], v13 offset:12288\n"
        "v_exp_f32 v84, v84\n"
        "v_exp_f32 v85, v85\n"
        "v_cvt_pk_bf16_f32 v105, v82, v83\n"
        "v_mfma_f32_32x32x16_bf16 v[64:79], v[120:123], a[200:203], v[64:79]\n"
        "ds_read_b128 v[136:139], v14 offset:12288\n"
        "v_exp_f32 v86, v86\n"
        "v_exp_f32 v87, v87\n"
        "v_cvt_pk_bf16_f32 v106, v84, v85\n"
        "v_mfma_f32_32x32x16_bf16 v[64:79], v[124:127], a[204:207], v[64:79]\n"
        "ds_read_b128 v[140:143], v15 offset:12288\n"
        "v_exp_f32 v88, v88\n"
        "v_exp_f32 v89, v89\n"
        "v_cvt_pk_bf16_f32 v107, v86, v87\n"
        "v_mfma_f32_32x32x16_bf16 a[0:15], v[96:99], v[144:147], a[0:15]\n"
        "ds_read_b64_tr_b16 v[160:161], v16 offset:12288\n"
        "v_exp_f32 v90, v90\n"
        "v_exp_f32 v91, v91\n"
        "v_cvt_pk_bf16_f32 v108, v88, v89\n"
        "v_mfma_f32_32x32x16_bf16 a[16:31], v[96:99], v[148:151], a[16:31]\n"
        "ds_read_b64_tr_b16 v[162:163], v17 offset:12288\n"
        "v_exp_f32 v92, v92\n"
        "v_exp_f32 v93, v93\n"
        "v_cvt_pk_bf16_f32 v109, v90, v91\n"
        "v_mfma_f32_32x32x16_bf16 a[0:15], v[100:103], v[152:155], a[0:15]\n"
        "ds_read_b64_tr_b16 v[164:165], v18 offset:12288\n"
        "v_exp_f32 v94, v94\n"
        "v_exp_f32 v95, v95\n"
        "v_cvt_pk_bf16_f32 v110, v92, v93\n"
        "v_mfma_f32_32x32x16_bf16 a[16:31], v[100:103], v[156:159], a[16:31]\n"
        "ds_read_b64_tr_b16 v[166:167], v19 offset:12288\n"
        "v_exp_f32 v64, v64\n"
        "v_exp_f32 v65, v65\n"
        "v_cvt_pk_bf16_f32 v111, v94, v95\n"
        "v_mfma_f32_32x32x16_bf16 v[80:95], v[112:115], a[208:211], 0\n"
        "ds_read_b64_tr_b16 v[168:169], v16 offset:14336\n"
        "v_exp_f32 v66, v66\n"
        "v_exp_f32 v67, v67\n"
        "v_cvt_pk_bf16_f32 v96, v64, v65\n"
        "v_mfma_f32_32x32x16_bf16 v[80:95], v[116:119], a[212:215], v[80:95]\n"
        "ds_read_b64_tr_b16 v[170:171], v17 offset:14336\n"
        "v_exp_f32 v68, v68\n"
        "v_exp_f32 v69, v69\n"
        "v_cvt_pk_bf16_f32 v97, v66, v67\n"
        "v_mfma_f32_32x32x16_bf16 v[80:95], v[120:123], a[216:219], v[80:95]\n"
        "ds_read_b64_tr_b16 v[172:173], v18 offset:14336\n"
        "v_exp_f32 v70, v70\n"
        "v_exp_f32 v71, v71\n"
        "v_cvt_pk_bf16_f32 v98, v68, v69\n"
        "v_mfma_f32_32x32x16_bf16 v[80:95], v[124:127], a[220:223], v[80:95]\n"
        "ds_read_b64_tr_b16 v[174:175], v19 offset:14336\n"
        "v_exp_f32 v72, v72\n"
        "v_exp_f32 v73, v73\n"
        "v_cvt_pk_bf16_f32 v99, v70, v71\n"
        "v_mfma_f32_32x32x16_bf16 a[32:47], v[104:107], v[144:147], a[32:47]\n"
        "s_waitcnt vmcnt(4)\n"
        "v_exp_f32 v74, v74\n"
        "v_exp_f32 v75, v75\n"
        "v_cvt_pk_bf16_f32 v100, v72, v73\n"
        "v_and_b32 v232, 3, v9\n"
        "v_mfma_f32_32x32x16_bf16 a[48:63], v[104:107], v[148:151], a[48:63]\n"
        "s_barrier\n"
        "buffer_store_dwordx2 v[30:31], v29, s[36:39], 0 offen sc1\n"
        "v_exp_f32 v76, v76\n"
        "v_exp_f32 v77, v77\n"
        "v_cvt_pk_bf16_f32 v101, v74, v75\n"
        "v_mfma_f32_32x32x16_bf16 a[32:47], v[108:111], v[152:155], a[32:47]\n"
        "v_exp_f32 v78, v78\n"
        "v_exp_f32 v79, v79\n"
        "v_cvt_pk_bf16_f32 v102, v76, v77\n"
        "v_min_u32 v54, 2, v232\n"
        "v_mfma_f32_32x32x16_bf16 a[48:63], v[108:111], v[156:159], a[48:63]\n"
        "v_exp_f32 v80, v80\n"
        "v_exp_f32 v81, v81\n"
        "v_cvt_pk_bf16_f32 v103, v78, v79\n"
        "v_sub_u32 v232, v232, v54\n"
        "v_mfma_f32_32x32x16_bf16 v[64:79], v[112:115], a[224:227], 0\n"
        "buffer_store_dwordx4 v[236:239], v27, s[36:39], 0 offen sc1\n"
        "v_exp_f32 v82, v82\n"
        "v_exp_f32 v83, v83\n"
        "v_cvt_pk_bf16_f32 v104, v80, v81\n"
        "v_mfma_f32_32x32x16_bf16 v[64:79], v[116:119], a[228:231], v[64:79]\n"
        "v_exp_f32 v84, v84\n"
        "v_exp_f32 v85, v85\n"
        "v_cvt_pk_bf16_f32 v105, v82, v83\n"
        "v_lshlrev_b32 v232, 4, v232\n"
        "v_mfma_f32_32x32x16_bf16 v[64:79], v[120:123], a[232:235], v[64:79]\n"
        "v_exp_f32 v86, v86\n"
        "v_exp_f32 v87, v87\n"
        "v_cvt_pk_bf16_f32 v106, v84, v85\n"
        "v_mov_b32 v234, 0x3f80\n"
        "v_mfma_f32_32x32x16_bf16 v[64:79], v[124:127], a[236:239], v[64:79]\n"
        "v_exp_f32 v88, v88\n"
        "v_exp_f32 v89, v89\n"
        "v_cvt_pk_bf16_f32 v107, v86, v87\n"
        "v_lshlrev_b32 v232, v232, v234\n"
        "v_mfma_f32_32x32x16_bf16 a[64:79], v[96:99], v[144:147], a[64:79]\n"
        "buffer_store_dwordx4 v[236:239], v27, s[36:39], s66 offen sc1\n"
        "v_exp_f32 v90, v90\n"
        "v_exp_f32 v91, v91\n"
        "v_cvt_pk_bf16_f32 v108, v88, v89\n"
        "v_mfma_f32_32x32x16_bf16 a[80:95], v[96:99], v[148:151], a[80:95]\n"
        "v_exp_f32 v92, v92\n"
        "v_exp_f32 v93, v93\n"
        "v_cvt_pk_bf16_f32 v109, v90, v91\n"
        "v_or_b32 v54, 12, v54\n"
        "v_mfma_f32_32x32x16_bf16 a[64:79], v[100:103], v[152:155], a[64:79]\n"
        "s_waitcnt lgkmcnt(0)\n"
        "v_exp_f32 v94, v94\n"
        "v_exp_f32 v95, v95\n"
        "v_cvt_pk_bf16_f32 v110, v92, v93\n"
        "v_mul_u32_u24 v54, 0x1111, v54\n"
        "v_mfma_f32_32x32x16_bf16 a[80:95], v[100:103], v[156:159], a[80:95]\n"
        "s_add_u32 m0, s62, 12288\n"
        "v_exp_f32 v64, v64\n"
        "v_exp_f32 v65, v65\n"
        "v_cvt_pk_bf16_f32 v111, v94, v95\n"
        "v_lshrrev_b32 v233, 2, v59\n"
        "v_mfma_f32_32x32x16_bf16 v[80:95], v[128:131], a[160:163], 0\n"
        "buffer_load_dwordx4 v10, s[44:47], s65 offen offset:0 sc1 lds\n"
        "v_exp_f32 v66, v66\n"
        "v_exp_f32 v67, v67\n"
        "v_cvt_pk_bf16_f32 v96, v64, v65\n"
        "v_mfma_f32_32x32x16_bf16 v[80:95], v[132:135], a[164:167], v[80:95]\n"
        "buffer_load_dwordx4 v11, s[44:47], s65 offen offset:1024 sc1 lds\n"
        "v_exp_f32 v68, v68\n"
        "v_exp_f32 v69, v69\n"
        "v_cvt_pk_bf16_f32 v97, v66, v67\n"
        "v_mfma_f32_32x32x16_bf16 v[80:95], v[136:139], a[168:171], v[80:95]\n"
        "buffer_load_dwordx4 v10, s[44:47], s65 offen offset:2048 sc1 lds\n"
        "v_exp_f32 v70, v70\n"
        "v_exp_f32 v71, v71\n"
        "v_cvt_pk_bf16_f32 v98, v68, v69\n"
        "v_mfma_f32_32x32x16_bf16 v[80:95], v[140:143], a[172:175], v[80:95]\n"
        "buffer_load_dwordx4 v11, s[44:47], s65 offen offset:3072 sc1 lds\n"
        "v_exp_f32 v72, v72\n"
        "v_exp_f32 v73, v73\n"
        "v_cvt_pk_bf16_f32 v99, v70, v71\n"
        "v_mfma_f32_32x32x16_bf16 a[96:111], v[104:107], v[144:147], a[96:111]\n"
        "buffer_store_dwordx4 v[236:239], v27, s[36:39], s67 offen sc1\n"
        "v_exp_f32 v74, v74\n"
        "v_exp_f32 v75, v75\n"
        "v_cvt_pk_bf16_f32 v100, v72, v73\n"
        "v_mfma_f32_32x32x16_bf16 a[112:127], v[104:107], v[148:151], a[112:127]\n"
        "s_add_u32 s65, s65, s66\n"
        "v_exp_f32 v76, v76\n"
        "v_exp_f32 v77, v77\n"
        "v_cvt_pk_bf16_f32 v101, v74, v75\n"
        "v_cmp_eq_u32 vcc, 0, v233\n"
        "v_mfma_f32_32x32x16_bf16 a[96:111], v[108:111], v[152:155], a[96:111]\n"
        "v_exp_f32 v78, v78\n"
        "v_exp_f32 v79, v79\n"
        "v_cvt_pk_bf16_f32 v102, v76, v77\n"
        "v_cndmask_b32 v250, 0, v232, vcc\n"
        "v_mfma_f32_32x32x16_bf16 a[112:127], v[108:111], v[156:159], a[112:127]\n"
        "v_exp_f32 v80, v80\n"
        "v_exp_f32 v81, v81\n"
        "v_cvt_pk_bf16_f32 v103, v78, v79\n"
        "v_accvgpr_write_b32 a240, v250\n"
        "v_mfma_f32_32x32x16_bf16 v[64:79], v[128:131], a[176:179], 0\n"
        "buffer_store_dwordx4 v[236:239], v28, s[36:39], s68 offen sc1\n"
        "v_exp_f32 v82, v82\n"
        "v_exp_f32 v83, v83\n"
        "v_cvt_pk_bf16_f32 v104, v80, v81\n"
        "v_mfma_f32_32x32x16_bf16 v[64:79], v[132:135], a[180:183], v[64:79]\n"
        "v_exp_f32 v84, v84\n"
        "v_exp_f32 v85, v85\n"
        "v_cvt_pk_bf16_f32 v105, v82, v83\n"
        "v_accvgpr_write_b32 a242, v250\n"
        "v_mfma_f32_32x32x16_bf16 v[64:79], v[136:139], a[184:187], v[64:79]\n"
        "v_exp_f32 v86, v86\n"
        "v_exp_f32 v87, v87\n"
        "v_cvt_pk_bf16_f32 v106, v84, v85\n"
        "v_cmp_eq_u32 vcc, 1, v233\n"
        "v_mfma_f32_32x32x16_bf16 v[64:79], v[140:143], a[188:191], v[64:79]\n"
        "v_exp_f32 v88, v88\n"
        "v_exp_f32 v89, v89\n"
        "v_cvt_pk_bf16_f32 v107, v86, v87\n"
        "v_cndmask_b32 v250, 0, v232, vcc\n"
        "v_mfma_f32_32x32x16_bf16 a[128:143], v[96:99], v[144:147], a[128:143]\n"
        "v_exp_f32 v90, v90\n"
        "v_exp_f32 v91, v91\n"
        "v_cvt_pk_bf16_f32 v108, v88, v89\n"
        "v_accvgpr_write_b32 a241, v250\n"
        "v_mfma_f32_32x32x16_bf16 a[144:159], v[96:99], v[148:151], a[144:159]\n"
        "v_exp_f32 v92, v92\n"
        "v_exp_f32 v93, v93\n"
        "v_cvt_pk_bf16_f32 v109, v90, v91\n"
        "v_accvgpr_write_b32 a243, v250\n"
        "v_mfma_f32_32x32x16_bf16 a[128:143], v[100:103], v[152:155], a[128:143]\n"
        "v_exp_f32 v94, v94\n"
        "v_exp_f32 v95, v95\n"
        "v_cvt_pk_bf16_f32 v110, v92, v93\n"
        "v_cmp_eq_u32 vcc, 2, v233\n"
        "v_mfma_f32_32x32x16_bf16 a[144:159], v[100:103], v[156:159], a[144:159]\n"
        "v_exp_f32 v64, v64\n"
        "v_exp_f32 v65, v65\n"
        "v_cvt_pk_bf16_f32 v111, v94, v95\n"
        "v_cndmask_b32 v250, 0, v232, vcc\n"
        "v_mfma_f32_32x32x16_bf16 v[80:95], v[128:131], a[192:195], 0\n"
        "ds_read_b128 v[112:115], v12 offset:0\n"
        "v_exp_f32 v66, v66\n"
        "v_exp_f32 v67, v67\n"
        "v_cvt_pk_bf16_f32 v96, v64, v65\n"
        "v_mfma_f32_32x32x16_bf16 v[80:95], v[132:135], a[196:199], v[80:95]\n"
        "ds_read_b128 v[116:119], v13 offset:0\n"
        "v_exp_f32 v68, v68\n"
        "v_exp_f32 v69, v69\n"
        "v_cvt_pk_bf16_f32 v97, v66, v67\n"
        "v_mfma_f32_32x32x16_bf16 v[80:95], v[136:139], a[200:203], v[80:95]\n"
        "ds_read_b128 v[120:123], v14 offset:0\n"
        "v_exp_f32 v70, v70\n"
        "v_exp_f32 v71, v71\n"
        "v_cvt_pk_bf16_f32 v98, v68, v69\n"
        "v_mfma_f32_32x32x16_bf16 v[80:95], v[140:143], a[204:207], v[80:95]\n"
        "ds_read_b128 v[124:127], v15 offset:0\n"
        "v_exp_f32 v72, v72\n"
        "v_exp_f32 v73, v73\n"
        "v_cvt_pk_bf16_f32 v99, v70, v71\n"
        "v_mfma_f32_32x32x16_bf16 a[0:15], v[104:107], v[160:163], a[0:15]\n"
        "ds_read_b64_tr_b16 v[144:145], v16 offset:0\n"
        "v_exp_f32 v74, v74\n"
        "v_exp_f32 v75, v75\n"
        "v_cvt_pk_bf16_f32 v100, v72, v73\n"
        "v_mfma_f32_32x32x16_bf16 a[16:31], v[104:107], v[164:167], a[16:31]\n"
        "ds_read_b64_tr_b16 v[146:147], v17 offset:0\n"
        "v_exp_f32 v76, v76\n"
        "v_exp_f32 v77, v77\n"
        "v_cvt_pk_bf16_f32 v101, v74, v75\n"
        "v_mfma_f32_32x32x16_bf16 a[0:15], v[108:111], v[168:171], a[0:15]\n"
        "ds_read_b64_tr_b16 v[148:149], v18 offset:0\n"
        "v_exp_f32 v78, v78\n"
        "v_exp_f32 v79, v79\n"
        "v_cvt_pk_bf16_f32 v102, v76, v77\n"
        "v_mfma_f32_32x32x16_bf16 a[16:31], v[108:111], v[172:175], a[16:31]\n"
        "ds_read_b64_tr_b16 v[150:151], v19 offset:0\n"
        "v_exp_f32 v80, v80\n"
        "v_exp_f32 v81, v81\n"
        "v_cvt_pk_bf16_f32 v103, v78, v79\n"
        "v_mfma_f32_32x32x16_bf16 v[64:79], v[128:131], a[208:211], 0\n"
        "ds_read_b64_tr_b16 v[152:153], v16 offset:2048\n"
        "v_exp_f32 v82, v82\n"
        "v_exp_f32 v83, v83\n"
        "v_cvt_pk_bf16_f32 v104, v80, v81\n"
        "v_mfma_f32_32x32x16_bf16 v[64:79], v[132:135], a[212:215], v[64:79]\n"
        "ds_read_b64_tr_b16 v[154:155], v17 offset:2048\n"
        "v_exp_f32 v84, v84\n"
        "v_exp_f32 v85, v85\n"
        "v_cvt_pk_bf16_f32 v105, v82, v83\n"
        "v_mfma_f32_32x32x16_bf16 v[64:79], v[136:139], a[216:219], v[64:79]\n"
        "ds_read_b64_tr_b16 v[156:157], v18 offset:2048\n"
        "v_exp_f32 v86, v86\n"
        "v_exp_f32 v87, v87\n"
        "v_cvt_pk_bf16_f32 v106, v84, v85\n"
        "v_mfma_f32_32x32x16_bf16 v[64:79], v[140:143], a[220:223], v[64:79]\n"
        "ds_read_b64_tr_b16 v[158:159], v19 offset:2048\n"
        "v_exp_f32 v88, v88\n"
        "v_exp_f32 v89, v89\n"
        "v_cvt_pk_bf16_f32 v107, v86, v87\n"
        "v_mfma_f32_32x32x16_bf16 a[32:47], v[96:99], v[160:163], a[32:47]\n"
        "v_exp_f32 v90, v90\n"
        "v_exp_f32 v91, v91\n"
        "v_cvt_pk_bf16_f32 v108, v88, v89\n"
        "v_accvgpr_write_b32 a244, v250\n"
        "v_mfma_f32_32x32x16_bf16 a[48:63], v[96:99], v[164:167], a[48:63]\n"
        "v_exp_f32 v92, v92\n"
        "v_exp_f32 v93, v93\n"
        "v_cvt_pk_bf16_f32 v109, v90, v91\n"
        "v_accvgpr_write_b32 a246, v250\n"
        "v_mfma_f32_32x32x16_bf16 a[32:47], v[100:103], v[168:171], a[32:47]\n"
        "v_exp_f32 v94, v94\n"
        "v_exp_f32 v95, v95\n"
        "v_cvt_pk_bf16_f32 v110, v92, v93\n"
        "v_cmp_eq_u32 vcc, 3, v233\n"
        "v_mfma_f32_32x32x16_bf16 a[48:63], v[100:103], v[172:175], a[48:63]\n"
        "v_exp_f32 v64, v64\n"
        "v_exp_f32 v65, v65\n"
        "v_cvt_pk_bf16_f32 v111, v94, v95\n"
        "v_cndmask_b32 v250, 0, v232, vcc\n"
        "v_mfma_f32_32x32x16_bf16 v[80:95], v[128:131], a[224:227], 0\n"
        "v_exp_f32 v66, v66\n"
        "v_exp_f32 v67, v67\n"
        "v_cvt_pk_bf16_f32 v96, v64, v65\n"
        "v_accvgpr_write_b32 a245, v250\n"
        "v_mfma_f32_32x32x16_bf16 v[80:95], v[132:135], a[228:231], v[80:95]\n"
        "v_exp_f32 v68, v68\n"
        "v_exp_f32 v69, v69\n"
        "v_cvt_pk_bf16_f32 v97, v66, v67\n"
        "v_accvgpr_write_b32 a247, v250\n"
        "v_mfma_f32_32x32x16_bf16 v[80:95], v[136:139], a[232:235], v[80:95]\n"
        "v_exp_f32 v70, v70\n"
        "v_exp_f32 v71, v71\n"
        "v_cvt_pk_bf16_f32 v98, v68, v69\n"
        "v_and_b32 v232, 1, v9\n"
        "v_mfma_f32_32x32x16_bf16 v[80:95], v[140:143], a[236:239], v[80:95]\n"
        "v_exp_f32 v72, v72\n"
        "v_exp_f32 v73, v73\n"
        "v_cvt_pk_bf16_f32 v99, v70, v71\n"
        "v_lshlrev_b32 v232, 4, v232\n"
        "v_mfma_f32_32x32x16_bf16 a[64:79], v[104:107], v[160:163], a[64:79]\n"
        "v_exp_f32 v74, v74\n"
        "v_exp_f32 v75, v75\n"
        "v_cvt_pk_bf16_f32 v100, v72, v73\n"
        "v_lshlrev_b32 v232, v232, v234\n"
        "v_mfma_f32_32x32x16_bf16 a[80:95], v[104:107], v[164:167], a[80:95]\n"
        "v_exp_f32 v76, v76\n"
        "v_exp_f32 v77, v77\n"
        "v_cvt_pk_bf16_f32 v101, v74, v75\n"
        "v_and_b32 v233, 2, v9\n"
        "v_mfma_f32_32x32x16_bf16 a[64:79], v[108:111], v[168:171], a[64:79]\n"
        "s_waitcnt lgkmcnt(0)\n"
        "v_exp_f32 v78, v78\n"
        "v_exp_f32 v79, v79\n"
        "v_cvt_pk_bf16_f32 v102, v76, v77\n"
        "v_cmp_eq_u32 vcc, 0, v233\n"
        "v_mfma_f32_32x32x16_bf16 a[80:95], v[108:111], v[172:175], a[80:95]\n"
        "s_add_u32 m0, s62, 0\n"
        "v_exp_f32 v80, v80\n"
        "v_exp_f32 v81, v81\n"
        "v_cvt_pk_bf16_f32 v103, v78, v79\n"
        "v_cndmask_b32 v250, 0, v232, vcc\n"
        "v_mfma_f32_32x32x16_bf16 v[64:79], v[112:115], a[160:163], 0\n"
        "buffer_load_dwordx4 v10, s[44:47], s65 offen offset:0 sc1 lds\n"
        "v_exp_f32 v82, v82\n"
        "v_exp_f32 v83, v83\n"
        "v_cvt_pk_bf16_f32 v104, v80, v81\n"
        "v_mfma_f32_32x32x16_bf16 v[64:79], v[116:119], a[164:167], v[64:79]\n"
        "buffer_load_dwordx4 v11, s[44:47], s65 offen offset:1024 sc1 lds\n"
        "v_exp_f32 v84, v84\n"
        "v_exp_f32 v85, v85\n"
        "v_cvt_pk_bf16_f32 v105, v82, v83\n"
        "v_mfma_f32_32x32x16_bf16 v[64:79], v[120:123], a[168:171], v[64:79]\n"
        "buffer_load_dwordx4 v10, s[44:47], s65 offen offset:2048 sc1 lds\n"
        "v_exp_f32 v86, v86\n"
        "v_exp_f32 v87, v87\n"
        "v_cvt_pk_bf16_f32 v106, v84, v85\n"
        "v_mfma_f32_32x32x16_bf16 v[64:79], v[124:127], a[172:175], v[64:79]\n"
        "buffer_load_dwordx4 v11, s[44:47], s65 offen offset:3072 sc1 lds\n"
        "v_exp_f32 v88, v88\n"
        "v_exp_f32 v89, v89\n"
        "v_cvt_pk_bf16_f32 v107, v86, v87\n"
        "v_mfma_f32_32x32x16_bf16 a[96:111], v[96:99], v[160:163], a[96:111]\n"
        "v_exp_f32 v90, v90\n"
        "v_exp_f32 v91, v91\n"
        "v_cvt_pk_bf16_f32 v108, v88, v89\n"
        "v_accvgpr_write_b32 a252, v250\n"
        "v_mfma_f32_32x32x16_bf16 a[112:127], v[96:99], v[164:167], a[112:127]\n"
        "s_add_u32 s65, s65, s66\n"
        "v_exp_f32 v92, v92\n"
        "v_exp_f32 v93, v93\n"
        "v_cvt_pk_bf16_f32 v109, v90, v91\n"
        "v_xor_b32 v250, v250, v232\n"
        "v_mfma_f32_32x32x16_bf16 a[96:111], v[100:103], v[168:171], a[96:111]\n"
        "v_exp_f32 v94, v94\n"
        "v_exp_f32 v95, v95\n"
        "v_cvt_pk_bf16_f32 v110, v92, v93\n"
        "v_accvgpr_write_b32 a253, v250\n"
        "v_mfma_f32_32x32x16_bf16 a[112:127], v[100:103], v[172:175], a[112:127]\n"
        "s_lshl_b32 s77, s58, 7\n"
        "v_exp_f32 v64, v64\n"
        "v_exp_f32 v65, v65\n"
        "v_cvt_pk_bf16_f32 v111, v94, v95\n"
        "v_mfma_f32_32x32x16_bf16 v[80:95], v[112:115], a[176:179], 0\n"
        "s_add_u32 s77, s77, 1280064\n"
        "v_exp_f32 v66, v66\n"
        "v_exp_f32 v67, v67\n"
        "v_cvt_pk_bf16_f32 v96, v64, v65\n"
        "v_mov_b32 v232, s77\n"
        "v_mfma_f32_32x32x16_bf16 v[80:95], v[116:119], a[180:183], v[80:95]\n"
        "v_exp_f32 v68, v68\n"
        "v_exp_f32 v69, v69\n"
        "v_cvt_pk_bf16_f32 v97, v66, v67\n"
        "v_cmp_eq_u32 vcc, 0, v8\n"
        "v_mfma_f32_32x32x16_bf16 v[80:95], v[120:123], a[184:187], v[80:95]\n"
        "v_exp_f32 v70, v70\n"
        "v_exp_f32 v71, v71\n"
        "v_cvt_pk_bf16_f32 v98, v68, v69\n"
        "v_cndmask_b32 v21, v235, v232, vcc\n"
        "v_mfma_f32_32x32x16_bf16 v[80:95], v[124:127], a[188:191], v[80:95]\n"
        "v_exp_f32 v72, v72\n"
        "v_exp_f32 v73, v73\n"
        "v_cvt_pk_bf16_f32 v99, v70, v71\n"
        "v_and_or_b32 v232, v9, 3, s75\n"
        "v_mfma_f32_32x32x16_bf16 a[128:143], v[104:107], v[160:163], a[128:143]\n"
        "v_exp_f32 v74, v74\n"
        "v_exp_f32 v75, v75\n"
        "v_cvt_pk_bf16_f32 v100, v72, v73\n"
        "v_lshlrev_b32 v232, 7, v232\n"
        "v_mfma_f32_32x32x16_bf16 a[144:159], v[104:107], v[164:167], a[144:159]\n"
        "v_exp_f32 v76, v76\n"
        "v_exp_f32 v77, v77\n"
        "v_cvt_pk_bf16_f32 v101, v74, v75\n"
        "v_add_u32 v45, 1280064, v232\n"
        "v_mfma_f32_32x32x16_bf16 a[128:143], v[108:111], v[168:171], a[128:143]\n"
        "s_lshl_b32 s77, s59, 12\n"
        "v_exp_f32 v78, v78\n"
        "v_exp_f32 v79, v79\n"
        "v_cvt_pk_bf16_f32 v102, v76, v77\n"
        "v_mfma_f32_32x32x16_bf16 a[144:159], v[108:111], v[172:175], a[144:159]\n"
        "s_add_u32 s77, s77, 1312512\n"
        "v_exp_f32 v80, v80\n"
        "v_exp_f32 v81, v81\n"
        "v_cvt_pk_bf16_f32 v103, v78, v79\n"
        "v_add_u32 v38, s77, v248\n"
        "v_mfma_f32_32x32x16_bf16 v[64:79], v[112:115], a[192:195], 0\n"
        "ds_read_b128 v[128:131], v12 offset:4096\n"
        "v_exp_f32 v82, v82\n"
        "v_exp_f32 v83, v83\n"
        "v_cvt_pk_bf16_f32 v104, v80, v81\n"
        "v_mfma_f32_32x32x16_bf16 v[64:79], v[116:119], a[196:199], v[64:79]\n"
        "ds_read_b128 v[132:135], v13 offset:4096\n"
        "v_exp_f32 v84, v84\n"
        "v_exp_f32 v85, v85\n"
        "v_cvt_pk_bf16_f32 v105, v82, v83\n"
        "v_mfma_f32_32x32x16_bf16 v[64:79], v[120:123], a[200:203], v[64:79]\n"
        "ds_read_b128 v[136:139], v14 offset:4096\n"
        "v_exp_f32 v86, v86\n"
        "v_exp_f32 v87, v87\n"
        "v_cvt_pk_bf16_f32 v106, v84, v85\n"
        "v_mfma_f32_32x32x16_bf16 v[64:79], v[124:127], a[204:207], v[64:79]\n"
        "ds_read_b128 v[140:143], v15 offset:4096\n"
        "v_exp_f32 v88, v88\n"
        "v_exp_f32 v89, v89\n"
        "v_cvt_pk_bf16_f32 v107, v86, v87\n"
        "v_mfma_f32_32x32x16_bf16 a[0:15], v[96:99], v[144:147], a[0:15]\n"
        "ds_read_b64_tr_b16 v[160:161], v16 offset:4096\n"
        "v_exp_f32 v90, v90\n"
        "v_exp_f32 v91, v91\n"
        "v_cvt_pk_bf16_f32 v108, v88, v89\n"
        "v_mfma_f32_32x32x16_bf16 a[16:31], v[96:99], v[148:151], a[16:31]\n"
        "ds_read_b64_tr_b16 v[162:163], v17 offset:4096\n"
        "v_exp_f32 v92, v92\n"
        "v_exp_f32 v93, v93\n"
        "v_cvt_pk_bf16_f32 v109, v90, v91\n"
        "v_mfma_f32_32x32x16_bf16 a[0:15], v[100:103], v[152:155], a[0:15]\n"
        "ds_read_b64_tr_b16 v[164:165], v18 offset:4096\n"
        "v_exp_f32 v94, v94\n"
        "v_exp_f32 v95, v95\n"
        "v_cvt_pk_bf16_f32 v110, v92, v93\n"
        "v_mfma_f32_32x32x16_bf16 a[16:31], v[100:103], v[156:159], a[16:31]\n"
        "ds_read_b64_tr_b16 v[166:167], v19 offset:4096\n"
        "v_exp_f32 v64, v64\n"
        "v_exp_f32 v65, v65\n"
        "v_cvt_pk_bf16_f32 v111, v94, v95\n"
        "v_mfma_f32_32x32x16_bf16 v[80:95], v[112:115], a[208:211], 0\n"
        "ds_read_b64_tr_b16 v[168:169], v16 offset:6144\n"
        "v_exp_f32 v66, v66\n"
        "v_exp_f32 v67, v67\n"
        "v_cvt_pk_bf16_f32 v96, v64, v65\n"
        "v_mfma_f32_32x32x16_bf16 v[80:95], v[116:119], a[212:215], v[80:95]\n"
        "ds_read_b64_tr_b16 v[170:171], v17 offset:6144\n"
        "v_exp_f32 v68, v68\n"
        "v_exp_f32 v69, v69\n"
        "v_cvt_pk_bf16_f32 v97, v66, v67\n"
        "v_mfma_f32_32x32x16_bf16 v[80:95], v[120:123], a[216:219], v[80:95]\n"
        "ds_read_b64_tr_b16 v[172:173], v18 offset:6144\n"
        "v_exp_f32 v70, v70\n"
        "v_exp_f32 v71, v71\n"
        "v_cvt_pk_bf16_f32 v98, v68, v69\n"
        "v_mfma_f32_32x32x16_bf16 v[80:95], v[124:127], a[220:223], v[80:95]\n"
        "ds_read_b64_tr_b16 v[174:175], v19 offset:6144\n"
        "v_exp_f32 v72, v72\n"
        "v_exp_f32 v73, v73\n"
        "v_cvt_pk_bf16_f32 v99, v70, v71\n"
        "v_mfma_f32_32x32x16_bf16 a[32:47], v[104:107], v[144:147], a[32:47]\n"
        "v_exp_f32 v74, v74\n"
        "v_exp_f32 v75, v75\n"
        "v_cvt_pk_bf16_f32 v100, v72, v73\n"
        "v_xor_b32 v232, v247, v60\n"
        "v_mfma_f32_32x32x16_bf16 a[48:63], v[104:107], v[148:151], a[48:63]\n"
        "v_exp_f32 v76, v76\n"
        "v_exp_f32 v77, v77\n"
        "v_cvt_pk_bf16_f32 v101, v74, v75\n"
        "v_lshlrev_b32 v232, 4, v232\n"
        "v_mfma_f32_32x32x16_bf16 a[32:47], v[108:111], v[152:155], a[32:47]\n"
        "v_exp_f32 v78, v78\n"
        "v_exp_f32 v79, v79\n"
        "v_cvt_pk_bf16_f32 v102, v76, v77\n"
        "v_lshl_add_u32 v232, v246, 7, v232\n"
        "v_mfma_f32_32x32x16_bf16 a[48:63], v[108:111], v[156:159], a[48:63]\n"
        "s_mul_i32 s77, s59, 1088\n"
        "v_exp_f32 v80, v80\n"
        "v_exp_f32 v81, v81\n"
        "v_cvt_pk_bf16_f32 v103, v78, v79\n"
        "v_xor_b32 v232, s77, v232\n"
        "v_mfma_f32_32x32x16_bf16 v[64:79], v[112:115], a[224:227], 0\n"
        "v_exp_f32 v82, v82\n"
        "v_exp_f32 v83, v83\n"
        "v_cvt_pk_bf16_f32 v104, v80, v81\n"
        "v_add_u32 v232, 1277952, v232\n"
        "v_mfma_f32_32x32x16_bf16 v[64:79], v[116:119], a[228:231], v[64:79]\n"
        "v_exp_f32 v84, v84\n"
        "v_exp_f32 v85, v85\n"
        "v_cvt_pk_bf16_f32 v105, v82, v83\n"
        "v_cmp_gt_u32 vcc, 128, v8\n"
        "v_mfma_f32_32x32x16_bf16 v[64:79], v[120:123], a[232:235], v[64:79]\n"
        "v_exp_f32 v86, v86\n"
        "v_exp_f32 v87, v87\n"
        "v_cvt_pk_bf16_f32 v106, v84, v85\n"
        "v_add_u32 v233, 1326848, v249\n"
        "v_mfma_f32_32x32x16_bf16 v[64:79], v[124:127], a[236:239], v[64:79]\n"
        "v_exp_f32 v88, v88\n"
        "v_exp_f32 v89, v89\n"
        "v_cvt_pk_bf16_f32 v107, v86, v87\n"
        "v_cndmask_b32 v56, v233, v232, vcc\n"
        "v_mfma_f32_32x32x16_bf16 a[64:79], v[96:99], v[144:147], a[64:79]\n"
        "ds_read_b128 v[46:49], v20 offset:24576\n"
        "v_exp_f32 v90, v90\n"
        "v_exp_f32 v91, v91\n"
        "v_cvt_pk_bf16_f32 v108, v88, v89\n"
        "v_mfma_f32_32x32x16_bf16 a[80:95], v[96:99], v[148:151], a[80:95]\n"
        "ds_read_b128 v[50:53], v20 offset:20480\n"
        "v_exp_f32 v92, v92\n"
        "v_exp_f32 v93, v93\n"
        "v_cvt_pk_bf16_f32 v109, v90, v91\n"
        "v_mfma_f32_32x32x16_bf16 a[64:79], v[100:103], v[152:155], a[64:79]\n"
        "s_waitcnt lgkmcnt(2)\n"
        "v_exp_f32 v94, v94\n"
        "v_exp_f32 v95, v95\n"
        "v_cvt_pk_bf16_f32 v110, v92, v93\n"
        "v_mfma_f32_32x32x16_bf16 a[80:95], v[100:103], v[156:159], a[80:95]\n"
        "s_add_u32 m0, s62, 4096\n"
        "v_exp_f32 v64, v64\n"
        "v_exp_f32 v65, v65\n"
        "v_cvt_pk_bf16_f32 v111, v94, v95\n"
        "v_mfma_f32_32x32x16_bf16 v[80:95], v[128:131], a[160:163], 0\n"
        "buffer_load_dwordx4 v10, s[44:47], s65 offen offset:0 sc1 lds\n"
        "v_exp_f32 v66, v66\n"
        "v_exp_f32 v67, v67\n"
        "v_cvt_pk_bf16_f32 v96, v64, v65\n"
        "v_mfma_f32_32x32x16_bf16 v[80:95], v[132:135], a[164:167], v[80:95]\n"
        "buffer_load_dwordx4 v11, s[44:47], s65 offen offset:1024 sc1 lds\n"
        "v_exp_f32 v68, v68\n"
        "v_exp_f32 v69, v69\n"
        "v_cvt_pk_bf16_f32 v97, v66, v67\n"
        "v_mfma_f32_32x32x16_bf16 v[80:95], v[136:139], a[168:171], v[80:95]\n"
        "buffer_load_dwordx4 v10, s[44:47], s65 offen offset:2048 sc1 lds\n"
        "v_exp_f32 v70, v70\n"
        "v_exp_f32 v71, v71\n"
        "v_cvt_pk_bf16_f32 v98, v68, v69\n"
        "v_mfma_f32_32x32x16_bf16 v[80:95], v[140:143], a[172:175], v[80:95]\n"
        "buffer_load_dwordx4 v11, s[44:47], s65 offen offset:3072 sc1 lds\n"
        "v_exp_f32 v72, v72\n"
        "v_exp_f32 v73, v73\n"
        "v_cvt_pk_bf16_f32 v99, v70, v71\n"
        "v_mfma_f32_32x32x16_bf16 a[96:111], v[104:107], v[144:147], a[96:111]\n"
        "buffer_load_dwordx2 v[208:209], v32, s[36:39], 0 offen sc1\n"
        "v_exp_f32 v74, v74\n"
        "v_exp_f32 v75, v75\n"
        "v_cvt_pk_bf16_f32 v100, v72, v73\n"
        "v_mfma_f32_32x32x16_bf16 a[112:127], v[104:107], v[148:151], a[112:127]\n"
        "s_add_u32 s65, s65, s66\n"
        "s_waitcnt lgkmcnt(1)\n"
        "ds_write_b128 v55, v[46:49]\n"
        "v_exp_f32 v76, v76\n"
        "v_exp_f32 v77, v77\n"
        "v_cvt_pk_bf16_f32 v101, v74, v75\n"
        "v_mfma_f32_32x32x16_bf16 a[96:111], v[108:111], v[152:155], a[96:111]\n"
        "v_exp_f32 v78, v78\n"
        "v_exp_f32 v79, v79\n"
        "v_cvt_pk_bf16_f32 v102, v76, v77\n"
        "v_mfma_f32_32x32x16_bf16 a[112:127], v[108:111], v[156:159], a[112:127]\n"
        "v_exp_f32 v80, v80\n"
        "v_exp_f32 v81, v81\n"
        "v_cvt_pk_bf16_f32 v103, v78, v79\n"
        "v_mfma_f32_32x32x16_bf16 v[64:79], v[128:131], a[176:179], 0\n"
        "s_waitcnt lgkmcnt(1)\n"
        "ds_write_b128 v55, v[50:53] offset:4608\n"
        "v_exp_f32 v82, v82\n"
        "v_exp_f32 v83, v83\n"
        "v_cvt_pk_bf16_f32 v104, v80, v81\n"
        "v_mfma_f32_32x32x16_bf16 v[64:79], v[132:135], a[180:183], v[64:79]\n"
        "s_mul_i32 s78, s59, 10\n"
        "v_exp_f32 v84, v84\n"
        "v_exp_f32 v85, v85\n"
        "v_cvt_pk_bf16_f32 v105, v82, v83\n"
        "v_mfma_f32_32x32x16_bf16 v[64:79], v[136:139], a[184:187], v[64:79]\n"
        "s_and_b32 s84, s78, -4\n"
        "v_exp_f32 v86, v86\n"
        "v_exp_f32 v87, v87\n"
        "v_cvt_pk_bf16_f32 v106, v84, v85\n"
        "v_mfma_f32_32x32x16_bf16 v[64:79], v[140:143], a[188:191], v[64:79]\n"
        "s_and_b32 s79, s78, 2\n"
        "v_exp_f32 v88, v88\n"
        "v_exp_f32 v89, v89\n"
        "v_cvt_pk_bf16_f32 v107, v86, v87\n"
        "v_mfma_f32_32x32x16_bf16 a[128:143], v[96:99], v[144:147], a[128:143]\n"
        "s_mul_i32 s77, s58, 40\n"
        "v_exp_f32 v90, v90\n"
        "v_exp_f32 v91, v91\n"
        "v_cvt_pk_bf16_f32 v108, v88, v89\n"
        "v_mfma_f32_32x32x16_bf16 a[144:159], v[96:99], v[148:151], a[144:159]\n"
        "s_add_u32 s77, s77, s84\n"
        "v_exp_f32 v92, v92\n"
        "v_exp_f32 v93, v93\n"
        "v_cvt_pk_bf16_f32 v109, v90, v91\n"
        "v_lshl_add_u32 v232, v60, 2, s77\n"
        "v_mfma_f32_32x32x16_bf16 a[128:143], v[100:103], v[152:155], a[128:143]\n"
        "v_exp_f32 v94, v94\n"
        "v_exp_f32 v95, v95\n"
        "v_cvt_pk_bf16_f32 v110, v92, v93\n"
        "v_min_u32 v48, 9996, v232\n"
        "v_mfma_f32_32x32x16_bf16 a[144:159], v[100:103], v[156:159], a[144:159]\n"
        "v_exp_f32 v64, v64\n"
        "v_exp_f32 v65, v65\n"
        "v_cvt_pk_bf16_f32 v111, v94, v95\n"
        "v_lshlrev_b32 v48, 1, v48\n"
        "v_mfma_f32_32x32x16_bf16 v[80:95], v[128:131], a[192:195], 0\n"
        "s_waitcnt vmcnt(18)\n"
        "ds_read_b128 v[112:115], v12 offset:8192\n"
        "v_exp_f32 v66, v66\n"
        "v_exp_f32 v67, v67\n"
        "v_cvt_pk_bf16_f32 v96, v64, v65\n"
        "v_mfma_f32_32x32x16_bf16 v[80:95], v[132:135], a[196:199], v[80:95]\n"
        "ds_read_b128 v[116:119], v13 offset:8192\n"
        "v_exp_f32 v68, v68\n"
        "v_exp_f32 v69, v69\n"
        "v_cvt_pk_bf16_f32 v97, v66, v67\n"
        "v_mfma_f32_32x32x16_bf16 v[80:95], v[136:139], a[200:203], v[80:95]\n"
        "ds_read_b128 v[120:123], v14 offset:8192\n"
        "v_exp_f32 v70, v70\n"
        "v_exp_f32 v71, v71\n"
        "v_cvt_pk_bf16_f32 v98, v68, v69\n"
        "v_mfma_f32_32x32x16_bf16 v[80:95], v[140:143], a[204:207], v[80:95]\n"
        "ds_read_b128 v[124:127], v15 offset:8192\n"
        "v_exp_f32 v72, v72\n"
        "v_exp_f32 v73, v73\n"
        "v_cvt_pk_bf16_f32 v99, v70, v71\n"
        "v_mfma_f32_32x32x16_bf16 a[0:15], v[104:107], v[160:163], a[0:15]\n"
        "ds_read_b64_tr_b16 v[144:145], v16 offset:8192\n"
        "v_exp_f32 v74, v74\n"
        "v_exp_f32 v75, v75\n"
        "v_cvt_pk_bf16_f32 v100, v72, v73\n"
        "v_mfma_f32_32x32x16_bf16 a[16:31], v[104:107], v[164:167], a[16:31]\n"
        "ds_read_b64_tr_b16 v[146:147], v17 offset:8192\n"
        "v_exp_f32 v76, v76\n"
        "v_exp_f32 v77, v77\n"
        "v_cvt_pk_bf16_f32 v101, v74, v75\n"
        "v_mfma_f32_32x32x16_bf16 a[0:15], v[108:111], v[168:171], a[0:15]\n"
        "ds_read_b64_tr_b16 v[148:149], v18 offset:8192\n"
        "v_exp_f32 v78, v78\n"
        "v_exp_f32 v79, v79\n"
        "v_cvt_pk_bf16_f32 v102, v76, v77\n"
        "v_mfma_f32_32x32x16_bf16 a[16:31], v[108:111], v[172:175], a[16:31]\n"
        "ds_read_b64_tr_b16 v[150:151], v19 offset:8192\n"
        "v_exp_f32 v80, v80\n"
        "v_exp_f32 v81, v81\n"
        "v_cvt_pk_bf16_f32 v103, v78, v79\n"
        "v_mfma_f32_32x32x16_bf16 v[64:79], v[128:131], a[208:211], 0\n"
        "ds_read_b64_tr_b16 v[152:153], v16 offset:10240\n"
        "v_exp_f32 v82, v82\n"
        "v_exp_f32 v83, v83\n"
        "v_cvt_pk_bf16_f32 v104, v80, v81\n"
        "v_mfma_f32_32x32x16_bf16 v[64:79], v[132:135], a[212:215], v[64:79]\n"
        "ds_read_b64_tr_b16 v[154:155], v17 offset:10240\n"
        "v_exp_f32 v84, v84\n"
        "v_exp_f32 v85, v85\n"
        "v_cvt_pk_bf16_f32 v105, v82, v83\n"
        "v_mfma_f32_32x32x16_bf16 v[64:79], v[136:139], a[216:219], v[64:79]\n"
        "ds_read_b64_tr_b16 v[156:157], v18 offset:10240\n"
        "v_exp_f32 v86, v86\n"
        "v_exp_f32 v87, v87\n"
        "v_cvt_pk_bf16_f32 v106, v84, v85\n"
        "v_mfma_f32_32x32x16_bf16 v[64:79], v[140:143], a[220:223], v[64:79]\n"
        "ds_read_b64_tr_b16 v[158:159], v19 offset:10240\n"
        "v_exp_f32 v88, v88\n"
        "v_exp_f32 v89, v89\n"
        "v_cvt_pk_bf16_f32 v107, v86, v87\n"
        "v_mfma_f32_32x32x16_bf16 a[32:47], v[96:99], v[160:163], a[32:47]\n"
        "v_exp_f32 v90, v90\n"
        "v_exp_f32 v91, v91\n"
        "v_cvt_pk_bf16_f32 v108, v88, v89\n"
        "v_lshlrev_b32 v232, 7, v232\n"
        "v_mfma_f32_32x32x16_bf16 a[48:63], v[96:99], v[164:167], a[48:63]\n"
        "v_exp_f32 v92, v92\n"
        "v_exp_f32 v93, v93\n"
        "v_cvt_pk_bf16_f32 v109, v90, v91\n"
        "v_lshl_add_u32 v232, v59, 3, v232\n"
        "v_mfma_f32_32x32x16_bf16 a[32:47], v[100:103], v[168:171], a[32:47]\n"
        "v_exp_f32 v94, v94\n"
        "v_exp_f32 v95, v95\n"
        "v_cvt_pk_bf16_f32 v110, v92, v93\n"
        "v_lshlrev_b32 v233, 2, v60\n"
        "v_mfma_f32_32x32x16_bf16 a[48:63], v[100:103], v[172:175], a[48:63]\n"
        "v_exp_f32 v64, v64\n"
        "v_exp_f32 v65, v65\n"
        "v_cvt_pk_bf16_f32 v111, v94, v95\n"
        "v_subrev_u32 v233, s79, v233\n"
        "v_mfma_f32_32x32x16_bf16 v[80:95], v[128:131], a[224:227], 0\n"
        "v_exp_f32 v66, v66\n"
        "v_exp_f32 v67, v67\n"
        "v_cvt_pk_bf16_f32 v96, v64, v65\n"
        "v_cmp_gt_u32 s[86:87], 10, v233\n"
        "v_mfma_f32_32x32x16_bf16 v[80:95], v[132:135], a[228:231], v[80:95]\n"
        "v_exp_f32 v68, v68\n"
        "v_exp_f32 v69, v69\n"
        "v_cvt_pk_bf16_f32 v97, v66, v67\n"
        "v_add_u32 v233, 2, v233\n"
        "v_mfma_f32_32x32x16_bf16 v[80:95], v[136:139], a[232:235], v[80:95]\n"
        "v_exp_f32 v70, v70\n"
        "v_exp_f32 v71, v71\n"
        "v_cvt_pk_bf16_f32 v98, v68, v69\n"
        "v_cndmask_b32 v46, v235, v232, s[86:87]\n"
        "v_mfma_f32_32x32x16_bf16 v[80:95], v[140:143], a[236:239], v[80:95]\n"
        "v_exp_f32 v72, v72\n"
        "v_exp_f32 v73, v73\n"
        "v_cvt_pk_bf16_f32 v99, v70, v71\n"
        "v_cmp_gt_u32 s[86:87], 10, v233\n"
        "v_mfma_f32_32x32x16_bf16 a[64:79], v[104:107], v[160:163], a[64:79]\n"
        "v_exp_f32 v74, v74\n"
        "v_exp_f32 v75, v75\n"
        "v_cvt_pk_bf16_f32 v100, v72, v73\n"
        "v_cndmask_b32 v47, v235, v232, s[86:87]\n"
        "v_mfma_f32_32x32x16_bf16 a[80:95], v[104:107], v[164:167], a[80:95]\n"
        "v_exp_f32 v76, v76\n"
        "v_exp_f32 v77, v77\n"
        "v_cvt_pk_bf16_f32 v101, v74, v75\n"
        "v_bfe_u32 v232, v9, 3, 2\n"
        "v_mfma_f32_32x32x16_bf16 a[64:79], v[108:111], v[168:171], a[64:79]\n"
        "s_waitcnt lgkmcnt(0)\n"
        "v_exp_f32 v78, v78\n"
        "v_exp_f32 v79, v79\n"
        "v_cvt_pk_bf16_f32 v102, v76, v77\n"
        "v_and_b32 v233, 3, v9\n"
        "v_mfma_f32_32x32x16_bf16 a[80:95], v[108:111], v[172:175], a[80:95]\n"
        "s_add_u32 m0, s62, 8192\n"
        "v_exp_f32 v80, v80\n"
        "v_exp_f32 v81, v81\n"
        "v_cvt_pk_bf16_f32 v103, v78, v79\n"
        "v_lshl_add_u32 v232, v232, 2, v233\n"
        "v_mfma_f32_32x32x16_bf16 v[64:79], v[112:115], a[160:163], 0\n"
        "s_waitcnt vmcnt(0)\n"
        "buffer_load_dwordx4 v10, s[44:47], s65 offen offset:0 sc1 lds\n"
        "v_exp_f32 v82, v82\n"
        "v_exp_f32 v83, v83\n"
        "v_cvt_pk_bf16_f32 v104, v80, v81\n"
        "v_mfma_f32_32x32x16_bf16 v[64:79], v[116:119], a[164:167], v[64:79]\n"
        "buffer_load_dwordx4 v11, s[44:47], s65 offen offset:1024 sc1 lds\n"
        "v_exp_f32 v84, v84\n"
        "v_exp_f32 v85, v85\n"
        "v_cvt_pk_bf16_f32 v105, v82, v83\n"
        "v_mfma_f32_32x32x16_bf16 v[64:79], v[120:123], a[168:171], v[64:79]\n"
        "buffer_load_dwordx4 v10, s[44:47], s65 offen offset:2048 sc1 lds\n"
        "v_exp_f32 v86, v86\n"
        "v_exp_f32 v87, v87\n"
        "v_cvt_pk_bf16_f32 v106, v84, v85\n"
        "v_mfma_f32_32x32x16_bf16 v[64:79], v[124:127], a[172:175], v[64:79]\n"
        "buffer_load_dwordx4 v11, s[44:47], s65 offen offset:3072 sc1 lds\n"
        "v_exp_f32 v88, v88\n"
        "v_exp_f32 v89, v89\n"
        "v_cvt_pk_bf16_f32 v107, v86, v87\n"
        "v_mfma_f32_32x32x16_bf16 a[96:111], v[96:99], v[160:163], a[96:111]\n"
        "v_exp_f32 v90, v90\n"
        "v_exp_f32 v91, v91\n"
        "v_cvt_pk_bf16_f32 v108, v88, v89\n"
        "v_lshrrev_b64 v[210:211], 24, v[208:209]\n"
        "v_mfma_f32_32x32x16_bf16 a[112:127], v[96:99], v[164:167], a[112:127]\n"
        "s_add_u32 s65, s65, s66\n"
        "v_exp_f32 v92, v92\n"
        "v_exp_f32 v93, v93\n"
        "v_cvt_pk_bf16_f32 v109, v90, v91\n"
        "v_cmp_ne_u64 vcc, s[54:55], v[210:211]\n"
        "v_mfma_f32_32x32x16_bf16 a[96:111], v[100:103], v[168:171], a[96:111]\n"
        "s_cbranch_vccnz 7f\n"
        "8:\n"
        "v_exp_f32 v94, v94\n"
        "v_exp_f32 v95, v95\n"
        "v_cvt_pk_bf16_f32 v110, v92, v93\n"
        "v_and_b32 v210, 0xfff, v208\n"
        "v_mfma_f32_32x32x16_bf16 a[112:127], v[100:103], v[172:175], a[112:127]\n"
        "v_exp_f32 v64, v64\n"
        "v_exp_f32 v65, v65\n"
        "v_cvt_pk_bf16_f32 v111, v94, v95\n"
        "v_lshl_add_u32 v210, v210, 2, v33\n"
        "v_mfma_f32_32x32x16_bf16 v[80:95], v[112:115], a[176:179], 0\n"
        "v_exp_f32 v66, v66\n"
        "v_exp_f32 v67, v67\n"
        "v_cvt_pk_bf16_f32 v96, v64, v65\n"
        "v_add_u32_dpp v212, v210, v39 quad_perm:[0,0,0,0] row_mask:0xf bank_mask:0xf\n"
        "v_mfma_f32_32x32x16_bf16 v[80:95], v[116:119], a[180:183], v[80:95]\n"
        "buffer_load_dwordx4 v[176:179], v212, s[36:39], 0 offen sc1\n"
        "v_exp_f32 v68, v68\n"
        "v_exp_f32 v69, v69\n"
        "v_cvt_pk_bf16_f32 v97, v66, v67\n"
        "v_mfma_f32_32x32x16_bf16 v[80:95], v[120:123], a[184:187], v[80:95]\n"
        "v_exp_f32 v70, v70\n"
        "v_exp_f32 v71, v71\n"
        "v_cvt_pk_bf16_f32 v98, v68, v69\n"
        "v_bfe_u32 v62, v208, 12, v34\n"
        "v_mfma_f32_32x32x16_bf16 v[80:95], v[124:127], a[188:191], v[80:95]\n"
        "v_exp_f32 v72, v72\n"
        "v_exp_f32 v73, v73\n"
        "v_cvt_pk_bf16_f32 v99, v70, v71\n"
        "v_min_u32 v211, 64, v62\n"
        "v_mfma_f32_32x32x16_bf16 a[128:143], v[104:107], v[160:163], a[128:143]\n"
        "v_exp_f32 v74, v74\n"
        "v_exp_f32 v75, v75\n"
        "v_cvt_pk_bf16_f32 v100, v72, v73\n"
        "v_bfe_u32 v234, v9, 2, 1\n"
        "v_mfma_f32_32x32x16_bf16 a[144:159], v[104:107], v[164:167], a[144:159]\n"
        "v_exp_f32 v76, v76\n"
        "v_exp_f32 v77, v77\n"
        "v_cvt_pk_bf16_f32 v101, v74, v75\n"
        "v_lshl_add_u32 v232, v234, 4, v232\n"
        "v_mfma_f32_32x32x16_bf16 a[128:143], v[108:111], v[168:171], a[128:143]\n"
        "v_exp_f32 v78, v78\n"
        "v_exp_f32 v79, v79\n"
        "v_cvt_pk_bf16_f32 v102, v76, v77\n"
        "v_add_u32_dpp v213, v210, v39 quad_perm:[1,1,1,1] row_mask:0xf bank_mask:0xf\n"
        "v_mfma_f32_32x32x16_bf16 a[144:159], v[108:111], v[172:175], a[144:159]\n"
        "buffer_load_dwordx4 v[180:183], v213, s[36:39], 0 offen sc1\n"
        "v_exp_f32 v80, v80\n"
        "v_exp_f32 v81, v81\n"
        "v_cvt_pk_bf16_f32 v103, v78, v79\n"
        "v_mfma_f32_32x32x16_bf16 v[64:79], v[112:115], a[192:195], 0\n"
        "ds_read_b128 v[128:131], v12 offset:12288\n"
        "v_exp_f32 v82, v82\n"
        "v_exp_f32 v83, v83\n"
        "v_cvt_pk_bf16_f32 v104, v80, v81\n"
        "v_mfma_f32_32x32x16_bf16 v[64:79], v[116:119], a[196:199], v[64:79]\n"
        "ds_read_b128 v[132:135], v13 offset:12288\n"
        "v_exp_f32 v84, v84\n"
        "v_exp_f32 v85, v85\n"
        "v_cvt_pk_bf16_f32 v105, v82, v83\n"
        "v_mfma_f32_32x32x16_bf16 v[64:79], v[120:123], a[200:203], v[64:79]\n"
        "ds_read_b128 v[136:139], v14 offset:12288\n"
        "v_exp_f32 v86, v86\n"
        "v_exp_f32 v87, v87\n"
        "v_cvt_pk_bf16_f32 v106, v84, v85\n"
        "v_mfma_f32_32x32x16_bf16 v[64:79], v[124:127], a[204:207], v[64:79]\n"
        "ds_read_b128 v[140:143], v15 offset:12288\n"
        "v_exp_f32 v88, v88\n"
        "v_exp_f32 v89, v89\n"
        "v_cvt_pk_bf16_f32 v107, v86, v87\n"
        "v_mfma_f32_32x32x16_bf16 a[0:15], v[96:99], v[144:147], a[0:15]\n"
        "ds_read_b64_tr_b16 v[160:161], v16 offset:12288\n"
        "v_exp_f32 v90, v90\n"
        "v_exp_f32 v91, v91\n"
        "v_cvt_pk_bf16_f32 v108, v88, v89\n"
        "v_mfma_f32_32x32x16_bf16 a[16:31], v[96:99], v[148:151], a[16:31]\n"
        "ds_read_b64_tr_b16 v[162:163], v17 offset:12288\n"
        "v_exp_f32 v92, v92\n"
        "v_exp_f32 v93, v93\n"
        "v_cvt_pk_bf16_f32 v109, v90, v91\n"
        "v_mfma_f32_32x32x16_bf16 a[0:15], v[100:103], v[152:155], a[0:15]\n"
        "ds_read_b64_tr_b16 v[164:165], v18 offset:12288\n"
        "v_exp_f32 v94, v94\n"
        "v_exp_f32 v95, v95\n"
        "v_cvt_pk_bf16_f32 v110, v92, v93\n"
        "v_mfma_f32_32x32x16_bf16 a[16:31], v[100:103], v[156:159], a[16:31]\n"
        "ds_read_b64_tr_b16 v[166:167], v19 offset:12288\n"
        "v_exp_f32 v64, v64\n"
        "v_exp_f32 v65, v65\n"
        "v_cvt_pk_bf16_f32 v111, v94, v95\n"
        "v_mfma_f32_32x32x16_bf16 v[80:95], v[112:115], a[208:211], 0\n"
        "ds_read_b64_tr_b16 v[168:169], v16 offset:14336\n"
        "v_exp_f32 v66, v66\n"
        "v_exp_f32 v67, v67\n"
        "v_cvt_pk_bf16_f32 v96, v64, v65\n"
        "v_mfma_f32_32x32x16_bf16 v[80:95], v[116:119], a[212:215], v[80:95]\n"
        "ds_read_b64_tr_b16 v[170:171], v17 offset:14336\n"
        "v_exp_f32 v68, v68\n"
        "v_exp_f32 v69, v69\n"
        "v_cvt_pk_bf16_f32 v97, v66, v67\n"
        "v_mfma_f32_32x32x16_bf16 v[80:95], v[120:123], a[216:219], v[80:95]\n"
        "ds_read_b64_tr_b16 v[172:173], v18 offset:14336\n"
        "v_exp_f32 v70, v70\n"
        "v_exp_f32 v71, v71\n"
        "v_cvt_pk_bf16_f32 v98, v68, v69\n"
        "v_mfma_f32_32x32x16_bf16 v[80:95], v[124:127], a[220:223], v[80:95]\n"
        "ds_read_b64_tr_b16 v[174:175], v19 offset:14336\n"
        "v_exp_f32 v72, v72\n"
        "v_exp_f32 v73, v73\n"
        "v_cvt_pk_bf16_f32 v99, v70, v71\n"
        "v_mfma_f32_32x32x16_bf16 a[32:47], v[104:107], v[144:147], a[32:47]\n"
        "v_exp_f32 v74, v74\n"
        "v_exp_f32 v75, v75\n"
        "v_cvt_pk_bf16_f32 v100, v72, v73\n"
        "v_add_u32 v211, 3, v211\n"
        "v_mfma_f32_32x32x16_bf16 a[48:63], v[104:107], v[148:151], a[48:63]\n"
        "v_exp_f32 v76, v76\n"
        "v_exp_f32 v77, v77\n"
        "v_cvt_pk_bf16_f32 v101, v74, v75\n"
        "v_lshrrev_b32 v211, 2, v211\n"
        "v_mfma_f32_32x32x16_bf16 a[32:47], v[108:111], v[152:155], a[32:47]\n"
        "v_exp_f32 v78, v78\n"
        "v_exp_f32 v79, v79\n"
        "v_cvt_pk_bf16_f32 v102, v76, v77\n"
        "v_add_u32_dpp v212, v210, v39 quad_perm:[2,2,2,2] row_mask:0xf bank_mask:0xf\n"
        "v_mfma_f32_32x32x16_bf16 a[48:63], v[108:111], v[156:159], a[48:63]\n"
        "buffer_load_dwordx4 v[184:187], v212, s[36:39], 0 offen sc1\n"
        "v_exp_f32 v80, v80\n"
        "v_exp_f32 v81, v81\n"
        "v_cvt_pk_bf16_f32 v103, v78, v79\n"
        "v_mfma_f32_32x32x16_bf16 v[64:79], v[112:115], a[224:227], 0\n"
        "v_exp_f32 v82, v82\n"
        "v_exp_f32 v83, v83\n"
        "v_cvt_pk_bf16_f32 v104, v80, v81\n"
        "v_lshlrev_b32 v211, v211, -1\n"
        "v_mfma_f32_32x32x16_bf16 v[64:79], v[116:119], a[228:231], v[64:79]\n"
        "v_exp_f32 v84, v84\n"
        "v_exp_f32 v85, v85\n"
        "v_cvt_pk_bf16_f32 v105, v82, v83\n"
        "v_mul_u32_u24 v232, 96, v232\n"
        "v_mfma_f32_32x32x16_bf16 v[64:79], v[120:123], a[232:235], v[64:79]\n"
        "v_exp_f32 v86, v86\n"
        "v_exp_f32 v87, v87\n"
        "v_cvt_pk_bf16_f32 v106, v84, v85\n"
        "v_lshrrev_b32 v234, 5, v9\n"
        "v_mfma_f32_32x32x16_bf16 v[64:79], v[124:127], a[236:239], v[64:79]\n"
        "v_exp_f32 v88, v88\n"
        "v_exp_f32 v89, v89\n"
        "v_cvt_pk_bf16_f32 v107, v86, v87\n"
        "v_lshl_add_u32 v232, v234, 3, v232\n"
        "v_mfma_f32_32x32x16_bf16 a[64:79], v[96:99], v[144:147], a[64:79]\n"
        "s_lshl_b32 s77, s59, 4\n"
        "v_exp_f32 v90, v90\n"
        "v_exp_f32 v91, v91\n"
        "v_cvt_pk_bf16_f32 v108, v88, v89\n"
        "v_add_u32_dpp v213, v210, v39 quad_perm:[3,3,3,3] row_mask:0xf bank_mask:0xf\n"
        "v_mfma_f32_32x32x16_bf16 a[80:95], v[96:99], v[148:151], a[80:95]\n"
        "s_add_u32 s77, s77, 20480\n"
        "buffer_load_dwordx4 v[188:191], v213, s[36:39], 0 offen sc1\n"
        "v_exp_f32 v92, v92\n"
        "v_exp_f32 v93, v93\n"
        "v_cvt_pk_bf16_f32 v109, v90, v91\n"
        "v_mfma_f32_32x32x16_bf16 a[64:79], v[100:103], v[152:155], a[64:79]\n"
        "s_waitcnt lgkmcnt(0)\n"
        "v_exp_f32 v94, v94\n"
        "v_exp_f32 v95, v95\n"
        "v_cvt_pk_bf16_f32 v110, v92, v93\n"
        "v_add_u32 v4, s77, v232\n"
        "v_mfma_f32_32x32x16_bf16 a[80:95], v[100:103], v[156:159], a[80:95]\n"
        "s_add_u32 m0, s62, 12288\n"
        "v_exp_f32 v64, v64\n"
        "v_exp_f32 v65, v65\n"
        "v_cvt_pk_bf16_f32 v111, v94, v95\n"
        "v_mfma_f32_32x32x16_bf16 v[80:95], v[128:131], a[160:163], 0\n"
        "buffer_load_dwordx4 v10, s[44:47], s65 offen offset:0 sc1 lds\n"
        "v_exp_f32 v66, v66\n"
        "v_exp_f32 v67, v67\n"
        "v_cvt_pk_bf16_f32 v96, v64, v65\n"
        "v_mfma_f32_32x32x16_bf16 v[80:95], v[132:135], a[164:167], v[80:95]\n"
        "buffer_load_dwordx4 v11, s[44:47], s65 offen offset:1024 sc1 lds\n"
        "v_exp_f32 v68, v68\n"
        "v_exp_f32 v69, v69\n"
        "v_cvt_pk_bf16_f32 v97, v66, v67\n"
        "v_mfma_f32_32x32x16_bf16 v[80:95], v[136:139], a[168:171], v[80:95]\n"
        "buffer_load_dwordx4 v10, s[44:47], s65 offen offset:2048 sc1 lds\n"
        "v_exp_f32 v70, v70\n"
        "v_exp_f32 v71, v71\n"
        "v_cvt_pk_bf16_f32 v98, v68, v69\n"
        "v_mfma_f32_32x32x16_bf16 v[80:95], v[140:143], a[172:175], v[80:95]\n"
        "buffer_load_dwordx4 v11, s[44:47], s65 offen offset:3072 sc1 lds\n"
        "v_exp_f32 v72, v72\n"
        "v_exp_f32 v73, v73\n"
        "v_cvt_pk_bf16_f32 v99, v70, v71\n"
        "v_mfma_f32_32x32x16_bf16 a[96:111], v[104:107], v[144:147], a[96:111]\n"
        "s_cmp_eq_u32 s59, 0\n"
        "s_cselect_b32 s77, 0, s83\n"
        "v_exp_f32 v74, v74\n"
        "v_exp_f32 v75, v75\n"
        "v_cvt_pk_bf16_f32 v100, v72, v73\n"
        "v_lshl_or_b32 v212, v211, 27, v210\n"
        "v_mfma_f32_32x32x16_bf16 a[112:127], v[104:107], v[148:151], a[112:127]\n"
        "s_add_u32 s65, s65, s66\n"
        "buffer_load_dwordx4 v[192:195], v212, s[36:39], 0 offen offset:64 sc1\n"
        "v_exp_f32 v76, v76\n"
        "v_exp_f32 v77, v77\n"
        "v_cvt_pk_bf16_f32 v101, v74, v75\n"
        "v_mfma_f32_32x32x16_bf16 a[96:111], v[108:111], v[152:155], a[96:111]\n"
        "v_exp_f32 v78, v78\n"
        "v_exp_f32 v79, v79\n"
        "v_cvt_pk_bf16_f32 v102, v76, v77\n"
        "v_or_b32 v2, s77, v4\n"
        "v_mfma_f32_32x32x16_bf16 a[112:127], v[108:111], v[156:159], a[112:127]\n"
        "v_exp_f32 v80, v80\n"
        "v_exp_f32 v81, v81\n"
        "v_cvt_pk_bf16_f32 v103, v78, v79\n"
        "v_lshl_or_b32 v213, v211, 26, v210\n"
        "v_mfma_f32_32x32x16_bf16 v[64:79], v[128:131], a[176:179], 0\n"
        "buffer_load_dwordx4 v[196:199], v213, s[36:39], 0 offen offset:80 sc1\n"
        "v_exp_f32 v82, v82\n"
        "v_exp_f32 v83, v83\n"
        "v_cvt_pk_bf16_f32 v104, v80, v81\n"
        "v_mfma_f32_32x32x16_bf16 v[64:79], v[132:135], a[180:183], v[64:79]\n"
        "v_exp_f32 v84, v84\n"
        "v_exp_f32 v85, v85\n"
        "v_cvt_pk_bf16_f32 v105, v82, v83\n"
        "v_lshrrev_b32 v232, 2, v9\n"
        "v_mfma_f32_32x32x16_bf16 v[64:79], v[136:139], a[184:187], v[64:79]\n"
        "v_exp_f32 v86, v86\n"
        "v_exp_f32 v87, v87\n"
        "v_cvt_pk_bf16_f32 v106, v84, v85\n"
        "v_lshl_or_b32 v212, v211, 25, v210\n"
        "v_mfma_f32_32x32x16_bf16 v[64:79], v[140:143], a[188:191], v[64:79]\n"
        "buffer_load_dwordx4 v[200:203], v212, s[36:39], 0 offen offset:96 sc1\n"
        "v_exp_f32 v88, v88\n"
        "v_exp_f32 v89, v89\n"
        "v_cvt_pk_bf16_f32 v107, v86, v87\n"
        "v_mfma_f32_32x32x16_bf16 a[128:143], v[96:99], v[144:147], a[128:143]\n"
        "v_exp_f32 v90, v90\n"
        "v_exp_f32 v91, v91\n"
        "v_cvt_pk_bf16_f32 v108, v88, v89\n"
        "v_mul_u32_u24 v232, 96, v232\n"
        "v_mfma_f32_32x32x16_bf16 a[144:159], v[96:99], v[148:151], a[144:159]\n"
        "v_exp_f32 v92, v92\n"
        "v_exp_f32 v93, v93\n"
        "v_cvt_pk_bf16_f32 v109, v90, v91\n"
        "v_lshl_or_b32 v213, v211, 24, v210\n"
        "v_mfma_f32_32x32x16_bf16 a[128:143], v[100:103], v[152:155], a[128:143]\n"
        "buffer_load_dwordx4 v[204:207], v213, s[36:39], 0 offen offset:112 sc1\n"
        "v_exp_f32 v94, v94\n"
        "v_exp_f32 v95, v95\n"
        "v_cvt_pk_bf16_f32 v110, v92, v93\n"
        "v_mfma_f32_32x32x16_bf16 a[144:159], v[100:103], v[156:159], a[144:159]\n"
        "v_exp_f32 v64, v64\n"
        "v_exp_f32 v65, v65\n"
        "v_cvt_pk_bf16_f32 v111, v94, v95\n"
        "v_lshl_add_u32 v232, v233, 3, v232\n"
        "v_mfma_f32_32x32x16_bf16 v[80:95], v[128:131], a[192:195], 0\n"
        "ds_read_b128 v[112:115], v12 offset:0\n"
        "v_exp_f32 v66, v66\n"
        "v_exp_f32 v67, v67\n"
        "v_cvt_pk_bf16_f32 v96, v64, v65\n"
        "v_mfma_f32_32x32x16_bf16 v[80:95], v[132:135], a[196:199], v[80:95]\n"
        "s_lshl1_add_u32 s77, s84, 20480\n"
        "ds_read_b128 v[116:119], v13 offset:0\n"
        "v_exp_f32 v68, v68\n"
        "v_exp_f32 v69, v69\n"
        "v_cvt_pk_bf16_f32 v97, v66, v67\n"
        "v_mfma_f32_32x32x16_bf16 v[80:95], v[136:139], a[200:203], v[80:95]\n"
        "ds_read_b128 v[120:123], v14 offset:0\n"
        "v_exp_f32 v70, v70\n"
        "v_exp_f32 v71, v71\n"
        "v_cvt_pk_bf16_f32 v98, v68, v69\n"
        "v_mfma_f32_32x32x16_bf16 v[80:95], v[140:143], a[204:207], v[80:95]\n"
        "ds_read_b128 v[124:127], v15 offset:0\n"
        "v_exp_f32 v72, v72\n"
        "v_exp_f32 v73, v73\n"
        "v_cvt_pk_bf16_f32 v99, v70, v71\n"
        "v_mfma_f32_32x32x16_bf16 a[0:15], v[104:107], v[160:163], a[0:15]\n"
        "s_waitcnt vmcnt(11)\n"
        "ds_read_b64_tr_b16 v[144:145], v16 offset:0\n"
        "v_exp_f32 v74, v74\n"
        "v_exp_f32 v75, v75\n"
        "v_cvt_pk_bf16_f32 v100, v72, v73\n"
        "v_mfma_f32_32x32x16_bf16 a[16:31], v[104:107], v[164:167], a[16:31]\n"
        "ds_read_b64_tr_b16 v[146:147], v17 offset:0\n"
        "v_exp_f32 v76, v76\n"
        "v_exp_f32 v77, v77\n"
        "v_cvt_pk_bf16_f32 v101, v74, v75\n"
        "v_mfma_f32_32x32x16_bf16 a[0:15], v[108:111], v[168:171], a[0:15]\n"
        "ds_read_b64_tr_b16 v[148:149], v18 offset:0\n"
        "v_exp_f32 v78, v78\n"
        "v_exp_f32 v79, v79\n"
        "v_cvt_pk_bf16_f32 v102, v76, v77\n"
        "v_mfma_f32_32x32x16_bf16 a[16:31], v[108:111], v[172:175], a[16:31]\n"
        "ds_read_b64_tr_b16 v[150:151], v19 offset:0\n"
        "v_exp_f32 v80, v80\n"
        "v_exp_f32 v81, v81\n"
        "v_cvt_pk_bf16_f32 v103, v78, v79\n"
        "v_mfma_f32_32x32x16_bf16 v[64:79], v[128:131], a[208:211], 0\n"
        "ds_read_b64_tr_b16 v[152:153], v16 offset:2048\n"
        "v_exp_f32 v82, v82\n"
        "v_exp_f32 v83, v83\n"
        "v_cvt_pk_bf16_f32 v104, v80, v81\n"
        "v_mfma_f32_32x32x16_bf16 v[64:79], v[132:135], a[212:215], v[64:79]\n"
        "ds_read_b64_tr_b16 v[154:155], v17 offset:2048\n"
        "v_exp_f32 v84, v84\n"
        "v_exp_f32 v85, v85\n"
        "v_cvt_pk_bf16_f32 v105, v82, v83\n"
        "v_mfma_f32_32x32x16_bf16 v[64:79], v[136:139], a[216:219], v[64:79]\n"
        "ds_read_b64_tr_b16 v[156:157], v18 offset:2048\n"
        "v_exp_f32 v86, v86\n"
        "v_exp_f32 v87, v87\n"
        "v_cvt_pk_bf16_f32 v106, v84, v85\n"
        "v_mfma_f32_32x32x16_bf16 v[64:79], v[140:143], a[220:223], v[64:79]\n"
        "ds_read_b64_tr_b16 v[158:159], v19 offset:2048\n"
        "v_exp_f32 v88, v88\n"
        "v_exp_f32 v89, v89\n"
        "v_cvt_pk_bf16_f32 v107, v86, v87\n"
        "v_mfma_f32_32x32x16_bf16 a[32:47], v[96:99], v[160:163], a[32:47]\n"
        "v_exp_f32 v90, v90\n"
        "v_exp_f32 v91, v91\n"
        "v_cvt_pk_bf16_f32 v108, v88, v89\n"
        "v_sub_u32_sdwa v208, s63, v176 dst_sel:DWORD dst_unused:UNUSED_PAD src0_sel:DWORD src1_sel:WORD_0\n"
        "v_mfma_f32_32x32x16_bf16 a[48:63], v[96:99], v[164:167], a[48:63]\n"
        "ds_add_rtn_u32 v216, v208, v61\n"
        "v_exp_f32 v92, v92\n"
        "v_exp_f32 v93, v93\n"
        "v_cvt_pk_bf16_f32 v109, v90, v91\n"
        "v_mfma_f32_32x32x16_bf16 a[32:47], v[100:103], v[168:171], a[32:47]\n"
        "v_exp_f32 v94, v94\n"
        "v_exp_f32 v95, v95\n"
        "v_cvt_pk_bf16_f32 v110, v92, v93\n"
        "v_sub_u32_sdwa v209, s63, v177 dst_sel:DWORD dst_unused:UNUSED_PAD src0_sel:DWORD src1_sel:WORD_0\n"
        "v_mfma_f32_32x32x16_bf16 a[48:63], v[100:103], v[172:175], a[48:63]\n"
        "ds_add_rtn_u32 v217, v209, v61\n"
        "v_exp_f32 v64, v64\n"
        "v_exp_f32 v65, v65\n"
        "v_cvt_pk_bf16_f32 v111, v94, v95\n"
        "v_mfma_f32_32x32x16_bf16 v[80:95], v[128:131], a[224:227], 0\n"
        "v_exp_f32 v66, v66\n"
        "v_exp_f32 v67, v67\n"
        "v_cvt_pk_bf16_f32 v96, v64, v65\n"
        "v_sub_u32_sdwa v210, s63, v178 dst_sel:DWORD dst_unused:UNUSED_PAD src0_sel:DWORD src1_sel:WORD_0\n"
        "v_mfma_f32_32x32x16_bf16 v[80:95], v[132:135], a[228:231], v[80:95]\n"
        "ds_add_rtn_u32 v218, v210, v61\n"
        "v_exp_f32 v68, v68\n"
        "v_exp_f32 v69, v69\n"
        "v_cvt_pk_bf16_f32 v97, v66, v67\n"
        "v_mfma_f32_32x32x16_bf16 v[80:95], v[136:139], a[232:235], v[80:95]\n"
        "v_exp_f32 v70, v70\n"
        "v_exp_f32 v71, v71\n"
        "v_cvt_pk_bf16_f32 v98, v68, v69\n"
        "v_sub_u32_sdwa v211, s63, v179 dst_sel:DWORD dst_unused:UNUSED_PAD src0_sel:DWORD src1_sel:WORD_0\n"
        "v_mfma_f32_32x32x16_bf16 v[80:95], v[140:143], a[236:239], v[80:95]\n"
        "ds_add_rtn_u32 v219, v211, v61\n"
        "v_exp_f32 v72, v72\n"
        "v_exp_f32 v73, v73\n"
        "v_cvt_pk_bf16_f32 v99, v70, v71\n"
        "v_mfma_f32_32x32x16_bf16 a[64:79], v[104:107], v[160:163], a[64:79]\n"
        "s_waitcnt lgkmcnt(3)\n"
        "ds_write_b16_d16_hi v216, v176 offset:16384\n"
        "v_exp_f32 v74, v74\n"
        "v_exp_f32 v75, v75\n"
        "v_cvt_pk_bf16_f32 v100, v72, v73\n"
        "v_mfma_f32_32x32x16_bf16 a[80:95], v[104:107], v[164:167], a[80:95]\n"
        "s_waitcnt vmcnt(10)\n"
        "v_exp_f32 v76, v76\n"
        "v_exp_f32 v77, v77\n"
        "v_cvt_pk_bf16_f32 v101, v74, v75\n"
        "v_sub_u32_sdwa v212, s63, v180 dst_sel:DWORD dst_unused:UNUSED_PAD src0_sel:DWORD src1_sel:WORD_0\n"
        "v_mfma_f32_32x32x16_bf16 a[64:79], v[108:111], v[168:171], a[64:79]\n"
        "ds_add_rtn_u32 v220, v212, v61\n"
        "v_exp_f32 v78, v78\n"
        "v_exp_f32 v79, v79\n"
        "v_cvt_pk_bf16_f32 v102, v76, v77\n"
        "v_mfma_f32_32x32x16_bf16 a[80:95], v[108:111], v[172:175], a[80:95]\n"
        "s_add_u32 m0, s62, 0\n"
        "v_exp_f32 v80, v80\n"
        "v_exp_f32 v81, v81\n"
        "v_cvt_pk_bf16_f32 v103, v78, v79\n"
        "v_add_u32 v5, s77, v232\n"
        "v_mfma_f32_32x32x16_bf16 v[64:79], v[112:115], a[160:163], 0\n"
        "s_waitcnt lgkmcnt(4)\n"
        "buffer_load_dwordx4 v10, s[44:47], s65 offen offset:0 sc1 lds\n"
        "v_exp_f32 v82, v82\n"
        "v_exp_f32 v83, v83\n"
        "v_cvt_pk_bf16_f32 v104, v80, v81\n"
        "v_mfma_f32_32x32x16_bf16 v[64:79], v[116:119], a[164:167], v[64:79]\n"
        "buffer_load_dwordx4 v11, s[44:47], s65 offen offset:1024 sc1 lds\n"
        "v_exp_f32 v84, v84\n"
        "v_exp_f32 v85, v85\n"
        "v_cvt_pk_bf16_f32 v105, v82, v83\n"
        "v_mfma_f32_32x32x16_bf16 v[64:79], v[120:123], a[168:171], v[64:79]\n"
        "buffer_load_dwordx4 v10, s[44:47], s65 offen offset:2048 sc1 lds\n"
        "v_exp_f32 v86, v86\n"
        "v_exp_f32 v87, v87\n"
        "v_cvt_pk_bf16_f32 v106, v84, v85\n"
        "v_mfma_f32_32x32x16_bf16 v[64:79], v[124:127], a[172:175], v[64:79]\n"
        "buffer_load_dwordx4 v11, s[44:47], s65 offen offset:3072 sc1 lds\n"
        "v_exp_f32 v88, v88\n"
        "v_exp_f32 v89, v89\n"
        "v_cvt_pk_bf16_f32 v107, v86, v87\n"
        "v_mfma_f32_32x32x16_bf16 a[96:111], v[96:99], v[160:163], a[96:111]\n"
        "ds_write_b16_d16_hi v217, v177 offset:16384\n"
        "v_exp_f32 v90, v90\n"
        "v_exp_f32 v91, v91\n"
        "v_cvt_pk_bf16_f32 v108, v88, v89\n"
        "v_mfma_f32_32x32x16_bf16 a[112:127], v[96:99], v[164:167], a[112:127]\n"
        "s_add_u32 s65, s65, s66\n"
        "v_exp_f32 v92, v92\n"
        "v_exp_f32 v93, v93\n"
        "v_cvt_pk_bf16_f32 v109, v90, v91\n"
        "v_sub_u32_sdwa v213, s63, v181 dst_sel:DWORD dst_unused:UNUSED_PAD src0_sel:DWORD src1_sel:WORD_0\n"
        "v_mfma_f32_32x32x16_bf16 a[96:111], v[100:103], v[168:171], a[96:111]\n"
        "ds_add_rtn_u32 v221, v213, v61\n"
        "v_exp_f32 v94, v94\n"
        "v_exp_f32 v95, v95\n"
        "v_cvt_pk_bf16_f32 v110, v92, v93\n"
        "v_mfma_f32_32x32x16_bf16 a[112:127], v[100:103], v[172:175], a[112:127]\n"
        "s_waitcnt lgkmcnt(5)\n"
        "ds_write_b16_d16_hi v218, v178 offset:16384\n"
        "v_exp_f32 v64, v64\n"
        "v_exp_f32 v65, v65\n"
        "v_cvt_pk_bf16_f32 v111, v94, v95\n"
        "v_mfma_f32_32x32x16_bf16 v[80:95], v[112:115], a[176:179], 0\n"
        "v_exp_f32 v66, v66\n"
        "v_exp_f32 v67, v67\n"
        "v_cvt_pk_bf16_f32 v96, v64, v65\n"
        "v_sub_u32_sdwa v214, s63, v182 dst_sel:DWORD dst_unused:UNUSED_PAD src0_sel:DWORD src1_sel:WORD_0\n"
        "v_mfma_f32_32x32x16_bf16 v[80:95], v[116:119], a[180:183], v[80:95]\n"
        "ds_add_rtn_u32 v222, v214, v61\n"
        "v_exp_f32 v68, v68\n"
        "v_exp_f32 v69, v69\n"
        "v_cvt_pk_bf16_f32 v97, v66, v67\n"
        "v_mfma_f32_32x32x16_bf16 v[80:95], v[120:123], a[184:187], v[80:95]\n"
        "s_waitcnt lgkmcnt(6)\n"
        "ds_write_b16_d16_hi v219, v179 offset:16384\n"
        "v_exp_f32 v70, v70\n"
        "v_exp_f32 v71, v71\n"
        "v_cvt_pk_bf16_f32 v98, v68, v69\n"
        "v_mfma_f32_32x32x16_bf16 v[80:95], v[124:127], a[188:191], v[80:95]\n"
        "v_exp_f32 v72, v72\n"
        "v_exp_f32 v73, v73\n"
        "v_cvt_pk_bf16_f32 v99, v70, v71\n"
        "v_sub_u32_sdwa v215, s63, v183 dst_sel:DWORD dst_unused:UNUSED_PAD src0_sel:DWORD src1_sel:WORD_0\n"
        "v_mfma_f32_32x32x16_bf16 a[128:143], v[104:107], v[160:163], a[128:143]\n"
        "ds_add_rtn_u32 v223, v215, v61\n"
        "v_exp_f32 v74, v74\n"
        "v_exp_f32 v75, v75\n"
        "v_cvt_pk_bf16_f32 v100, v72, v73\n"
        "v_mfma_f32_32x32x16_bf16 a[144:159], v[104:107], v[164:167], a[144:159]\n"
        "s_waitcnt lgkmcnt(6)\n"
        "ds_write_b16_d16_hi v220, v180 offset:16384\n"
        "v_exp_f32 v76, v76\n"
        "v_exp_f32 v77, v77\n"
        "v_cvt_pk_bf16_f32 v101, v74, v75\n"
        "v_mfma_f32_32x32x16_bf16 a[128:143], v[108:111], v[168:171], a[128:143]\n"
        "s_waitcnt vmcnt(13)\n"
        "v_exp_f32 v78, v78\n"
        "v_exp_f32 v79, v79\n"
        "v_cvt_pk_bf16_f32 v102, v76, v77\n"
        "v_sub_u32_sdwa v208, s63, v184 dst_sel:DWORD dst_unused:UNUSED_PAD src0_sel:DWORD src1_sel:WORD_0\n"
        "v_mfma_f32_32x32x16_bf16 a[144:159], v[108:111], v[172:175], a[144:159]\n"
        "ds_add_rtn_u32 v216, v208, v61\n"
        "v_exp_f32 v80, v80\n"
        "v_exp_f32 v81, v81\n"
        "v_cvt_pk_bf16_f32 v103, v78, v79\n"
        "v_mfma_f32_32x32x16_bf16 v[64:79], v[112:115], a[192:195], 0\n"
        "ds_read_b128 v[128:131], v12 offset:4096\n"
        "v_exp_f32 v82, v82\n"
        "v_exp_f32 v83, v83\n"
        "v_cvt_pk_bf16_f32 v104, v80, v81\n"
        "v_mfma_f32_32x32x16_bf16 v[64:79], v[116:119], a[196:199], v[64:79]\n"
        "s_waitcnt lgkmcnt(7)\n"
        "ds_read_b128 v[132:135], v13 offset:4096\n"
        "v_exp_f32 v84, v84\n"
        "v_exp_f32 v85, v85\n"
        "v_cvt_pk_bf16_f32 v105, v82, v83\n"
        "v_mfma_f32_32x32x16_bf16 v[64:79], v[120:123], a[200:203], v[64:79]\n"
        "ds_read_b128 v[136:139], v14 offset:4096\n"
        "v_exp_f32 v86, v86\n"
        "v_exp_f32 v87, v87\n"
        "v_cvt_pk_bf16_f32 v106, v84, v85\n"
        "v_mfma_f32_32x32x16_bf16 v[64:79], v[124:127], a[204:207], v[64:79]\n"
        "ds_read_b128 v[140:143], v15 offset:4096\n"
        "v_exp_f32 v88, v88\n"
        "v_exp_f32 v89, v89\n"
        "v_cvt_pk_bf16_f32 v107, v86, v87\n"
        "v_mfma_f32_32x32x16_bf16 a[0:15], v[96:99], v[144:147], a[0:15]\n"
        "ds_read_b64_tr_b16 v[160:161], v16 offset:4096\n"
        "v_exp_f32 v90, v90\n"
        "v_exp_f32 v91, v91\n"
        "v_cvt_pk_bf16_f32 v108, v88, v89\n"
        "v_mfma_f32_32x32x16_bf16 a[16:31], v[96:99], v[148:151], a[16:31]\n"
        "ds_read_b64_tr_b16 v[162:163], v17 offset:4096\n"
        "v_exp_f32 v92, v92\n"
        "v_exp_f32 v93, v93\n"
        "v_cvt_pk_bf16_f32 v109, v90, v91\n"
        "v_mfma_f32_32x32x16_bf16 a[0:15], v[100:103], v[152:155], a[0:15]\n"
        "ds_read_b64_tr_b16 v[164:165], v18 offset:4096\n"
        "v_exp_f32 v94, v94\n"
        "v_exp_f32 v95, v95\n"
        "v_cvt_pk_bf16_f32 v110, v92, v93\n"
        "v_mfma_f32_32x32x16_bf16 a[16:31], v[100:103], v[156:159], a[16:31]\n"
        "ds_read_b64_tr_b16 v[166:167], v19 offset:4096\n"
        "v_exp_f32 v64, v64\n"
        "v_exp_f32 v65, v65\n"
        "v_cvt_pk_bf16_f32 v111, v94, v95\n"
        "v_mfma_f32_32x32x16_bf16 v[80:95], v[112:115], a[208:211], 0\n"
        "ds_read_b64_tr_b16 v[168:169], v16 offset:6144\n"
        "v_exp_f32 v66, v66\n"
        "v_exp_f32 v67, v67\n"
        "v_cvt_pk_bf16_f32 v96, v64, v65\n"
        "v_mfma_f32_32x32x16_bf16 v[80:95], v[116:119], a[212:215], v[80:95]\n"
        "ds_read_b64_tr_b16 v[170:171], v17 offset:6144\n"
        "v_exp_f32 v68, v68\n"
        "v_exp_f32 v69, v69\n"
        "v_cvt_pk_bf16_f32 v97, v66, v67\n"
        "v_mfma_f32_32x32x16_bf16 v[80:95], v[120:123], a[216:219], v[80:95]\n"
        "ds_read_b64_tr_b16 v[172:173], v18 offset:6144\n"
        "v_exp_f32 v70, v70\n"
        "v_exp_f32 v71, v71\n"
        "v_cvt_pk_bf16_f32 v98, v68, v69\n"
        "v_mfma_f32_32x32x16_bf16 v[80:95], v[124:127], a[220:223], v[80:95]\n"
        "ds_read_b64_tr_b16 v[174:175], v19 offset:6144\n"
        "v_exp_f32 v72, v72\n"
        "v_exp_f32 v73, v73\n"
        "v_cvt_pk_bf16_f32 v99, v70, v71\n"
        "v_mfma_f32_32x32x16_bf16 a[32:47], v[104:107], v[144:147], a[32:47]\n"
        "ds_write_b16_d16_hi v221, v181 offset:16384\n"
        "v_exp_f32 v74, v74\n"
        "v_exp_f32 v75, v75\n"
        "v_cvt_pk_bf16_f32 v100, v72, v73\n"
        "v_mfma_f32_32x32x16_bf16 a[48:63], v[104:107], v[148:151], a[48:63]\n"
        "v_exp_f32 v76, v76\n"
        "v_exp_f32 v77, v77\n"
        "v_cvt_pk_bf16_f32 v101, v74, v75\n"
        "v_sub_u32_sdwa v209, s63, v185 dst_sel:DWORD dst_unused:UNUSED_PAD src0_sel:DWORD src1_sel:WORD_0\n"
        "v_mfma_f32_32x32x16_bf16 a[32:47], v[108:111], v[152:155], a[32:47]\n"
        "ds_add_rtn_u32 v217, v209, v61\n"
        "v_exp_f32 v78, v78\n"
        "v_exp_f32 v79, v79\n"
        "v_cvt_pk_bf16_f32 v102, v76, v77\n"
        "v_mfma_f32_32x32x16_bf16 a[48:63], v[108:111], v[156:159], a[48:63]\n"
        "s_waitcnt lgkmcnt(14)\n"
        "ds_write_b16_d16_hi v222, v182 offset:16384\n"
        "v_exp_f32 v80, v80\n"
        "v_exp_f32 v81, v81\n"
        "v_cvt_pk_bf16_f32 v103, v78, v79\n"
        "v_mfma_f32_32x32x16_bf16 v[64:79], v[112:115], a[224:227], 0\n"
        "v_exp_f32 v82, v82\n"
        "v_exp_f32 v83, v83\n"
        "v_cvt_pk_bf16_f32 v104, v80, v81\n"
        "v_sub_u32_sdwa v210, s63, v186 dst_sel:DWORD dst_unused:UNUSED_PAD src0_sel:DWORD src1_sel:WORD_0\n"
        "v_mfma_f32_32x32x16_bf16 v[64:79], v[116:119], a[228:231], v[64:79]\n"
        "ds_add_rtn_u32 v218, v210, v61\n"
        "v_exp_f32 v84, v84\n"
        "v_exp_f32 v85, v85\n"
        "v_cvt_pk_bf16_f32 v105, v82, v83\n"
        "v_mfma_f32_32x32x16_bf16 v[64:79], v[120:123], a[232:235], v[64:79]\n"
        "ds_write_b16_d16_hi v223, v183 offset:16384\n"
        "v_exp_f32 v86, v86\n"
        "v_exp_f32 v87, v87\n"
        "v_cvt_pk_bf16_f32 v106, v84, v85\n"
        "v_mfma_f32_32x32x16_bf16 v[64:79], v[124:127], a[236:239], v[64:79]\n"
        "v_exp_f32 v88, v88\n"
        "v_exp_f32 v89, v89\n"
        "v_cvt_pk_bf16_f32 v107, v86, v87\n"
        "v_sub_u32_sdwa v211, s63, v187 dst_sel:DWORD dst_unused:UNUSED_PAD src0_sel:DWORD src1_sel:WORD_0\n"
        "v_mfma_f32_32x32x16_bf16 a[64:79], v[96:99], v[144:147], a[64:79]\n"
        "ds_add_rtn_u32 v219, v211, v61\n"
        "v_exp_f32 v90, v90\n"
        "v_exp_f32 v91, v91\n"
        "v_cvt_pk_bf16_f32 v108, v88, v89\n"
        "v_mfma_f32_32x32x16_bf16 a[80:95], v[96:99], v[148:151], a[80:95]\n"
        "ds_write_b16_d16_hi v216, v184 offset:16384\n"
        "v_exp_f32 v92, v92\n"
        "v_exp_f32 v93, v93\n"
        "v_cvt_pk_bf16_f32 v109, v90, v91\n"
        "v_mfma_f32_32x32x16_bf16 a[64:79], v[100:103], v[152:155], a[64:79]\n"
        "s_waitcnt lgkmcnt(7)\n"
        "v_exp_f32 v94, v94\n"
        "v_exp_f32 v95, v95\n"
        "v_cvt_pk_bf16_f32 v110, v92, v93\n"
        "v_mfma_f32_32x32x16_bf16 a[80:95], v[100:103], v[156:159], a[80:95]\n"
        "s_add_u32 m0, s62, 4096\n"
        "v_exp_f32 v64, v64\n"
        "v_exp_f32 v65, v65\n"
        "v_cvt_pk_bf16_f32 v111, v94, v95\n"
        "v_mfma_f32_32x32x16_bf16 v[80:95], v[128:131], a[160:163], 0\n"
        "s_waitcnt vmcnt(12)\n"
        "buffer_load_dwordx4 v10, s[44:47], s65 offen offset:0 sc1 lds\n"
        "v_exp_f32 v66, v66\n"
        "v_exp_f32 v67, v67\n"
        "v_cvt_pk_bf16_f32 v96, v64, v65\n"
        "v_mfma_f32_32x32x16_bf16 v[80:95], v[132:135], a[164:167], v[80:95]\n"
        "buffer_load_dwordx4 v11, s[44:47], s65 offen offset:1024 sc1 lds\n"
        "v_exp_f32 v68, v68\n"
        "v_exp_f32 v69, v69\n"
        "v_cvt_pk_bf16_f32 v97, v66, v67\n"
        "v_mfma_f32_32x32x16_bf16 v[80:95], v[136:139], a[168:171], v[80:95]\n"
        "buffer_load_dwordx4 v10, s[44:47], s65 offen offset:2048 sc1 lds\n"
        "v_exp_f32 v70, v70\n"
        "v_exp_f32 v71, v71\n"
        "v_cvt_pk_bf16_f32 v98, v68, v69\n"
        "v_mfma_f32_32x32x16_bf16 v[80:95], v[140:143], a[172:175], v[80:95]\n"
        "buffer_load_dwordx4 v11, s[44:47], s65 offen offset:3072 sc1 lds\n"
        "v_exp_f32 v72, v72\n"
        "v_exp_f32 v73, v73\n"
        "v_cvt_pk_bf16_f32 v99, v70, v71\n"
        "v_mfma_f32_32x32x16_bf16 a[96:111], v[104:107], v[144:147], a[96:111]\n"
        "v_exp_f32 v74, v74\n"
        "v_exp_f32 v75, v75\n"
        "v_cvt_pk_bf16_f32 v100, v72, v73\n"
        "v_sub_u32_sdwa v212, s63, v188 dst_sel:DWORD dst_unused:UNUSED_PAD src0_sel:DWORD src1_sel:WORD_0\n"
        "v_mfma_f32_32x32x16_bf16 a[112:127], v[104:107], v[148:151], a[112:127]\n"
        "s_add_u32 s65, s65, s66\n"
        "ds_add_rtn_u32 v220, v212, v61\n"
        "v_exp_f32 v76, v76\n"
        "v_exp_f32 v77, v77\n"
        "v_cvt_pk_bf16_f32 v101, v74, v75\n"
        "v_mfma_f32_32x32x16_bf16 a[96:111], v[108:111], v[152:155], a[96:111]\n"
        "s_waitcnt lgkmcnt(6)\n"
        "ds_write_b16_d16_hi v217, v185 offset:16384\n"
        "v_exp_f32 v78, v78\n"
        "v_exp_f32 v79, v79\n"
        "v_cvt_pk_bf16_f32 v102, v76, v77\n"
        "v_mfma_f32_32x32x16_bf16 a[112:127], v[108:111], v[156:159], a[112:127]\n"
        "v_exp_f32 v80, v80\n"
        "v_exp_f32 v81, v81\n"
        "v_cvt_pk_bf16_f32 v103, v78, v79\n"
        "v_sub_u32_sdwa v213, s63, v189 dst_sel:DWORD dst_unused:UNUSED_PAD src0_sel:DWORD src1_sel:WORD_0\n"
        "v_mfma_f32_32x32x16_bf16 v[64:79], v[128:131], a[176:179], 0\n"
        "ds_add_rtn_u32 v221, v213, v61\n"
        "v_exp_f32 v82, v82\n"
        "v_exp_f32 v83, v83\n"
        "v_cvt_pk_bf16_f32 v104, v80, v81\n"
        "v_mfma_f32_32x32x16_bf16 v[64:79], v[132:135], a[180:183], v[64:79]\n"
        "s_waitcnt lgkmcnt(6)\n"
        "ds_write_b16_d16_hi v218, v186 offset:16384\n"
        "v_exp_f32 v84, v84\n"
        "v_exp_f32 v85, v85\n"
        "v_cvt_pk_bf16_f32 v105, v82, v83\n"
        "v_mfma_f32_32x32x16_bf16 v[64:79], v[136:139], a[184:187], v[64:79]\n"
        "v_exp_f32 v86, v86\n"
        "v_exp_f32 v87, v87\n"
        "v_cvt_pk_bf16_f32 v106, v84, v85\n"
        "v_sub_u32_sdwa v214, s63, v190 dst_sel:DWORD dst_unused:UNUSED_PAD src0_sel:DWORD src1_sel:WORD_0\n"
        "v_mfma_f32_32x32x16_bf16 v[64:79], v[140:143], a[188:191], v[64:79]\n"
        "ds_add_rtn_u32 v222, v214, v61\n"
        "v_exp_f32 v88, v88\n"
        "v_exp_f32 v89, v89\n"
        "v_cvt_pk_bf16_f32 v107, v86, v87\n"
        "v_mfma_f32_32x32x16_bf16 a[128:143], v[96:99], v[144:147], a[128:143]\n"
        "s_waitcnt lgkmcnt(6)\n"
        "ds_write_b16_d16_hi v219, v187 offset:16384\n"
        "v_exp_f32 v90, v90\n"
        "v_exp_f32 v91, v91\n"
        "v_cvt_pk_bf16_f32 v108, v88, v89\n"
        "v_mfma_f32_32x32x16_bf16 a[144:159], v[96:99], v[148:151], a[144:159]\n"
        "v_exp_f32 v92, v92\n"
        "v_exp_f32 v93, v93\n"
        "v_cvt_pk_bf16_f32 v109, v90, v91\n"
        "v_sub_u32_sdwa v215, s63, v191 dst_sel:DWORD dst_unused:UNUSED_PAD src0_sel:DWORD src1_sel:WORD_0\n"
        "v_mfma_f32_32x32x16_bf16 a[128:143], v[100:103], v[152:155], a[128:143]\n"
        "ds_add_rtn_u32 v223, v215, v61\n"
        "v_exp_f32 v94, v94\n"
        "v_exp_f32 v95, v95\n"
        "v_cvt_pk_bf16_f32 v110, v92, v93\n"
        "v_mfma_f32_32x32x16_bf16 a[144:159], v[100:103], v[156:159], a[144:159]\n"
        "s_waitcnt lgkmcnt(6)\n"
        "ds_write_b16_d16_hi v220, v188 offset:16384\n"
        "v_exp_f32 v64, v64\n"
        "v_exp_f32 v65, v65\n"
        "v_cvt_pk_bf16_f32 v111, v94, v95\n"
        "v_mfma_f32_32x32x16_bf16 v[80:95], v[128:131], a[192:195], 0\n"
        "ds_read_b128 v[112:115], v12 offset:8192\n"
        "v_exp_f32 v66, v66\n"
        "v_exp_f32 v67, v67\n"
        "v_cvt_pk_bf16_f32 v96, v64, v65\n"
        "v_mfma_f32_32x32x16_bf16 v[80:95], v[132:135], a[196:199], v[80:95]\n"
        "s_waitcnt vmcnt(11)\n"
        "ds_read_b128 v[116:119], v13 offset:8192\n"
        "v_exp_f32 v68, v68\n"
        "v_exp_f32 v69, v69\n"
        "v_cvt_pk_bf16_f32 v97, v66, v67\n"
        "v_mfma_f32_32x32x16_bf16 v[80:95], v[136:139], a[200:203], v[80:95]\n"
        "ds_read_b128 v[120:123], v14 offset:8192\n"
        "v_exp_f32 v70, v70\n"
        "v_exp_f32 v71, v71\n"
        "v_cvt_pk_bf16_f32 v98, v68, v69\n"
        "v_mfma_f32_32x32x16_bf16 v[80:95], v[140:143], a[204:207], v[80:95]\n"
        "ds_read_b128 v[124:127], v15 offset:8192\n"
        "v_exp_f32 v72, v72\n"
        "v_exp_f32 v73, v73\n"
        "v_cvt_pk_bf16_f32 v99, v70, v71\n"
        "v_mfma_f32_32x32x16_bf16 a[0:15], v[104:107], v[160:163], a[0:15]\n"
        "ds_read_b64_tr_b16 v[144:145], v16 offset:8192\n"
        "v_exp_f32 v74, v74\n"
        "v_exp_f32 v75, v75\n"
        "v_cvt_pk_bf16_f32 v100, v72, v73\n"
        "v_mfma_f32_32x32x16_bf16 a[16:31], v[104:107], v[164:167], a[16:31]\n"
        "ds_read_b64_tr_b16 v[146:147], v17 offset:8192\n"
        "v_exp_f32 v76, v76\n"
        "v_exp_f32 v77, v77\n"
        "v_cvt_pk_bf16_f32 v101, v74, v75\n"
        "v_mfma_f32_32x32x16_bf16 a[0:15], v[108:111], v[168:171], a[0:15]\n"
        "ds_read_b64_tr_b16 v[148:149], v18 offset:8192\n"
        "v_exp_f32 v78, v78\n"
        "v_exp_f32 v79, v79\n"
        "v_cvt_pk_bf16_f32 v102, v76, v77\n"
        "v_mfma_f32_32x32x16_bf16 a[16:31], v[108:111], v[172:175], a[16:31]\n"
        "ds_read_b64_tr_b16 v[150:151], v19 offset:8192\n"
        "v_exp_f32 v80, v80\n"
        "v_exp_f32 v81, v81\n"
        "v_cvt_pk_bf16_f32 v103, v78, v79\n"
        "v_mfma_f32_32x32x16_bf16 v[64:79], v[128:131], a[208:211], 0\n"
        "ds_read_b64_tr_b16 v[152:153], v16 offset:10240\n"
        "v_exp_f32 v82, v82\n"
        "v_exp_f32 v83, v83\n"
        "v_cvt_pk_bf16_f32 v104, v80, v81\n"
        "v_mfma_f32_32x32x16_bf16 v[64:79], v[132:135], a[212:215], v[64:79]\n"
        "ds_read_b64_tr_b16 v[154:155], v17 offset:10240\n"
        "v_exp_f32 v84, v84\n"
        "v_exp_f32 v85, v85\n"
        "v_cvt_pk_bf16_f32 v105, v82, v83\n"
        "v_mfma_f32_32x32x16_bf16 v[64:79], v[136:139], a[216:219], v[64:79]\n"
        "ds_read_b64_tr_b16 v[156:157], v18 offset:10240\n"
        "v_exp_f32 v86, v86\n"
        "v_exp_f32 v87, v87\n"
        "v_cvt_pk_bf16_f32 v106, v84, v85\n"
        "v_mfma_f32_32x32x16_bf16 v[64:79], v[140:143], a[220:223], v[64:79]\n"
        "ds_read_b64_tr_b16 v[158:159], v19 offset:10240\n"
        "v_exp_f32 v88, v88\n"
        "v_exp_f32 v89, v89\n"
        "v_cvt_pk_bf16_f32 v107, v86, v87\n"
        "v_mfma_f32_32x32x16_bf16 a[32:47], v[96:99], v[160:163], a[32:47]\n"
        "v_exp_f32 v90, v90\n"
        "v_exp_f32 v91, v91\n"
        "v_cvt_pk_bf16_f32 v108, v88, v89\n"
        "v_sub_u32_sdwa v208, s63, v192 dst_sel:DWORD dst_unused:UNUSED_PAD src0_sel:DWORD src1_sel:WORD_0\n"
        "v_mfma_f32_32x32x16_bf16 a[48:63], v[96:99], v[164:167], a[48:63]\n"
        "ds_add_rtn_u32 v216, v208, v61\n"
        "v_exp_f32 v92, v92\n"
        "v_exp_f32 v93, v93\n"
        "v_cvt_pk_bf16_f32 v109, v90, v91\n"
        "v_mfma_f32_32x32x16_bf16 a[32:47], v[100:103], v[168:171], a[32:47]\n"
        "s_waitcnt lgkmcnt(14)\n"
        "ds_write_b16_d16_hi v221, v189 offset:16384\n"
        "v_exp_f32 v94, v94\n"
        "v_exp_f32 v95, v95\n"
        "v_cvt_pk_bf16_f32 v110, v92, v93\n"
        "v_mfma_f32_32x32x16_bf16 a[48:63], v[100:103], v[172:175], a[48:63]\n"
        "v_exp_f32 v64, v64\n"
        "v_exp_f32 v65, v65\n"
        "v_cvt_pk_bf16_f32 v111, v94, v95\n"
        "v_sub_u32_sdwa v209, s63, v193 dst_sel:DWORD dst_unused:UNUSED_PAD src0_sel:DWORD src1_sel:WORD_0\n"
        "v_mfma_f32_32x32x16_bf16 v[80:95], v[128:131], a[224:227], 0\n"
        "ds_add_rtn_u32 v217, v209, v61\n"
        "v_exp_f32 v66, v66\n"
        "v_exp_f32 v67, v67\n"
        "v_cvt_pk_bf16_f32 v96, v64, v65\n"
        "v_mfma_f32_32x32x16_bf16 v[80:95], v[132:135], a[228:231], v[80:95]\n"
        "ds_write_b16_d16_hi v222, v190 offset:16384\n"
        "v_exp_f32 v68, v68\n"
        "v_exp_f32 v69, v69\n"
        "v_cvt_pk_bf16_f32 v97, v66, v67\n"
        "v_mfma_f32_32x32x16_bf16 v[80:95], v[136:139], a[232:235], v[80:95]\n"
        "v_exp_f32 v70, v70\n"
        "v_exp_f32 v71, v71\n"
        "v_cvt_pk_bf16_f32 v98, v68, v69\n"
        "v_sub_u32_sdwa v210, s63, v194 dst_sel:DWORD dst_unused:UNUSED_PAD src0_sel:DWORD src1_sel:WORD_0\n"
        "v_mfma_f32_32x32x16_bf16 v[80:95], v[140:143], a[236:239], v[80:95]\n"
        "ds_add_rtn_u32 v218, v210, v61\n"
        "v_exp_f32 v72, v72\n"
        "v_exp_f32 v73, v73\n"
        "v_cvt_pk_bf16_f32 v99, v70, v71\n"
        "v_mfma_f32_32x32x16_bf16 a[64:79], v[104:107], v[160:163], a[64:79]\n"
        "ds_write_b16_d16_hi v223, v191 offset:16384\n"
        "v_exp_f32 v74, v74\n"
        "v_exp_f32 v75, v75\n"
        "v_cvt_pk_bf16_f32 v100, v72, v73\n"
        "v_mfma_f32_32x32x16_bf16 a[80:95], v[104:107], v[164:167], a[80:95]\n"
        "v_exp_f32 v76, v76\n"
        "v_exp_f32 v77, v77\n"
        "v_cvt_pk_bf16_f32 v101, v74, v75\n"
        "v_sub_u32_sdwa v211, s63, v195 dst_sel:DWORD dst_unused:UNUSED_PAD src0_sel:DWORD src1_sel:WORD_0\n"
        "v_mfma_f32_32x32x16_bf16 a[64:79], v[108:111], v[168:171], a[64:79]\n"
        "s_waitcnt lgkmcnt(6)\n"
        "ds_add_rtn_u32 v219, v211, v61\n"
        "v_exp_f32 v78, v78\n"
        "v_exp_f32 v79, v79\n"
        "v_cvt_pk_bf16_f32 v102, v76, v77\n"
        "v_mfma_f32_32x32x16_bf16 a[80:95], v[108:111], v[172:175], a[80:95]\n"
        "s_add_u32 m0, s62, 8192\n"
        "v_exp_f32 v80, v80\n"
        "v_exp_f32 v81, v81\n"
        "v_cvt_pk_bf16_f32 v103, v78, v79\n"
        "v_mfma_f32_32x32x16_bf16 v[64:79], v[112:115], a[160:163], 0\n"
        "s_waitcnt lgkmcnt(6)\n"
        "buffer_load_dwordx4 v10, s[44:47], s65 offen offset:0 sc1 lds\n"
        "v_exp_f32 v82, v82\n"
        "v_exp_f32 v83, v83\n"
        "v_cvt_pk_bf16_f32 v104, v80, v81\n"
        "v_mfma_f32_32x32x16_bf16 v[64:79], v[116:119], a[164:167], v[64:79]\n"
        "buffer_load_dwordx4 v11, s[44:47], s65 offen offset:1024 sc1 lds\n"
        "v_exp_f32 v84, v84\n"
        "v_exp_f32 v85, v85\n"
        "v_cvt_pk_bf16_f32 v105, v82, v83\n"
        "v_mfma_f32_32x32x16_bf16 v[64:79], v[120:123], a[168:171], v[64:79]\n"
        "buffer_load_dwordx4 v10, s[44:47], s65 offen offset:2048 sc1 lds\n"
        "v_exp_f32 v86, v86\n"
        "v_exp_f32 v87, v87\n"
        "v_cvt_pk_bf16_f32 v106, v84, v85\n"
        "v_mfma_f32_32x32x16_bf16 v[64:79], v[124:127], a[172:175], v[64:79]\n"
        "buffer_load_dwordx4 v11, s[44:47], s65 offen offset:3072 sc1 lds\n"
        "v_exp_f32 v88, v88\n"
        "v_exp_f32 v89, v89\n"
        "v_cvt_pk_bf16_f32 v107, v86, v87\n"
        "v_mfma_f32_32x32x16_bf16 a[96:111], v[96:99], v[160:163], a[96:111]\n"
        "ds_write_b16_d16_hi v216, v192 offset:16384\n"
        "v_exp_f32 v90, v90\n"
        "v_exp_f32 v91, v91\n"
        "v_cvt_pk_bf16_f32 v108, v88, v89\n"
        "v_mfma_f32_32x32x16_bf16 a[112:127], v[96:99], v[164:167], a[112:127]\n"
        "s_add_u32 s65, s65, s66\n"
        "v_exp_f32 v92, v92\n"
        "v_exp_f32 v93, v93\n"
        "v_cvt_pk_bf16_f32 v109, v90, v91\n"
        "v_mfma_f32_32x32x16_bf16 a[96:111], v[100:103], v[168:171], a[96:111]\n"
        "s_waitcnt vmcnt(12)\n"
        "v_exp_f32 v94, v94\n"
        "v_exp_f32 v95, v95\n"
        "v_cvt_pk_bf16_f32 v110, v92, v93\n"
        "v_sub_u32_sdwa v212, s63, v196 dst_sel:DWORD dst_unused:UNUSED_PAD src0_sel:DWORD src1_sel:WORD_0\n"
        "v_mfma_f32_32x32x16_bf16 a[112:127], v[100:103], v[172:175], a[112:127]\n"
        "ds_add_rtn_u32 v220, v212, v61\n"
        "v_exp_f32 v64, v64\n"
        "v_exp_f32 v65, v65\n"
        "v_cvt_pk_bf16_f32 v111, v94, v95\n"
        "v_mfma_f32_32x32x16_bf16 v[80:95], v[112:115], a[176:179], 0\n"
        "s_waitcnt lgkmcnt(6)\n"
        "ds_write_b16_d16_hi v217, v193 offset:16384\n"
        "v_exp_f32 v66, v66\n"
        "v_exp_f32 v67, v67\n"
        "v_cvt_pk_bf16_f32 v96, v64, v65\n"
        "v_mfma_f32_32x32x16_bf16 v[80:95], v[116:119], a[180:183], v[80:95]\n"
        "v_exp_f32 v68, v68\n"
        "v_exp_f32 v69, v69\n"
        "v_cvt_pk_bf16_f32 v97, v66, v67\n"
        "v_sub_u32_sdwa v213, s63, v197 dst_sel:DWORD dst_unused:UNUSED_PAD src0_sel:DWORD src1_sel:WORD_0\n"
        "v_mfma_f32_32x32x16_bf16 v[80:95], v[120:123], a[184:187], v[80:95]\n"
        "ds_add_rtn_u32 v221, v213, v61\n"
        "v_exp_f32 v70, v70\n"
        "v_exp_f32 v71, v71\n"
        "v_cvt_pk_bf16_f32 v98, v68, v69\n"
        "v_mfma_f32_32x32x16_bf16 v[80:95], v[124:127], a[188:191], v[80:95]\n"
        "s_waitcnt lgkmcnt(6)\n"
        "ds_write_b16_d16_hi v218, v194 offset:16384\n"
        "v_exp_f32 v72, v72\n"
        "v_exp_f32 v73, v73\n"
        "v_cvt_pk_bf16_f32 v99, v70, v71\n"
        "v_mfma_f32_32x32x16_bf16 a[128:143], v[104:107], v[160:163], a[128:143]\n"
        "v_exp_f32 v74, v74\n"
        "v_exp_f32 v75, v75\n"
        "v_cvt_pk_bf16_f32 v100, v72, v73\n"
        "v_sub_u32_sdwa v214, s63, v198 dst_sel:DWORD dst_unused:UNUSED_PAD src0_sel:DWORD src1_sel:WORD_0\n"
        "v_mfma_f32_32x32x16_bf16 a[144:159], v[104:107], v[164:167], a[144:159]\n"
        "ds_add_rtn_u32 v222, v214, v61\n"
        "v_exp_f32 v76, v76\n"
        "v_exp_f32 v77, v77\n"
        "v_cvt_pk_bf16_f32 v101, v74, v75\n"
        "v_mfma_f32_32x32x16_bf16 a[128:143], v[108:111], v[168:171], a[128:143]\n"
        "s_waitcnt lgkmcnt(6)\n"
        "ds_write_b16_d16_hi v219, v195 offset:16384\n"
        "v_exp_f32 v78, v78\n"
        "v_exp_f32 v79, v79\n"
        "v_cvt_pk_bf16_f32 v102, v76, v77\n"
        "v_mfma_f32_32x32x16_bf16 a[144:159], v[108:111], v[172:175], a[144:159]\n"
        "v_exp_f32 v80, v80\n"
        "v_exp_f32 v81, v81\n"
        "v_cvt_pk_bf16_f32 v103, v78, v79\n"
        "v_sub_u32_sdwa v215, s63, v199 dst_sel:DWORD dst_unused:UNUSED_PAD src0_sel:DWORD src1_sel:WORD_0\n"
        "v_mfma_f32_32x32x16_bf16 v[64:79], v[112:115], a[192:195], 0\n"
        "ds_read_b128 v[128:131], v12 offset:12288\n"
        "v_exp_f32 v82, v82\n"
        "v_exp_f32 v83, v83\n"
        "v_cvt_pk_bf16_f32 v104, v80, v81\n"
        "v_mfma_f32_32x32x16_bf16 v[64:79], v[116:119], a[196:199], v[64:79]\n"
        "ds_read_b128 v[132:135], v13 offset:12288\n"
        "v_exp_f32 v84, v84\n"
        "v_exp_f32 v85, v85\n"
        "v_cvt_pk_bf16_f32 v105, v82, v83\n"
        "v_mfma_f32_32x32x16_bf16 v[64:79], v[120:123], a[200:203], v[64:79]\n"
        "ds_read_b128 v[136:139], v14 offset:12288\n"
        "v_exp_f32 v86, v86\n"
        "v_exp_f32 v87, v87\n"
        "v_cvt_pk_bf16_f32 v106, v84, v85\n"
        "v_mfma_f32_32x32x16_bf16 v[64:79], v[124:127], a[204:207], v[64:79]\n"
        "ds_read_b128 v[140:143], v15 offset:12288\n"
        "v_exp_f32 v88, v88\n"
        "v_exp_f32 v89, v89\n"
        "v_cvt_pk_bf16_f32 v107, v86, v87\n"
        "v_mfma_f32_32x32x16_bf16 a[0:15], v[96:99], v[144:147], a[0:15]\n"
        "ds_read_b64_tr_b16 v[160:161], v16 offset:12288\n"
        "v_exp_f32 v90, v90\n"
        "v_exp_f32 v91, v91\n"
        "v_cvt_pk_bf16_f32 v108, v88, v89\n"
        "v_mfma_f32_32x32x16_bf16 a[16:31], v[96:99], v[148:151], a[16:31]\n"
        "ds_read_b64_tr_b16 v[162:163], v17 offset:12288\n"
        "v_exp_f32 v92, v92\n"
        "v_exp_f32 v93, v93\n"
        "v_cvt_pk_bf16_f32 v109, v90, v91\n"
        "v_mfma_f32_32x32x16_bf16 a[0:15], v[100:103], v[152:155], a[0:15]\n"
        "ds_read_b64_tr_b16 v[164:165], v18 offset:12288\n"
        "v_exp_f32 v94, v94\n"
        "v_exp_f32 v95, v95\n"
        "v_cvt_pk_bf16_f32 v110, v92, v93\n"
        "v_mfma_f32_32x32x16_bf16 a[16:31], v[100:103], v[156:159], a[16:31]\n"
        "ds_read_b64_tr_b16 v[166:167], v19 offset:12288\n"
        "v_exp_f32 v64, v64\n"
        "v_exp_f32 v65, v65\n"
        "v_cvt_pk_bf16_f32 v111, v94, v95\n"
        "v_mfma_f32_32x32x16_bf16 v[80:95], v[112:115], a[208:211], 0\n"
        "ds_read_b64_tr_b16 v[168:169], v16 offset:14336\n"
        "v_exp_f32 v66, v66\n"
        "v_exp_f32 v67, v67\n"
        "v_cvt_pk_bf16_f32 v96, v64, v65\n"
        "v_mfma_f32_32x32x16_bf16 v[80:95], v[116:119], a[212:215], v[80:95]\n"
        "ds_read_b64_tr_b16 v[170:171], v17 offset:14336\n"
        "v_exp_f32 v68, v68\n"
        "v_exp_f32 v69, v69\n"
        "v_cvt_pk_bf16_f32 v97, v66, v67\n"
        "v_mfma_f32_32x32x16_bf16 v[80:95], v[120:123], a[216:219], v[80:95]\n"
        "ds_read_b64_tr_b16 v[172:173], v18 offset:14336\n"
        "v_exp_f32 v70, v70\n"
        "v_exp_f32 v71, v71\n"
        "v_cvt_pk_bf16_f32 v98, v68, v69\n"
        "v_mfma_f32_32x32x16_bf16 v[80:95], v[124:127], a[220:223], v[80:95]\n"
        "ds_read_b64_tr_b16 v[174:175], v19 offset:14336\n"
        "v_exp_f32 v72, v72\n"
        "v_exp_f32 v73, v73\n"
        "v_cvt_pk_bf16_f32 v99, v70, v71\n"
        "v_mfma_f32_32x32x16_bf16 a[32:47], v[104:107], v[144:147], a[32:47]\n"
        "ds_add_rtn_u32 v223, v215, v61\n"
        "v_exp_f32 v74, v74\n"
        "v_exp_f32 v75, v75\n"
        "v_cvt_pk_bf16_f32 v100, v72, v73\n"
        "v_mfma_f32_32x32x16_bf16 a[48:63], v[104:107], v[148:151], a[48:63]\n"
        "s_waitcnt lgkmcnt(14)\n"
        "ds_write_b16_d16_hi v220, v196 offset:16384\n"
        "v_exp_f32 v76, v76\n"
        "v_exp_f32 v77, v77\n"
        "v_cvt_pk_bf16_f32 v101, v74, v75\n"
        "v_mfma_f32_32x32x16_bf16 a[32:47], v[108:111], v[152:155], a[32:47]\n"
        "ds_write_b16_d16_hi v221, v197 offset:16384\n"
        "v_exp_f32 v78, v78\n"
        "v_exp_f32 v79, v79\n"
        "v_cvt_pk_bf16_f32 v102, v76, v77\n"
        "v_mfma_f32_32x32x16_bf16 a[48:63], v[108:111], v[156:159], a[48:63]\n"
        "ds_write_b16_d16_hi v222, v198 offset:16384\n"
        "v_exp_f32 v80, v80\n"
        "v_exp_f32 v81, v81\n"
        "v_cvt_pk_bf16_f32 v103, v78, v79\n"
        "v_mfma_f32_32x32x16_bf16 v[64:79], v[112:115], a[224:227], 0\n"
        "s_waitcnt lgkmcnt(3)\n"
        "ds_write_b16_d16_hi v223, v199 offset:16384\n"
        "v_exp_f32 v82, v82\n"
        "v_exp_f32 v83, v83\n"
        "v_cvt_pk_bf16_f32 v104, v80, v81\n"
        "v_mfma_f32_32x32x16_bf16 v[64:79], v[116:119], a[228:231], v[64:79]\n"
        "v_exp_f32 v84, v84\n"
        "v_exp_f32 v85, v85\n"
        "v_cvt_pk_bf16_f32 v105, v82, v83\n"
        "v_cmp_lt_u32 vcc, 24, v62\n"
        "v_mfma_f32_32x32x16_bf16 v[64:79], v[120:123], a[232:235], v[64:79]\n"
        "s_cbranch_vccnz 5f\n"
        "6:\n"
        "v_exp_f32 v86, v86\n"
        "v_exp_f32 v87, v87\n"
        "v_cvt_pk_bf16_f32 v106, v84, v85\n"
        "v_cmp_lt_u32 vcc, 32, v62\n"
        "v_mfma_f32_32x32x16_bf16 v[64:79], v[124:127], a[236:239], v[64:79]\n"
        "v_exp_f32 v88, v88\n"
        "v_exp_f32 v89, v89\n"
        "v_cvt_pk_bf16_f32 v107, v86, v87\n"
        "v_mfma_f32_32x32x16_bf16 a[64:79], v[96:99], v[144:147], a[64:79]\n"
        "v_exp_f32 v90, v90\n"
        "v_exp_f32 v91, v91\n"
        "v_cvt_pk_bf16_f32 v108, v88, v89\n"
        "v_cndmask_b32 v62, -16, v63, vcc\n"
        "v_mfma_f32_32x32x16_bf16 a[80:95], v[96:99], v[148:151], a[80:95]\n"
        "ds_write_b32 v62, v61 offset:4\n"
        "v_exp_f32 v92, v92\n"
        "v_exp_f32 v93, v93\n"
        "v_cvt_pk_bf16_f32 v109, v90, v91\n"
        "v_mfma_f32_32x32x16_bf16 a[64:79], v[100:103], v[152:155], a[64:79]\n"
        "v_exp_f32 v94, v94\n"
        "v_exp_f32 v95, v95\n"
        "v_cvt_pk_bf16_f32 v110, v92, v93\n"
        "v_mfma_f32_32x32x16_bf16 a[80:95], v[100:103], v[156:159], a[80:95]\n"
        "s_add_u32 m0, s62, 12288\n"
        "v_exp_f32 v64, v64\n"
        "v_exp_f32 v65, v65\n"
        "v_cvt_pk_bf16_f32 v111, v94, v95\n"
        "v_mfma_f32_32x32x16_bf16 v[80:95], v[128:131], a[160:163], 0\n"
        "s_waitcnt lgkmcnt(0)\n"
        "buffer_load_dwordx4 v10, s[44:47], s65 offen offset:0 sc1 lds\n"
        "v_exp_f32 v66, v66\n"
        "v_exp_f32 v67, v67\n"
        "v_cvt_pk_bf16_f32 v96, v64, v65\n"
        "v_mfma_f32_32x32x16_bf16 v[80:95], v[132:135], a[164:167], v[80:95]\n"
        "s_barrier\n"
        "buffer_load_dwordx4 v11, s[44:47], s65 offen offset:1024 sc1 lds\n"
        "v_exp_f32 v68, v68\n"
        "v_exp_f32 v69, v69\n"
        "v_cvt_pk_bf16_f32 v97, v66, v67\n"
        "v_mfma_f32_32x32x16_bf16 v[80:95], v[136:139], a[168:171], v[80:95]\n"
        "buffer_load_dwordx4 v10, s[44:47], s65 offen offset:2048 sc1 lds\n"
        "v_exp_f32 v70, v70\n"
        "v_exp_f32 v71, v71\n"
        "v_cvt_pk_bf16_f32 v98, v68, v69\n"
        "v_mfma_f32_32x32x16_bf16 v[80:95], v[140:143], a[172:175], v[80:95]\n"
        "buffer_load_dwordx4 v11, s[44:47], s65 offen offset:3072 sc1 lds\n"
        "v_exp_f32 v72, v72\n"
        "v_exp_f32 v73, v73\n"
        "v_cvt_pk_bf16_f32 v99, v70, v71\n"
        "v_mfma_f32_32x32x16_bf16 a[96:111], v[104:107], v[144:147], a[96:111]\n"
        "v_exp_f32 v74, v74\n"
        "v_exp_f32 v75, v75\n"
        "v_cvt_pk_bf16_f32 v100, v72, v73\n"
        "v_mfma_f32_32x32x16_bf16 a[112:127], v[104:107], v[148:151], a[112:127]\n"
        "s_add_u32 s65, s65, s66\n"
        "v_exp_f32 v76, v76\n"
        "v_exp_f32 v77, v77\n"
        "v_cvt_pk_bf16_f32 v101, v74, v75\n"
        "v_mfma_f32_32x32x16_bf16 a[96:111], v[108:111], v[152:155], a[96:111]\n"
        "v_exp_f32 v78, v78\n"
        "v_exp_f32 v79, v79\n"
        "v_cvt_pk_bf16_f32 v102, v76, v77\n"
        "v_mfma_f32_32x32x16_bf16 a[112:127], v[108:111], v[156:159], a[112:127]\n"
        "v_exp_f32 v80, v80\n"
        "v_exp_f32 v81, v81\n"
        "v_cvt_pk_bf16_f32 v103, v78, v79\n"
        "v_mfma_f32_32x32x16_bf16 v[64:79], v[128:131], a[176:179], 0\n"
        "v_exp_f32 v82, v82\n"
        "v_exp_f32 v83, v83\n"
        "v_cvt_pk_bf16_f32 v104, v80, v81\n"
        "v_mfma_f32_32x32x16_bf16 v[64:79], v[132:135], a[180:183], v[64:79]\n"
        "v_exp_f32 v84, v84\n"
        "v_exp_f32 v85, v85\n"
        "v_cvt_pk_bf16_f32 v105, v82, v83\n"
        "v_mfma_f32_32x32x16_bf16 v[64:79], v[136:139], a[184:187], v[64:79]\n"
        "v_exp_f32 v86, v86\n"
        "v_exp_f32 v87, v87\n"
        "v_cvt_pk_bf16_f32 v106, v84, v85\n"
        "v_mfma_f32_32x32x16_bf16 v[64:79], v[140:143], a[188:191], v[64:79]\n"
        "v_exp_f32 v88, v88\n"
        "v_exp_f32 v89, v89\n"
        "v_cvt_pk_bf16_f32 v107, v86, v87\n"
        "v_mfma_f32_32x32x16_bf16 a[128:143], v[96:99], v[144:147], a[128:143]\n"
        "v_exp_f32 v90, v90\n"
        "v_exp_f32 v91, v91\n"
        "v_cvt_pk_bf16_f32 v108, v88, v89\n"
        "v_mfma_f32_32x32x16_bf16 a[144:159], v[96:99], v[148:151], a[144:159]\n"
        "v_exp_f32 v92, v92\n"
        "v_exp_f32 v93, v93\n"
        "v_cvt_pk_bf16_f32 v109, v90, v91\n"
        "v_mfma_f32_32x32x16_bf16 a[128:143], v[100:103], v[152:155], a[128:143]\n"
        "v_exp_f32 v94, v94\n"
        "v_exp_f32 v95, v95\n"
        "v_cvt_pk_bf16_f32 v110, v92, v93\n"
        "v_mfma_f32_32x32x16_bf16 a[144:159], v[100:103], v[156:159], a[144:159]\n"
        "v_exp_f32 v64, v64\n"
        "v_exp_f32 v65, v65\n"
        "v_cvt_pk_bf16_f32 v111, v94, v95\n"
        "v_mfma_f32_32x32x16_bf16 v[80:95], v[128:131], a[192:195], 0\n"
        "s_waitcnt vmcnt(12)\n"
        "ds_read_b128 v[112:115], v12 offset:0\n"
        "v_exp_f32 v66, v66\n"
        "v_exp_f32 v67, v67\n"
        "v_cvt_pk_bf16_f32 v96, v64, v65\n"
        "v_mfma_f32_32x32x16_bf16 v[80:95], v[132:135], a[196:199], v[80:95]\n"
        "ds_read_b128 v[116:119], v13 offset:0\n"
        "v_exp_f32 v68, v68\n"
        "v_exp_f32 v69, v69\n"
        "v_cvt_pk_bf16_f32 v97, v66, v67\n"
        "v_mfma_f32_32x32x16_bf16 v[80:95], v[136:139], a[200:203], v[80:95]\n"
        "ds_read_b128 v[120:123], v14 offset:0\n"
        "v_exp_f32 v70, v70\n"
        "v_exp_f32 v71, v71\n"
        "v_cvt_pk_bf16_f32 v98, v68, v69\n"
        "v_mfma_f32_32x32x16_bf16 v[80:95], v[140:143], a[204:207], v[80:95]\n"
        "ds_read_b128 v[124:127], v15 offset:0\n"
        "v_exp_f32 v72, v72\n"
        "v_exp_f32 v73, v73\n"
        "v_cvt_pk_bf16_f32 v99, v70, v71\n"
        "v_mfma_f32_32x32x16_bf16 a[0:15], v[104:107], v[160:163], a[0:15]\n"
        "ds_read_b64_tr_b16 v[144:145], v16 offset:0\n"
        "v_exp_f32 v74, v74\n"
        "v_exp_f32 v75, v75\n"
        "v_cvt_pk_bf16_f32 v100, v72, v73\n"
        "v_mfma_f32_32x32x16_bf16 a[16:31], v[104:107], v[164:167], a[16:31]\n"
        "ds_read_b64_tr_b16 v[146:147], v17 offset:0\n"
        "v_exp_f32 v76, v76\n"
        "v_exp_f32 v77, v77\n"
        "v_cvt_pk_bf16_f32 v101, v74, v75\n"
        "v_mfma_f32_32x32x16_bf16 a[0:15], v[108:111], v[168:171], a[0:15]\n"
        "ds_read_b64_tr_b16 v[148:149], v18 offset:0\n"
        "v_exp_f32 v78, v78\n"
        "v_exp_f32 v79, v79\n"
        "v_cvt_pk_bf16_f32 v102, v76, v77\n"
        "v_mfma_f32_32x32x16_bf16 a[16:31], v[108:111], v[172:175], a[16:31]\n"
        "ds_read_b64_tr_b16 v[150:151], v19 offset:0\n"
        "v_exp_f32 v80, v80\n"
        "v_exp_f32 v81, v81\n"
        "v_cvt_pk_bf16_f32 v103, v78, v79\n"
        "34:\n"
        "v_mfma_f32_32x32x16_bf16 v[64:79], v[128:131], a[208:211], 0\n"
        "ds_read_b64_tr_b16 v[152:153], v16 offset:2048\n"
        "v_exp_f32 v82, v82\n"
        "v_exp_f32 v83, v83\n"
        "v_cvt_pk_bf16_f32 v104, v80, v81\n"
        "v_mfma_f32_32x32x16_bf16 v[64:79], v[132:135], a[212:215], v[64:79]\n"
        "ds_read_b64_tr_b16 v[154:155], v17 offset:2048\n"
        "v_exp_f32 v84, v84\n"
        "v_exp_f32 v85, v85\n"
        "v_cvt_pk_bf16_f32 v105, v82, v83\n"
        "v_mfma_f32_32x32x16_bf16 v[64:79], v[136:139], a[216:219], v[64:79]\n"
        "ds_read_b64_tr_b16 v[156:157], v18 offset:2048\n"
        "v_exp_f32 v86, v86\n"
        "v_exp_f32 v87, v87\n"
        "v_cvt_pk_bf16_f32 v106, v84, v85\n"
        "v_mfma_f32_32x32x16_bf16 v[64:79], v[140:143], a[220:223], v[64:79]\n"
        "ds_read_b64_tr_b16 v[158:159], v19 offset:2048\n"
        "v_exp_f32 v88, v88\n"
        "v_exp_f32 v89, v89\n"
        "v_cvt_pk_bf16_f32 v107, v86, v87\n"
        "v_mfma_f32_32x32x16_bf16 a[32:47], v[96:99], v[160:163], a[32:47]\n"
        "ds_read_b128 v[224:227], v35 offset:0\n"
        "v_exp_f32 v90, v90\n"
        "v_exp_f32 v91, v91\n"
        "v_cvt_pk_bf16_f32 v108, v88, v89\n"
        "v_mfma_f32_32x32x16_bf16 a[48:63], v[96:99], v[164:167], a[48:63]\n"
        "ds_read_b128 v[228:231], v35 offset:16\n"
        "v_exp_f32 v92, v92\n"
        "v_exp_f32 v93, v93\n"
        "v_cvt_pk_bf16_f32 v109, v90, v91\n"
        "v_mov_b32 v239, 0\n"
        "v_mfma_f32_32x32x16_bf16 a[32:47], v[100:103], v[168:171], a[32:47]\n"
        "v_exp_f32 v94, v94\n"
        "v_exp_f32 v95, v95\n"
        "v_cvt_pk_bf16_f32 v110, v92, v93\n"
        "v_mov_b32 v236, 0\n"
        "v_mov_b32 v240, 0\n"
        "v_mfma_f32_32x32x16_bf16 a[48:63], v[100:103], v[172:175], a[48:63]\n"
        "v_exp_f32 v64, v64\n"
        "v_exp_f32 v65, v65\n"
        "v_cvt_pk_bf16_f32 v111, v94, v95\n"
        "v_mov_b32 v237, 0\n"
        "v_mov_b32 v241, 0\n"
        "v_mfma_f32_32x32x16_bf16 v[80:95], v[128:131], a[224:227], 0\n"
        "v_exp_f32 v66, v66\n"
        "v_exp_f32 v67, v67\n"
        "v_cvt_pk_bf16_f32 v96, v64, v65\n"
        "v_mov_b32 v238, 0\n"
        "v_mov_b32 v242, 0\n"
        "v_mfma_f32_32x32x16_bf16 v[80:95], v[132:135], a[228:231], v[80:95]\n"
        "s_waitcnt lgkmcnt(0)\n"
        "v_exp_f32 v68, v68\n"
        "v_exp_f32 v69, v69\n"
        "v_cvt_pk_bf16_f32 v97, v66, v67\n"
        "v_mad_u32_u16 v232, v224, s70, v37 op_sel:[0,0,0,0]\n"
        "v_mov_b32 v243, 0\n"
        "v_mfma_f32_32x32x16_bf16 v[80:95], v[136:139], a[232:235], v[80:95]\n"
        "buffer_load_dwordx4 v[176:179], v232, s[40:43], 0 offen sc1\n"
        "v_exp_f32 v70, v70\n"
        "v_exp_f32 v71, v71\n"
        "v_cvt_pk_bf16_f32 v98, v68, v69\n"
        "v_mov_b32 v244, 0\n"
        "v_mfma_f32_32x32x16_bf16 v[80:95], v[140:143], a[236:239], v[80:95]\n"
        "v_exp_f32 v72, v72\n"
        "v_exp_f32 v73, v73\n"
        "v_cvt_pk_bf16_f32 v99, v70, v71\n"
        "v_mad_u32_u16 v233, v224, s70, v37 op_sel:[1,0,0,0]\n"
        "v_mov_b32 v245, 0\n"
        "v_mfma_f32_32x32x16_bf16 a[64:79], v[104:107], v[160:163], a[64:79]\n"
        "buffer_load_dwordx4 v[180:183], v233, s[40:43], 0 offen sc1\n"
        "v_exp_f32 v74, v74\n"
        "v_exp_f32 v75, v75\n"
        "v_cvt_pk_bf16_f32 v100, v72, v73\n"
        "v_mov_b32 v246, 0\n"
        "v_mfma_f32_32x32x16_bf16 a[80:95], v[104:107], v[164:167], a[80:95]\n"
        "v_exp_f32 v76, v76\n"
        "v_exp_f32 v77, v77\n"
        "v_cvt_pk_bf16_f32 v101, v74, v75\n"
        "v_mad_u32_u16 v232, v225, s70, v37 op_sel:[0,0,0,0]\n"
        "v_mov_b32 v247, 0\n"
        "v_mfma_f32_32x32x16_bf16 a[64:79], v[108:111], v[168:171], a[64:79]\n"
        "buffer_load_dwordx4 v[184:187], v232, s[40:43], 0 offen sc1\n"
        "v_exp_f32 v78, v78\n"
        "v_exp_f32 v79, v79\n"
        "v_cvt_pk_bf16_f32 v102, v76, v77\n"
        "v_mfma_f32_32x32x16_bf16 a[80:95], v[108:111], v[172:175], a[80:95]\n"
        "s_add_u32 m0, s62, 0\n"
        "v_exp_f32 v80, v80\n"
        "v_exp_f32 v81, v81\n"
        "v_cvt_pk_bf16_f32 v103, v78, v79\n"
        "v_mad_u32_u16 v233, v225, s70, v37 op_sel:[1,0,0,0]\n"
        "v_mfma_f32_32x32x16_bf16 v[64:79], v[112:115], a[160:163], 0\n"
        "buffer_load_dwordx4 v10, s[44:47], s65 offen offset:0 sc1 lds\n"
        "v_exp_f32 v82, v82\n"
        "v_exp_f32 v83, v83\n"
        "v_cvt_pk_bf16_f32 v104, v80, v81\n"
        "v_mfma_f32_32x32x16_bf16 v[64:79], v[116:119], a[164:167], v[64:79]\n"
        "buffer_load_dwordx4 v11, s[44:47], s65 offen offset:1024 sc1 lds\n"
        "v_exp_f32 v84, v84\n"
        "v_exp_f32 v85, v85\n"
        "v_cvt_pk_bf16_f32 v105, v82, v83\n"
        "v_mfma_f32_32x32x16_bf16 v[64:79], v[120:123], a[168:171], v[64:79]\n"
        "buffer_load_dwordx4 v10, s[44:47], s65 offen offset:2048 sc1 lds\n"
        "v_exp_f32 v86, v86\n"
        "v_exp_f32 v87, v87\n"
        "v_cvt_pk_bf16_f32 v106, v84, v85\n"
        "v_mfma_f32_32x32x16_bf16 v[64:79], v[124:127], a[172:175], v[64:79]\n"
        "buffer_load_dwordx4 v11, s[44:47], s65 offen offset:3072 sc1 lds\n"
        "v_exp_f32 v88, v88\n"
        "v_exp_f32 v89, v89\n"
        "v_cvt_pk_bf16_f32 v107, v86, v87\n"
        "v_mfma_f32_32x32x16_bf16 a[96:111], v[96:99], v[160:163], a[96:111]\n"
        "buffer_load_dwordx4 v[188:191], v233, s[40:43], 0 offen sc1\n"
        "v_exp_f32 v90, v90\n"
        "v_exp_f32 v91, v91\n"
        "v_cvt_pk_bf16_f32 v108, v88, v89\n"
        "v_mfma_f32_32x32x16_bf16 a[112:127], v[96:99], v[164:167], a[112:127]\n"
        "s_add_u32 s65, s65, s66\n"
        "v_exp_f32 v92, v92\n"
        "v_exp_f32 v93, v93\n"
        "v_cvt_pk_bf16_f32 v109, v90, v91\n"
        "v_mad_u32_u16 v232, v226, s70, v37 op_sel:[0,0,0,0]\n"
        "v_mfma_f32_32x32x16_bf16 a[96:111], v[100:103], v[168:171], a[96:111]\n"
        "buffer_load_dwordx4 v[192:195], v232, s[40:43], 0 offen sc1\n"
        "v_exp_f32 v94, v94\n"
        "v_exp_f32 v95, v95\n"
        "v_cvt_pk_bf16_f32 v110, v92, v93\n"
        "v_mfma_f32_32x32x16_bf16 a[112:127], v[100:103], v[172:175], a[112:127]\n"
        "v_exp_f32 v64, v64\n"
        "v_exp_f32 v65, v65\n"
        "v_cvt_pk_bf16_f32 v111, v94, v95\n"
        "v_mad_u32_u16 v233, v226, s70, v37 op_sel:[1,0,0,0]\n"
        "v_mfma_f32_32x32x16_bf16 v[80:95], v[112:115], a[176:179], 0\n"
        "buffer_load_dwordx4 v[196:199], v233, s[40:43], 0 offen sc1\n"
        "v_exp_f32 v66, v66\n"
        "v_exp_f32 v67, v67\n"
        "v_cvt_pk_bf16_f32 v96, v64, v65\n"
        "v_mfma_f32_32x32x16_bf16 v[80:95], v[116:119], a[180:183], v[80:95]\n"
        "v_exp_f32 v68, v68\n"
        "v_exp_f32 v69, v69\n"
        "v_cvt_pk_bf16_f32 v97, v66, v67\n"
        "v_mad_u32_u16 v232, v227, s70, v37 op_sel:[0,0,0,0]\n"
        "v_mfma_f32_32x32x16_bf16 v[80:95], v[120:123], a[184:187], v[80:95]\n"
        "buffer_load_dwordx4 v[200:203], v232, s[40:43], 0 offen sc1\n"
        "v_exp_f32 v70, v70\n"
        "v_exp_f32 v71, v71\n"
        "v_cvt_pk_bf16_f32 v98, v68, v69\n"
        "v_mfma_f32_32x32x16_bf16 v[80:95], v[124:127], a[188:191], v[80:95]\n"
        "v_exp_f32 v72, v72\n"
        "v_exp_f32 v73, v73\n"
        "v_cvt_pk_bf16_f32 v99, v70, v71\n"
        "v_mad_u32_u16 v233, v227, s70, v37 op_sel:[1,0,0,0]\n"
        "v_mfma_f32_32x32x16_bf16 a[128:143], v[104:107], v[160:163], a[128:143]\n"
        "buffer_load_dwordx4 v[204:207], v233, s[40:43], 0 offen sc1\n"
        "v_exp_f32 v74, v74\n"
        "v_exp_f32 v75, v75\n"
        "v_cvt_pk_bf16_f32 v100, v72, v73\n"
        "v_mfma_f32_32x32x16_bf16 a[144:159], v[104:107], v[164:167], a[144:159]\n"
        "v_exp_f32 v76, v76\n"
        "v_exp_f32 v77, v77\n"
        "v_cvt_pk_bf16_f32 v101, v74, v75\n"
        "v_mad_u32_u16 v232, v228, s70, v37 op_sel:[0,0,0,0]\n"
        "v_mfma_f32_32x32x16_bf16 a[128:143], v[108:111], v[168:171], a[128:143]\n"
        "buffer_load_dwordx4 v[208:211], v232, s[40:43], 0 offen sc1\n"
        "v_exp_f32 v78, v78\n"
        "v_exp_f32 v79, v79\n"
        "v_cvt_pk_bf16_f32 v102, v76, v77\n"
        "v_mfma_f32_32x32x16_bf16 a[144:159], v[108:111], v[172:175], a[144:159]\n"
        "v_exp_f32 v80, v80\n"
        "v_exp_f32 v81, v81\n"
        "v_cvt_pk_bf16_f32 v103, v78, v79\n"
        "v_mad_u32_u16 v233, v228, s70, v37 op_sel:[1,0,0,0]\n"
        "v_mfma_f32_32x32x16_bf16 v[64:79], v[112:115], a[192:195], 0\n"
        "s_waitcnt vmcnt(21)\n"
        "ds_read_b128 v[128:131], v12 offset:4096\n"
        "v_exp_f32 v82, v82\n"
        "v_exp_f32 v83, v83\n"
        "v_cvt_pk_bf16_f32 v104, v80, v81\n"
        "v_mfma_f32_32x32x16_bf16 v[64:79], v[116:119], a[196:199], v[64:79]\n"
        "ds_read_b128 v[132:135], v13 offset:4096\n"
        "v_exp_f32 v84, v84\n"
        "v_exp_f32 v85, v85\n"
        "v_cvt_pk_bf16_f32 v105, v82, v83\n"
        "v_mfma_f32_32x32x16_bf16 v[64:79], v[120:123], a[200:203], v[64:79]\n"
        "ds_read_b128 v[136:139], v14 offset:4096\n"
        "v_exp_f32 v86, v86\n"
        "v_exp_f32 v87, v87\n"
        "v_cvt_pk_bf16_f32 v106, v84, v85\n"
        "v_mfma_f32_32x32x16_bf16 v[64:79], v[124:127], a[204:207], v[64:79]\n"
        "ds_read_b128 v[140:143], v15 offset:4096\n"
        "v_exp_f32 v88, v88\n"
        "v_exp_f32 v89, v89\n"
        "v_cvt_pk_bf16_f32 v107, v86, v87\n"
        "v_mfma_f32_32x32x16_bf16 a[0:15], v[96:99], v[144:147], a[0:15]\n"
        "ds_read_b64_tr_b16 v[160:161], v16 offset:4096\n"
        "v_exp_f32 v90, v90\n"
        "v_exp_f32 v91, v91\n"
        "v_cvt_pk_bf16_f32 v108, v88, v89\n"
        "v_mfma_f32_32x32x16_bf16 a[16:31], v[96:99], v[148:151], a[16:31]\n"
        "ds_read_b64_tr_b16 v[162:163], v17 offset:4096\n"
        "v_exp_f32 v92, v92\n"
        "v_exp_f32 v93, v93\n"
        "v_cvt_pk_bf16_f32 v109, v90, v91\n"
        "v_mfma_f32_32x32x16_bf16 a[0:15], v[100:103], v[152:155], a[0:15]\n"
        "ds_read_b64_tr_b16 v[164:165], v18 offset:4096\n"
        "v_exp_f32 v94, v94\n"
        "v_exp_f32 v95, v95\n"
        "v_cvt_pk_bf16_f32 v110, v92, v93\n"
        "v_mfma_f32_32x32x16_bf16 a[16:31], v[100:103], v[156:159], a[16:31]\n"
        "ds_read_b64_tr_b16 v[166:167], v19 offset:4096\n"
        "v_exp_f32 v64, v64\n"
        "v_exp_f32 v65, v65\n"
        "v_cvt_pk_bf16_f32 v111, v94, v95\n"
        "v_mfma_f32_32x32x16_bf16 v[80:95], v[112:115], a[208:211], 0\n"
        "ds_read_b64_tr_b16 v[168:169], v16 offset:6144\n"
        "v_exp_f32 v66, v66\n"
        "v_exp_f32 v67, v67\n"
        "v_cvt_pk_bf16_f32 v96, v64, v65\n"
        "v_mfma_f32_32x32x16_bf16 v[80:95], v[116:119], a[212:215], v[80:95]\n"
        "ds_read_b64_tr_b16 v[170:171], v17 offset:6144\n"
        "v_exp_f32 v68, v68\n"
        "v_exp_f32 v69, v69\n"
        "v_cvt_pk_bf16_f32 v97, v66, v67\n"
        "v_mfma_f32_32x32x16_bf16 v[80:95], v[120:123], a[216:219], v[80:95]\n"
        "ds_read_b64_tr_b16 v[172:173], v18 offset:6144\n"
        "v_exp_f32 v70, v70\n"
        "v_exp_f32 v71, v71\n"
        "v_cvt_pk_bf16_f32 v98, v68, v69\n"
        "v_mfma_f32_32x32x16_bf16 v[80:95], v[124:127], a[220:223], v[80:95]\n"
        "ds_read_b64_tr_b16 v[174:175], v19 offset:6144\n"
        "v_exp_f32 v72, v72\n"
        "v_exp_f32 v73, v73\n"
        "v_cvt_pk_bf16_f32 v99, v70, v71\n"
        "v_mfma_f32_32x32x16_bf16 a[32:47], v[104:107], v[144:147], a[32:47]\n"
        "ds_read_b128 v[224:227], v35 offset:544\n"
        "v_exp_f32 v74, v74\n"
        "v_exp_f32 v75, v75\n"
        "v_cvt_pk_bf16_f32 v100, v72, v73\n"
        "v_mfma_f32_32x32x16_bf16 a[48:63], v[104:107], v[148:151], a[48:63]\n"
        "buffer_load_dwordx4 v[212:215], v233, s[40:43], 0 offen sc1\n"
        "v_exp_f32 v76, v76\n"
        "v_exp_f32 v77, v77\n"
        "v_cvt_pk_bf16_f32 v101, v74, v75\n"
        "v_mfma_f32_32x32x16_bf16 a[32:47], v[108:111], v[152:155], a[32:47]\n"
        "v_exp_f32 v78, v78\n"
        "v_exp_f32 v79, v79\n"
        "v_cvt_pk_bf16_f32 v102, v76, v77\n"
        "v_mad_u32_u16 v232, v229, s70, v37 op_sel:[0,0,0,0]\n"
        "v_mfma_f32_32x32x16_bf16 a[48:63], v[108:111], v[156:159], a[48:63]\n"
        "buffer_load_dwordx4 v[216:219], v232, s[40:43], 0 offen sc1\n"
        "v_exp_f32 v80, v80\n"
        "v_exp_f32 v81, v81\n"
        "v_cvt_pk_bf16_f32 v103, v78, v79\n"
        "v_mfma_f32_32x32x16_bf16 v[64:79], v[112:115], a[224:227], 0\n"
        "v_exp_f32 v82, v82\n"
        "v_exp_f32 v83, v83\n"
        "v_cvt_pk_bf16_f32 v104, v80, v81\n"
        "v_mad_u32_u16 v233, v229, s70, v37 op_sel:[1,0,0,0]\n"
        "v_mfma_f32_32x32x16_bf16 v[64:79], v[116:119], a[228:231], v[64:79]\n"
        "buffer_load_dwordx4 v[220:223], v233, s[40:43], 0 offen sc1\n"
        "v_exp_f32 v84, v84\n"
        "v_exp_f32 v85, v85\n"
        "v_cvt_pk_bf16_f32 v105, v82, v83\n"
        "v_mfma_f32_32x32x16_bf16 v[64:79], v[120:123], a[232:235], v[64:79]\n"
        "v_exp_f32 v86, v86\n"
        "v_exp_f32 v87, v87\n"
        "v_cvt_pk_bf16_f32 v106, v84, v85\n"
        "v_mad_u32_u16 v232, v230, s70, v37 op_sel:[0,0,0,0]\n"
        "v_mfma_f32_32x32x16_bf16 v[64:79], v[124:127], a[236:239], v[64:79]\n"
        "v_exp_f32 v88, v88\n"
        "v_exp_f32 v89, v89\n"
        "v_cvt_pk_bf16_f32 v107, v86, v87\n"
        "v_mad_u32_u16 v233, v230, s70, v37 op_sel:[1,0,0,0]\n"
        "v_mfma_f32_32x32x16_bf16 a[64:79], v[96:99], v[144:147], a[64:79]\n"
        "s_waitcnt vmcnt(14)\n"
        "v_exp_f32 v90, v90\n"
        "v_exp_f32 v91, v91\n"
        "v_cvt_pk_bf16_f32 v108, v88, v89\n"
        "v_lshlrev_b32 v234, 4, v9\n"
        "v_smfmac_f32_16x16x64_bf16 v[236:239], a[240:243], v[176:183], v54\n"
        "buffer_load_dwordx4 v[176:179], v232, s[40:43], 0 offen sc1\n"
        "buffer_load_dwordx4 v[180:183], v233, s[40:43], 0 offen sc1\n"
        "v_mfma_f32_32x32x16_bf16 a[80:95], v[96:99], v[148:151], a[80:95]\n"
        "s_mul_i32 s77, s58, 15360\n"
        "v_exp_f32 v92, v92\n"
        "v_exp_f32 v93, v93\n"
        "v_cvt_pk_bf16_f32 v109, v90, v91\n"
        "v_mad_u32_u16 v232, v231, s70, v37 op_sel:[0,0,0,0]\n"
        "v_mfma_f32_32x32x16_bf16 a[64:79], v[100:103], v[152:155], a[64:79]\n"
        "s_waitcnt lgkmcnt(1)\n"
        "v_exp_f32 v94, v94\n"
        "v_exp_f32 v95, v95\n"
        "v_cvt_pk_bf16_f32 v110, v92, v93\n"
        "v_mad_u32_u16 v233, v231, s70, v37 op_sel:[1,0,0,0]\n"
        "v_mfma_f32_32x32x16_bf16 a[80:95], v[100:103], v[156:159], a[80:95]\n"
        "s_add_u32 m0, s62, 4096\n"
        "v_exp_f32 v64, v64\n"
        "v_exp_f32 v65, v65\n"
        "v_cvt_pk_bf16_f32 v111, v94, v95\n"
        "v_mfma_f32_32x32x16_bf16 v[80:95], v[128:131], a[160:163], 0\n"
        "s_add_u32 s77, s77, 4434944\n"
        "buffer_load_dwordx4 v10, s[44:47], s65 offen offset:0 sc1 lds\n"
        "v_exp_f32 v66, v66\n"
        "v_exp_f32 v67, v67\n"
        "v_cvt_pk_bf16_f32 v96, v64, v65\n"
        "v_mfma_f32_32x32x16_bf16 v[80:95], v[132:135], a[164:167], v[80:95]\n"
        "s_waitcnt vmcnt(11)\n"
        "buffer_load_dwordx4 v11, s[44:47], s65 offen offset:1024 sc1 lds\n"
        "v_exp_f32 v68, v68\n"
        "v_exp_f32 v69, v69\n"
        "v_cvt_pk_bf16_f32 v97, v66, v67\n"
        "v_smfmac_f32_16x16x64_bf16 v[236:239], a[240:243], v[184:191], v54\n"
        "s_waitcnt lgkmcnt(0)\n"
        "buffer_load_dwordx4 v[184:187], v232, s[40:43], 0 offen sc1\n"
        "buffer_load_dwordx4 v[188:191], v233, s[40:43], 0 offen sc1\n"
        "v_mfma_f32_32x32x16_bf16 v[80:95], v[136:139], a[168:171], v[80:95]\n"
        "s_lshl_b32 s78, s59, 10\n"
        "buffer_load_dwordx4 v10, s[44:47], s65 offen offset:2048 sc1 lds\n"
        "v_exp_f32 v70, v70\n"
        "v_exp_f32 v71, v71\n"
        "v_cvt_pk_bf16_f32 v98, v68, v69\n"
        "v_mfma_f32_32x32x16_bf16 v[80:95], v[140:143], a[172:175], v[80:95]\n"
        "s_add_u32 s76, s77, s78\n"
        "buffer_load_dwordx4 v11, s[44:47], s65 offen offset:3072 sc1 lds\n"
        "v_exp_f32 v72, v72\n"
        "v_exp_f32 v73, v73\n"
        "v_cvt_pk_bf16_f32 v99, v70, v71\n"
        "v_mfma_f32_32x32x16_bf16 a[96:111], v[104:107], v[144:147], a[96:111]\n"
        "v_exp_f32 v74, v74\n"
        "v_exp_f32 v75, v75\n"
        "v_cvt_pk_bf16_f32 v100, v72, v73\n"
        "v_mad_u32_u16 v232, v224, s70, v37 op_sel:[0,0,0,0]\n"
        "v_mfma_f32_32x32x16_bf16 a[112:127], v[104:107], v[148:151], a[112:127]\n"
        "s_add_u32 s65, s65, s66\n"
        "v_exp_f32 v76, v76\n"
        "v_exp_f32 v77, v77\n"
        "v_cvt_pk_bf16_f32 v101, v74, v75\n"
        "v_mad_u32_u16 v233, v224, s70, v37 op_sel:[1,0,0,0]\n"
        "v_mfma_f32_32x32x16_bf16 a[96:111], v[108:111], v[152:155], a[96:111]\n"
        "v_exp_f32 v78, v78\n"
        "v_exp_f32 v79, v79\n"
        "v_cvt_pk_bf16_f32 v102, v76, v77\n"
        "v_add_u32 v44, s76, v234\n"
        "v_mfma_f32_32x32x16_bf16 a[112:127], v[108:111], v[156:159], a[112:127]\n"
        "s_waitcnt vmcnt(14)\n"
        "v_exp_f32 v80, v80\n"
        "v_exp_f32 v81, v81\n"
        "v_cvt_pk_bf16_f32 v103, v78, v79\n"
        "v_smfmac_f32_16x16x64_bf16 v[236:239], a[240:243], v[192:199], v54\n"
        "buffer_load_dwordx4 v[192:195], v232, s[40:43], 0 offen sc1\n"
        "buffer_load_dwordx4 v[196:199], v233, s[40:43], 0 offen sc1\n"
        "v_mfma_f32_32x32x16_bf16 v[64:79], v[128:131], a[176:179], 0\n"
        "ds_read_b128 v[228:231], v35 offset:560\n"
        "v_exp_f32 v82, v82\n"
        "v_exp_f32 v83, v83\n"
        "v_cvt_pk_bf16_f32 v104, v80, v81\n"
        "v_mfma_f32_32x32x16_bf16 v[64:79], v[132:135], a[180:183], v[64:79]\n"
        "s_cmp_eq_u32 s59, 0\n"
        "s_cselect_b32 s75, 0, s83\n"
        "v_exp_f32 v84, v84\n"
        "v_exp_f32 v85, v85\n"
        "v_cvt_pk_bf16_f32 v105, v82, v83\n"
        "v_mad_u32_u16 v232, v225, s70, v37 op_sel:[0,0,0,0]\n"
        "v_mfma_f32_32x32x16_bf16 v[64:79], v[136:139], a[184:187], v[64:79]\n"
        "s_add_u32 s78, s75, 4096\n"
        "v_exp_f32 v86, v86\n"
        "v_exp_f32 v87, v87\n"
        "v_cvt_pk_bf16_f32 v106, v84, v85\n"
        "v_mad_u32_u16 v233, v225, s70, v37 op_sel:[1,0,0,0]\n"
        "v_mfma_f32_32x32x16_bf16 v[64:79], v[140:143], a[188:191], v[64:79]\n"
        "v_exp_f32 v88, v88\n"
        "v_exp_f32 v89, v89\n"
        "v_cvt_pk_bf16_f32 v107, v86, v87\n"
        "v_add_u32 v49, s78, v44\n"
        "v_mfma_f32_32x32x16_bf16 a[128:143], v[96:99], v[144:147], a[128:143]\n"
        "s_waitcnt vmcnt(14)\n"
        "v_exp_f32 v90, v90\n"
        "v_exp_f32 v91, v91\n"
        "v_cvt_pk_bf16_f32 v108, v88, v89\n"
        "v_add_u32 v43, 4429824, v234\n"
        "v_smfmac_f32_16x16x64_bf16 v[236:239], a[240:243], v[200:207], v54\n"
        "buffer_load_dwordx4 v[200:203], v232, s[40:43], 0 offen sc1\n"
        "buffer_load_dwordx4 v[204:207], v233, s[40:43], 0 offen sc1\n"
        "v_mfma_f32_32x32x16_bf16 a[144:159], v[96:99], v[148:151], a[144:159]\n"
        "s_add_u32 s78, s59, 4\n"
        "v_exp_f32 v92, v92\n"
        "v_exp_f32 v93, v93\n"
        "v_cvt_pk_bf16_f32 v109, v90, v91\n"
        "v_mad_u32_u16 v232, v226, s70, v37 op_sel:[0,0,0,0]\n"
        "v_mfma_f32_32x32x16_bf16 a[128:143], v[100:103], v[152:155], a[128:143]\n"
        "v_exp_f32 v94, v94\n"
        "v_exp_f32 v95, v95\n"
        "v_cvt_pk_bf16_f32 v110, v92, v93\n"
        "v_mad_u32_u16 v233, v226, s70, v37 op_sel:[1,0,0,0]\n"
        "v_mfma_f32_32x32x16_bf16 a[144:159], v[100:103], v[156:159], a[144:159]\n"
        "v_exp_f32 v64, v64\n"
        "v_exp_f32 v65, v65\n"
        "v_cvt_pk_bf16_f32 v111, v94, v95\n"
        "v_and_b32 v50, 3, v9\n"
        "v_mfma_f32_32x32x16_bf16 v[80:95], v[128:131], a[192:195], 0\n"
        "ds_read_b128 v[112:115], v12 offset:8192\n"
        "v_exp_f32 v66, v66\n"
        "v_exp_f32 v67, v67\n"
        "v_cvt_pk_bf16_f32 v96, v64, v65\n"
        "v_mfma_f32_32x32x16_bf16 v[80:95], v[132:135], a[196:199], v[80:95]\n"
        "ds_read_b128 v[116:119], v13 offset:8192\n"
        "v_exp_f32 v68, v68\n"
        "v_exp_f32 v69, v69\n"
        "v_cvt_pk_bf16_f32 v97, v66, v67\n"
        "v_mfma_f32_32x32x16_bf16 v[80:95], v[136:139], a[200:203], v[80:95]\n"
        "s_waitcnt vmcnt(14)\n"
        "ds_read_b128 v[120:123], v14 offset:8192\n"
        "v_exp_f32 v70, v70\n"
        "v_exp_f32 v71, v71\n"
        "v_cvt_pk_bf16_f32 v98, v68, v69\n"
        "v_smfmac_f32_16x16x64_bf16 v[236:239], a[240:243], v[208:215], v54\n"
        "buffer_load_dwordx4 v[208:211], v232, s[40:43], 0 offen sc1\n"
        "buffer_load_dwordx4 v[212:215], v233, s[40:43], 0 offen sc1\n"
        "v_mfma_f32_32x32x16_bf16 v[80:95], v[140:143], a[204:207], v[80:95]\n"
        "ds_read_b128 v[124:127], v15 offset:8192\n"
        "v_exp_f32 v72, v72\n"
        "v_exp_f32 v73, v73\n"
        "v_cvt_pk_bf16_f32 v99, v70, v71\n"
        "v_mad_u32_u16 v232, v227, s70, v37 op_sel:[0,0,0,0]\n"
        "v_mfma_f32_32x32x16_bf16 a[0:15], v[104:107], v[160:163], a[0:15]\n"
        "ds_read_b64_tr_b16 v[144:145], v16 offset:8192\n"
        "v_exp_f32 v74, v74\n"
        "v_exp_f32 v75, v75\n"
        "v_cvt_pk_bf16_f32 v100, v72, v73\n"
        "v_mad_u32_u16 v233, v227, s70, v37 op_sel:[1,0,0,0]\n"
        "v_mfma_f32_32x32x16_bf16 a[16:31], v[104:107], v[164:167], a[16:31]\n"
        "ds_read_b64_tr_b16 v[146:147], v17 offset:8192\n"
        "v_exp_f32 v76, v76\n"
        "v_exp_f32 v77, v77\n"
        "v_cvt_pk_bf16_f32 v101, v74, v75\n"
        "v_mfma_f32_32x32x16_bf16 a[0:15], v[108:111], v[168:171], a[0:15]\n"
        "ds_read_b64_tr_b16 v[148:149], v18 offset:8192\n"
        "v_exp_f32 v78, v78\n"
        "v_exp_f32 v79, v79\n"
        "v_cvt_pk_bf16_f32 v102, v76, v77\n"
        "v_mfma_f32_32x32x16_bf16 a[16:31], v[108:111], v[172:175], a[16:31]\n"
        "s_waitcnt vmcnt(14)\n"
        "ds_read_b64_tr_b16 v[150:151], v19 offset:8192\n"
        "v_exp_f32 v80, v80\n"
        "v_exp_f32 v81, v81\n"
        "v_cvt_pk_bf16_f32 v103, v78, v79\n"
        "v_smfmac_f32_16x16x64_bf16 v[236:239], a[240:243], v[216:223], v54\n"
        "s_waitcnt lgkmcnt(8)\n"
        "buffer_load_dwordx4 v[216:219], v232, s[40:43], 0 offen sc1\n"
        "buffer_load_dwordx4 v[220:223], v233, s[40:43], 0 offen sc1\n"
        "v_mfma_f32_32x32x16_bf16 v[64:79], v[128:131], a[208:211], 0\n"
        "ds_read_b64_tr_b16 v[152:153], v16 offset:10240\n"
        "v_exp_f32 v82, v82\n"
        "v_exp_f32 v83, v83\n"
        "v_cvt_pk_bf16_f32 v104, v80, v81\n"
        "v_mfma_f32_32x32x16_bf16 v[64:79], v[132:135], a[212:215], v[64:79]\n"
        "ds_read_b64_tr_b16 v[154:155], v17 offset:10240\n"
        "v_exp_f32 v84, v84\n"
        "v_exp_f32 v85, v85\n"
        "v_cvt_pk_bf16_f32 v105, v82, v83\n"
        "v_mfma_f32_32x32x16_bf16 v[64:79], v[136:139], a[216:219], v[64:79]\n"
        "ds_read_b64_tr_b16 v[156:157], v18 offset:10240\n"
        "v_exp_f32 v86, v86\n"
        "v_exp_f32 v87, v87\n"
        "v_cvt_pk_bf16_f32 v106, v84, v85\n"
        "v_mfma_f32_32x32x16_bf16 v[64:79], v[140:143], a[220:223], v[64:79]\n"
        "ds_read_b64_tr_b16 v[158:159], v19 offset:10240\n"
        "v_exp_f32 v88, v88\n"
        "v_exp_f32 v89, v89\n"
        "v_cvt_pk_bf16_f32 v107, v86, v87\n"
        "v_mfma_f32_32x32x16_bf16 a[32:47], v[96:99], v[160:163], a[32:47]\n"
        "v_exp_f32 v90, v90\n"
        "v_exp_f32 v91, v91\n"
        "v_cvt_pk_bf16_f32 v108, v88, v89\n"
        "v_mad_u32_u16 v232, v228, s70, v37 op_sel:[0,0,0,0]\n"
        "v_mfma_f32_32x32x16_bf16 a[48:63], v[96:99], v[164:167], a[48:63]\n"
        "s_waitcnt vmcnt(14)\n"
        "v_exp_f32 v92, v92\n"
        "v_exp_f32 v93, v93\n"
        "v_cvt_pk_bf16_f32 v109, v90, v91\n"
        "v_mad_u32_u16 v233, v228, s70, v37 op_sel:[1,0,0,0]\n"
        "v_smfmac_f32_16x16x64_bf16 v[236:239], a[240:243], v[176:183], v54\n"
        "buffer_load_dwordx4 v[176:179], v232, s[40:43], 0 offen sc1\n"
        "buffer_load_dwordx4 v[180:183], v233, s[40:43], 0 offen sc1\n"
        "v_mfma_f32_32x32x16_bf16 a[32:47], v[100:103], v[168:171], a[32:47]\n"
        "ds_read_b128 v[224:227], v35 offset:1088\n"
        "v_exp_f32 v94, v94\n"
        "v_exp_f32 v95, v95\n"
        "v_cvt_pk_bf16_f32 v110, v92, v93\n"
        "v_mfma_f32_32x32x16_bf16 a[48:63], v[100:103], v[172:175], a[48:63]\n"
        "v_exp_f32 v64, v64\n"
        "v_exp_f32 v65, v65\n"
        "v_cvt_pk_bf16_f32 v111, v94, v95\n"
        "v_mad_u32_u16 v232, v229, s70, v37 op_sel:[0,0,0,0]\n"
        "v_mfma_f32_32x32x16_bf16 v[80:95], v[128:131], a[224:227], 0\n"
        "v_exp_f32 v66, v66\n"
        "v_exp_f32 v67, v67\n"
        "v_cvt_pk_bf16_f32 v96, v64, v65\n"
        "v_mad_u32_u16 v233, v229, s70, v37 op_sel:[1,0,0,0]\n"
        "v_mfma_f32_32x32x16_bf16 v[80:95], v[132:135], a[228:231], v[80:95]\n"
        "v_exp_f32 v68, v68\n"
        "v_exp_f32 v69, v69\n"
        "v_cvt_pk_bf16_f32 v97, v66, v67\n"
        "v_lshl_add_u32 v50, v50, 2, s78\n"
        "v_mfma_f32_32x32x16_bf16 v[80:95], v[136:139], a[232:235], v[80:95]\n"
        "s_waitcnt vmcnt(12)\n"
        "v_exp_f32 v70, v70\n"
        "v_exp_f32 v71, v71\n"
        "v_cvt_pk_bf16_f32 v98, v68, v69\n"
        "v_mul_u32_u24 v51, 13, v50\n"
        "v_smfmac_f32_16x16x64_bf16 v[236:239], a[240:243], v[184:191], v54\n"
        "buffer_load_dwordx4 v[184:187], v232, s[40:43], 0 offen sc1\n"
        "buffer_load_dwordx4 v[188:191], v233, s[40:43], 0 offen sc1\n"
        "v_mfma_f32_32x32x16_bf16 v[80:95], v[140:143], a[236:239], v[80:95]\n"
        "v_exp_f32 v72, v72\n"
        "v_exp_f32 v73, v73\n"
        "v_cvt_pk_bf16_f32 v99, v70, v71\n"
        "v_mad_u32_u16 v232, v230, s70, v37 op_sel:[0,0,0,0]\n"
        "v_mfma_f32_32x32x16_bf16 a[64:79], v[104:107], v[160:163], a[64:79]\n"
        "v_exp_f32 v74, v74\n"
        "v_exp_f32 v75, v75\n"
        "v_cvt_pk_bf16_f32 v100, v72, v73\n"
        "v_mad_u32_u16 v233, v230, s70, v37 op_sel:[1,0,0,0]\n"
        "v_mfma_f32_32x32x16_bf16 a[80:95], v[104:107], v[164:167], a[80:95]\n"
        "v_exp_f32 v76, v76\n"
        "v_exp_f32 v77, v77\n"
        "v_cvt_pk_bf16_f32 v101, v74, v75\n"
        "v_lshrrev_b32 v51, 6, v51\n"
        "v_mfma_f32_32x32x16_bf16 a[64:79], v[108:111], v[168:171], a[64:79]\n"
        "s_waitcnt lgkmcnt(1)\n"
        "v_exp_f32 v78, v78\n"
        "v_exp_f32 v79, v79\n"
        "v_cvt_pk_bf16_f32 v102, v76, v77\n"
        "v_add_u32 v51, s60, v51\n"
        "v_mfma_f32_32x32x16_bf16 a[80:95], v[108:111], v[172:175], a[80:95]\n"
        "s_add_u32 m0, s62, 8192\n"
        "v_exp_f32 v80, v80\n"
        "v_exp_f32 v81, v81\n"
        "v_cvt_pk_bf16_f32 v103, v78, v79\n"
        "v_mfma_f32_32x32x16_bf16 v[64:79], v[112:115], a[160:163], 0\n"
        "s_waitcnt vmcnt(10)\n"
        "buffer_load_dwordx4 v10, s[44:47], s65 offen offset:0 sc1 lds\n"
        "v_exp_f32 v82, v82\n"
        "v_exp_f32 v83, v83\n"
        "v_cvt_pk_bf16_f32 v104, v80, v81\n"
        "v_smfmac_f32_16x16x64_bf16 v[236:239], a[244:247], v[192:199], v54\n"
        "buffer_load_dwordx4 v[192:195], v232, s[40:43], 0 offen sc1\n"
        "buffer_load_dwordx4 v[196:199], v233, s[40:43], 0 offen sc1\n"
        "v_mfma_f32_32x32x16_bf16 v[64:79], v[116:119], a[164:167], v[64:79]\n"
        "s_lshl_b32 s78, s61, 2\n"
        "buffer_load_dwordx4 v11, s[44:47], s65 offen offset:1024 sc1 lds\n"
        "v_exp_f32 v84, v84\n"
        "v_exp_f32 v85, v85\n"
        "v_cvt_pk_bf16_f32 v105, v82, v83\n"
        "v_mfma_f32_32x32x16_bf16 v[64:79], v[120:123], a[168:171], v[64:79]\n"
        "buffer_load_dwordx4 v10, s[44:47], s65 offen offset:2048 sc1 lds\n"
        "v_exp_f32 v86, v86\n"
        "v_exp_f32 v87, v87\n"
        "v_cvt_pk_bf16_f32 v106, v84, v85\n"
        "v_mfma_f32_32x32x16_bf16 v[64:79], v[124:127], a[172:175], v[64:79]\n"
        "buffer_load_dwordx4 v11, s[44:47], s65 offen offset:3072 sc1 lds\n"
        "v_exp_f32 v88, v88\n"
        "v_exp_f32 v89, v89\n"
        "v_cvt_pk_bf16_f32 v107, v86, v87\n"
        "v_mfma_f32_32x32x16_bf16 a[96:111], v[96:99], v[160:163], a[96:111]\n"
        "s_waitcnt vmcnt(14)\n"
        "v_exp_f32 v90, v90\n"
        "v_exp_f32 v91, v91\n"
        "v_cvt_pk_bf16_f32 v108, v88, v89\n"
        "v_mad_u32_u16 v232, v231, s70, v37 op_sel:[0,0,0,0]\n"
        "v_mfma_f32_32x32x16_bf16 a[112:127], v[96:99], v[164:167], a[112:127]\n"
        "s_add_u32 s65, s65, s66\n"
        "v_exp_f32 v92, v92\n"
        "v_exp_f32 v93, v93\n"
        "v_cvt_pk_bf16_f32 v109, v90, v91\n"
        "v_mad_u32_u16 v233, v231, s70, v37 op_sel:[1,0,0,0]\n"
        "v_smfmac_f32_16x16x64_bf16 v[236:239], a[244:247], v[200:207], v54\n"
        "s_waitcnt lgkmcnt(0)\n"
        "buffer_load_dwordx4 v[200:203], v232, s[40:43], 0 offen sc1\n"
        "buffer_load_dwordx4 v[204:207], v233, s[40:43], 0 offen sc1\n"
        "v_mfma_f32_32x32x16_bf16 a[96:111], v[100:103], v[168:171], a[96:111]\n"
        "v_exp_f32 v94, v94\n"
        "v_exp_f32 v95, v95\n"
        "v_cvt_pk_bf16_f32 v110, v92, v93\n"
        "v_mad_u32_u16 v232, v224, s70, v37 op_sel:[0,0,0,0]\n"
        "v_mfma_f32_32x32x16_bf16 a[112:127], v[100:103], v[172:175], a[112:127]\n"
        "v_exp_f32 v64, v64\n"
        "v_exp_f32 v65, v65\n"
        "v_cvt_pk_bf16_f32 v111, v94, v95\n"
        "v_mad_u32_u16 v233, v224, s70, v37 op_sel:[1,0,0,0]\n"
        "v_mfma_f32_32x32x16_bf16 v[80:95], v[112:115], a[176:179], 0\n"
        "v_exp_f32 v66, v66\n"
        "v_exp_f32 v67, v67\n"
        "v_cvt_pk_bf16_f32 v96, v64, v65\n"
        "v_and_or_b32 v51, v51, 3, s78\n"
        "v_mfma_f32_32x32x16_bf16 v[80:95], v[116:119], a[180:183], v[80:95]\n"
        "v_exp_f32 v68, v68\n"
        "v_exp_f32 v69, v69\n"
        "v_cvt_pk_bf16_f32 v97, v66, v67\n"
        "v_cmp_ne_u32 vcc, s58, v51\n"
        "v_mfma_f32_32x32x16_bf16 v[80:95], v[120:123], a[184:187], v[80:95]\n"
        "v_exp_f32 v70, v70\n"
        "v_exp_f32 v71, v71\n"
        "v_cvt_pk_bf16_f32 v98, v68, v69\n"
        "v_lshlrev_b32 v50, 10, v50\n"
        "v_mfma_f32_32x32x16_bf16 v[80:95], v[124:127], a[188:191], v[80:95]\n"
        "s_waitcnt vmcnt(14)\n"
        "v_exp_f32 v72, v72\n"
        "v_exp_f32 v73, v73\n"
        "v_cvt_pk_bf16_f32 v99, v70, v71\n"
        "v_cndmask_b32 v51, -1, v51, vcc\n"
        "v_smfmac_f32_16x16x64_bf16 v[236:239], a[244:247], v[208:215], v54\n"
        "buffer_load_dwordx4 v[208:211], v232, s[40:43], 0 offen sc1\n"
        "buffer_load_dwordx4 v[212:215], v233, s[40:43], 0 offen sc1\n"
        "v_mfma_f32_32x32x16_bf16 a[128:143], v[104:107], v[160:163], a[128:143]\n"
        "s_movk_i32 s78, 15360\n"
        "ds_read_b128 v[228:231], v35 offset:1104\n"
        "v_exp_f32 v74, v74\n"
        "v_exp_f32 v75, v75\n"
        "v_cvt_pk_bf16_f32 v100, v72, v73\n"
        "v_mfma_f32_32x32x16_bf16 a[144:159], v[104:107], v[164:167], a[144:159]\n"
        "v_exp_f32 v76, v76\n"
        "v_exp_f32 v77, v77\n"
        "v_cvt_pk_bf16_f32 v101, v74, v75\n"
        "v_mad_u32_u16 v232, v225, s70, v37 op_sel:[0,0,0,0]\n"
        "v_mfma_f32_32x32x16_bf16 a[128:143], v[108:111], v[168:171], a[128:143]\n"
        "v_exp_f32 v78, v78\n"
        "v_exp_f32 v79, v79\n"
        "v_cvt_pk_bf16_f32 v102, v76, v77\n"
        "v_mad_u32_u16 v233, v225, s70, v37 op_sel:[1,0,0,0]\n"
        "v_mfma_f32_32x32x16_bf16 a[144:159], v[108:111], v[172:175], a[144:159]\n"
        "s_waitcnt vmcnt(14)\n"
        "v_exp_f32 v80, v80\n"
        "v_exp_f32 v81, v81\n"
        "v_cvt_pk_bf16_f32 v103, v78, v79\n"
        "v_cmp_gt_u32 vcc, 250, v51\n"
        "v_mfma_f32_32x32x16_bf16 v[64:79], v[112:115], a[192:195], 0\n"
        "ds_read_b128 v[128:131], v12 offset:12288\n"
        "v_exp_f32 v82, v82\n"
        "v_exp_f32 v83, v83\n"
        "v_cvt_pk_bf16_f32 v104, v80, v81\n"
        "v_smfmac_f32_16x16x64_bf16 v[236:239], a[244:247], v[216:223], v54\n"
        "buffer_load_dwordx4 v[216:219], v232, s[40:43], 0 offen sc1\n"
        "buffer_load_dwordx4 v[220:223], v233, s[40:43], 0 offen sc1\n"
        "v_mfma_f32_32x32x16_bf16 v[64:79], v[116:119], a[196:199], v[64:79]\n"
        "ds_read_b128 v[132:135], v13 offset:12288\n"
        "v_exp_f32 v84, v84\n"
        "v_exp_f32 v85, v85\n"
        "v_cvt_pk_bf16_f32 v105, v82, v83\n"
        "v_mad_u32_u16 v232, v226, s70, v37 op_sel:[0,0,0,0]\n"
        "v_mfma_f32_32x32x16_bf16 v[64:79], v[120:123], a[200:203], v[64:79]\n"
        "ds_read_b128 v[136:139], v14 offset:12288\n"
        "v_exp_f32 v86, v86\n"
        "v_exp_f32 v87, v87\n"
        "v_cvt_pk_bf16_f32 v106, v84, v85\n"
        "v_mad_u32_u16 v233, v226, s70, v37 op_sel:[1,0,0,0]\n"
        "v_mfma_f32_32x32x16_bf16 v[64:79], v[124:127], a[204:207], v[64:79]\n"
        "ds_read_b128 v[140:143], v15 offset:12288\n"
        "v_exp_f32 v88, v88\n"
        "v_exp_f32 v89, v89\n"
        "v_cvt_pk_bf16_f32 v107, v86, v87\n"
        "v_mfma_f32_32x32x16_bf16 a[0:15], v[96:99], v[144:147], a[0:15]\n"
        "ds_read_b64_tr_b16 v[160:161], v16 offset:12288\n"
        "v_exp_f32 v90, v90\n"
        "v_exp_f32 v91, v91\n"
        "v_cvt_pk_bf16_f32 v108, v88, v89\n"
        "v_mfma_f32_32x32x16_bf16 a[16:31], v[96:99], v[148:151], a[16:31]\n"
        "ds_read_b64_tr_b16 v[162:163], v17 offset:12288\n"
        "v_exp_f32 v92, v92\n"
        "v_exp_f32 v93, v93\n"
        "v_cvt_pk_bf16_f32 v109, v90, v91\n"
        "v_mfma_f32_32x32x16_bf16 a[0:15], v[100:103], v[152:155], a[0:15]\n"
        "s_waitcnt vmcnt(14)\n"
        "ds_read_b64_tr_b16 v[164:165], v18 offset:12288\n"
        "v_exp_f32 v94, v94\n"
        "v_exp_f32 v95, v95\n"
        "v_cvt_pk_bf16_f32 v110, v92, v93\n"
        "v_smfmac_f32_16x16x64_bf16 v[236:239], a[244:247], v[176:183], v54\n"
        "buffer_load_dwordx4 v[176:179], v232, s[40:43], 0 offen sc1\n"
        "buffer_load_dwordx4 v[180:183], v233, s[40:43], 0 offen sc1\n"
        "v_mfma_f32_32x32x16_bf16 a[16:31], v[100:103], v[156:159], a[16:31]\n"
        "ds_read_b64_tr_b16 v[166:167], v19 offset:12288\n"
        "v_exp_f32 v64, v64\n"
        "v_exp_f32 v65, v65\n"
        "v_cvt_pk_bf16_f32 v111, v94, v95\n"
        "v_mad_u32_u16 v232, v227, s70, v37 op_sel:[0,0,0,0]\n"
        "v_mfma_f32_32x32x16_bf16 v[80:95], v[112:115], a[208:211], 0\n"
        "ds_read_b64_tr_b16 v[168:169], v16 offset:14336\n"
        "v_exp_f32 v66, v66\n"
        "v_exp_f32 v67, v67\n"
        "v_cvt_pk_bf16_f32 v96, v64, v65\n"
        "v_mad_u32_u16 v233, v227, s70, v37 op_sel:[1,0,0,0]\n"
        "v_mfma_f32_32x32x16_bf16 v[80:95], v[116:119], a[212:215], v[80:95]\n"
        "ds_read_b64_tr_b16 v[170:171], v17 offset:14336\n"
        "v_exp_f32 v68, v68\n"
        "v_exp_f32 v69, v69\n"
        "v_cvt_pk_bf16_f32 v97, v66, v67\n"
        "v_mfma_f32_32x32x16_bf16 v[80:95], v[120:123], a[216:219], v[80:95]\n"
        "ds_read_b64_tr_b16 v[172:173], v18 offset:14336\n"
        "v_exp_f32 v70, v70\n"
        "v_exp_f32 v71, v71\n"
        "v_cvt_pk_bf16_f32 v98, v68, v69\n"
        "v_mfma_f32_32x32x16_bf16 v[80:95], v[124:127], a[220:223], v[80:95]\n"
        "s_waitcnt vmcnt(14)\n"
        "ds_read_b64_tr_b16 v[174:175], v19 offset:14336\n"
        "v_exp_f32 v72, v72\n"
        "v_exp_f32 v73, v73\n"
        "v_cvt_pk_bf16_f32 v99, v70, v71\n"
        "v_smfmac_f32_16x16x64_bf16 v[236:239], a[244:247], v[184:191], v54\n"
        "s_waitcnt lgkmcnt(12)\n"
        "buffer_load_dwordx4 v[184:187], v232, s[40:43], 0 offen sc1\n"
        "buffer_load_dwordx4 v[188:191], v233, s[40:43], 0 offen sc1\n"
        "v_mfma_f32_32x32x16_bf16 a[32:47], v[104:107], v[144:147], a[32:47]\n"
        "v_exp_f32 v74, v74\n"
        "v_exp_f32 v75, v75\n"
        "v_cvt_pk_bf16_f32 v100, v72, v73\n"
        "v_mad_u32_u16 v232, v228, s70, v37 op_sel:[0,0,0,0]\n"
        "v_mfma_f32_32x32x16_bf16 a[48:63], v[104:107], v[148:151], a[48:63]\n"
        "v_exp_f32 v76, v76\n"
        "v_exp_f32 v77, v77\n"
        "v_cvt_pk_bf16_f32 v101, v74, v75\n"
        "v_mad_u32_u16 v233, v228, s70, v37 op_sel:[1,0,0,0]\n"
        "v_mfma_f32_32x32x16_bf16 a[32:47], v[108:111], v[152:155], a[32:47]\n"
        "v_exp_f32 v78, v78\n"
        "v_exp_f32 v79, v79\n"
        "v_cvt_pk_bf16_f32 v102, v76, v77\n"
        "v_mad_u32_u24 v50, v51, s78, v50\n"
        "v_mfma_f32_32x32x16_bf16 a[48:63], v[108:111], v[156:159], a[48:63]\n"
        "v_exp_f32 v80, v80\n"
        "v_exp_f32 v81, v81\n"
        "v_cvt_pk_bf16_f32 v103, v78, v79\n"
        "v_cndmask_b32 v50, 2.0, v50, vcc\n"
        "v_mfma_f32_32x32x16_bf16 v[64:79], v[112:115], a[224:227], 0\n"
        "v_exp_f32 v82, v82\n"
        "v_exp_f32 v83, v83\n"
        "v_cvt_pk_bf16_f32 v104, v80, v81\n"
        "v_readlane_b32 s64, v50, 0\n"
        "v_mfma_f32_32x32x16_bf16 v[64:79], v[116:119], a[228:231], v[64:79]\n"
        "s_waitcnt vmcnt(13)\n"
        "v_exp_f32 v84, v84\n"
        "v_exp_f32 v85, v85\n"
        "v_cvt_pk_bf16_f32 v105, v82, v83\n"
        "v_readlane_b32 s67, v50, 1\n"
        "v_smfmac_f32_16x16x64_bf16 v[236:239], a[244:247], v[192:199], v54\n"
        "buffer_load_dwordx4 v[192:195], v232, s[40:43], 0 offen sc1\n"
        "buffer_load_dwordx4 v[196:199], v233, s[40:43], 0 offen sc1\n"
        "v_mfma_f32_32x32x16_bf16 v[64:79], v[120:123], a[232:235], v[64:79]\n"
        "ds_read_b128 v[224:227], v35 offset:1632\n"
        "v_exp_f32 v86, v86\n"
        "v_exp_f32 v87, v87\n"
        "v_cvt_pk_bf16_f32 v106, v84, v85\n"
        "v_mfma_f32_32x32x16_bf16 v[64:79], v[124:127], a[236:239], v[64:79]\n"
        "v_exp_f32 v88, v88\n"
        "v_exp_f32 v89, v89\n"
        "v_cvt_pk_bf16_f32 v107, v86, v87\n"
        "v_mad_u32_u16 v232, v229, s70, v37 op_sel:[0,0,0,0]\n"
        "v_mfma_f32_32x32x16_bf16 a[64:79], v[96:99], v[144:147], a[64:79]\n"
        "v_exp_f32 v90, v90\n"
        "v_exp_f32 v91, v91\n"
        "v_cvt_pk_bf16_f32 v108, v88, v89\n"
        "v_mad_u32_u16 v233, v229, s70, v37 op_sel:[1,0,0,0]\n"
        "v_mfma_f32_32x32x16_bf16 a[80:95], v[96:99], v[148:151], a[80:95]\n"
        "s_waitcnt vmcnt(10)\n"
        "v_exp_f32 v92, v92\n"
        "v_exp_f32 v93, v93\n"
        "v_cvt_pk_bf16_f32 v109, v90, v91\n"
        "v_readlane_b32 s68, v50, 2\n"
        "v_mfma_f32_32x32x16_bf16 a[64:79], v[100:103], v[152:155], a[64:79]\n"
        "s_waitcnt lgkmcnt(1)\n"
        "v_exp_f32 v94, v94\n"
        "v_exp_f32 v95, v95\n"
        "v_cvt_pk_bf16_f32 v110, v92, v93\n"
        "v_readlane_b32 s61, v50, 3\n"
        "v_smfmac_f32_16x16x64_bf16 v[236:239], a[244:247], v[200:207], v54\n"
        "buffer_load_dwordx4 v[200:203], v232, s[40:43], 0 offen sc1\n"
        "buffer_load_dwordx4 v[204:207], v233, s[40:43], 0 offen sc1\n"
        "v_mfma_f32_32x32x16_bf16 a[80:95], v[100:103], v[156:159], a[80:95]\n"
        "s_add_u32 m0, s62, 12288\n"
        "v_exp_f32 v64, v64\n"
        "v_exp_f32 v65, v65\n"
        "v_cvt_pk_bf16_f32 v111, v94, v95\n"
        "v_mad_u32_u16 v232, v230, s70, v37 op_sel:[0,0,0,0]\n"
        "v_mfma_f32_32x32x16_bf16 v[80:95], v[128:131], a[160:163], 0\n"
        "s_movk_i32 s80, 5120\n"
        "buffer_load_dwordx4 v10, s[44:47], s65 offen offset:0 sc1 lds\n"
        "v_exp_f32 v66, v66\n"
        "v_exp_f32 v67, v67\n"
        "v_cvt_pk_bf16_f32 v96, v64, v65\n"
        "v_mfma_f32_32x32x16_bf16 v[80:95], v[132:135], a[164:167], v[80:95]\n"
        "s_movk_i32 s81, 10240\n"
        "buffer_load_dwordx4 v11, s[44:47], s65 offen offset:1024 sc1 lds\n"
        "v_exp_f32 v68, v68\n"
        "v_exp_f32 v69, v69\n"
        "v_cvt_pk_bf16_f32 v97, v66, v67\n"
        "v_mfma_f32_32x32x16_bf16 v[80:95], v[136:139], a[168:171], v[80:95]\n"
        "buffer_load_dwordx4 v10, s[44:47], s65 offen offset:2048 sc1 lds\n"
        "v_exp_f32 v70, v70\n"
        "v_exp_f32 v71, v71\n"
        "v_cvt_pk_bf16_f32 v98, v68, v69\n"
        "v_mfma_f32_32x32x16_bf16 v[80:95], v[140:143], a[172:175], v[80:95]\n"
        "buffer_load_dwordx4 v11, s[44:47], s65 offen offset:3072 sc1 lds\n"
        "v_exp_f32 v72, v72\n"
        "v_exp_f32 v73, v73\n"
        "v_cvt_pk_bf16_f32 v99, v70, v71\n"
        "v_mfma_f32_32x32x16_bf16 a[96:111], v[104:107], v[144:147], a[96:111]\n"
        "s_waitcnt vmcnt(14)\n"
        "v_exp_f32 v74, v74\n"
        "v_exp_f32 v75, v75\n"
        "v_cvt_pk_bf16_f32 v100, v72, v73\n"
        "v_mad_u32_u16 v233, v230, s70, v37 op_sel:[1,0,0,0]\n"
        "v_smfmac_f32_16x16x64_bf16 v[240:243], a[240:243], v[208:215], v54\n"
        "buffer_load_dwordx4 v[208:211], v232, s[40:43], 0 offen sc1\n"
        "buffer_load_dwordx4 v[212:215], v233, s[40:43], 0 offen sc1\n"
        "v_mfma_f32_32x32x16_bf16 a[112:127], v[104:107], v[148:151], a[112:127]\n"
        "s_add_u32 s65, s65, s66\n"
        "v_exp_f32 v76, v76\n"
        "v_exp_f32 v77, v77\n"
        "v_cvt_pk_bf16_f32 v101, v74, v75\n"
        "v_mad_u32_u16 v232, v231, s70, v37 op_sel:[0,0,0,0]\n"
        "v_mfma_f32_32x32x16_bf16 a[96:111], v[108:111], v[152:155], a[96:111]\n"
        "v_exp_f32 v78, v78\n"
        "v_exp_f32 v79, v79\n"
        "v_cvt_pk_bf16_f32 v102, v76, v77\n"
        "v_mad_u32_u16 v233, v231, s70, v37 op_sel:[1,0,0,0]\n"
        "v_mfma_f32_32x32x16_bf16 a[112:127], v[108:111], v[156:159], a[112:127]\n"
        "v_exp_f32 v80, v80\n"
        "v_exp_f32 v81, v81\n"
        "v_cvt_pk_bf16_f32 v103, v78, v79\n"
        "v_add_u32 v1, 133760, v234\n"
        "v_mfma_f32_32x32x16_bf16 v[64:79], v[128:131], a[176:179], 0\n"
        "s_xor_b32 s77, s59, 2\n"
        "v_exp_f32 v82, v82\n"
        "v_exp_f32 v83, v83\n"
        "v_cvt_pk_bf16_f32 v104, v80, v81\n"
        "v_mfma_f32_32x32x16_bf16 v[64:79], v[132:135], a[180:183], v[64:79]\n"
        "s_waitcnt vmcnt(14)\n"
        "v_exp_f32 v84, v84\n"
        "v_exp_f32 v85, v85\n"
        "v_cvt_pk_bf16_f32 v105, v82, v83\n"
        "v_smfmac_f32_16x16x64_bf16 v[240:243], a[240:243], v[216:223], v54\n"
        "s_waitcnt lgkmcnt(0)\n"
        "buffer_load_dwordx4 v[216:219], v232, s[40:43], 0 offen sc1\n"
        "buffer_load_dwordx4 v[220:223], v233, s[40:43], 0 offen sc1\n"
        "v_mfma_f32_32x32x16_bf16 v[64:79], v[136:139], a[184:187], v[64:79]\n"
        "s_mul_i32 s77, s77, 30720\n"
        "v_exp_f32 v86, v86\n"
        "v_exp_f32 v87, v87\n"
        "v_cvt_pk_bf16_f32 v106, v84, v85\n"
        "v_mad_u32_u16 v232, v224, s70, v37 op_sel:[0,0,0,0]\n"
        "v_mfma_f32_32x32x16_bf16 v[64:79], v[140:143], a[188:191], v[64:79]\n"
        "s_add_u32 s77, s77, 10240\n"
        "v_exp_f32 v88, v88\n"
        "v_exp_f32 v89, v89\n"
        "v_cvt_pk_bf16_f32 v107, v86, v87\n"
        "v_mad_u32_u16 v233, v224, s70, v37 op_sel:[1,0,0,0]\n"
        "v_mfma_f32_32x32x16_bf16 a[128:143], v[96:99], v[144:147], a[128:143]\n"
        "v_exp_f32 v90, v90\n"
        "v_exp_f32 v91, v91\n"
        "v_cvt_pk_bf16_f32 v108, v88, v89\n"
        "v_and_b32 v50, 2, v60\n"
        "v_mfma_f32_32x32x16_bf16 a[144:159], v[96:99], v[148:151], a[144:159]\n"
        "v_exp_f32 v92, v92\n"
        "v_exp_f32 v93, v93\n"
        "v_cvt_pk_bf16_f32 v109, v90, v91\n"
        "v_lshrrev_b32 v51, 3, v59\n"
        "v_mfma_f32_32x32x16_bf16 a[128:143], v[100:103], v[152:155], a[128:143]\n"
        "v_exp_f32 v94, v94\n"
        "v_exp_f32 v95, v95\n"
        "v_cvt_pk_bf16_f32 v110, v92, v93\n"
        "v_add3_u32 v50, v50, v51, s79\n"
        "v_mfma_f32_32x32x16_bf16 a[144:159], v[100:103], v[156:159], a[144:159]\n"
        "s_waitcnt vmcnt(14)\n"
        "v_exp_f32 v64, v64\n"
        "v_exp_f32 v65, v65\n"
        "v_cvt_pk_bf16_f32 v111, v94, v95\n"
        "v_mul_u32_u24 v50, 144, v50\n"
        "v_smfmac_f32_16x16x64_bf16 v[240:243], a[240:243], v[176:183], v54\n"
        "buffer_load_dwordx4 v[176:179], v232, s[40:43], 0 offen sc1\n"
        "buffer_load_dwordx4 v[180:183], v233, s[40:43], 0 offen sc1\n"
        "v_mfma_f32_32x32x16_bf16 v[80:95], v[128:131], a[192:195], 0\n"
        "ds_read_b128 v[112:115], v12 offset:0\n"
        "ds_read_b128 v[228:231], v35 offset:1648\n"
        "v_exp_f32 v66, v66\n"
        "v_exp_f32 v67, v67\n"
        "v_cvt_pk_bf16_f32 v96, v64, v65\n"
        "v_mfma_f32_32x32x16_bf16 v[80:95], v[132:135], a[196:199], v[80:95]\n"
        "ds_read_b128 v[116:119], v13 offset:0\n"
        "v_exp_f32 v68, v68\n"
        "v_exp_f32 v69, v69\n"
        "v_cvt_pk_bf16_f32 v97, v66, v67\n"
        "v_mad_u32_u16 v232, v225, s70, v37 op_sel:[0,0,0,0]\n"
        "v_mfma_f32_32x32x16_bf16 v[80:95], v[136:139], a[200:203], v[80:95]\n"
        "ds_read_b128 v[120:123], v14 offset:0\n"
        "v_exp_f32 v70, v70\n"
        "v_exp_f32 v71, v71\n"
        "v_cvt_pk_bf16_f32 v98, v68, v69\n"
        "v_mad_u32_u16 v233, v225, s70, v37 op_sel:[1,0,0,0]\n"
        "v_mfma_f32_32x32x16_bf16 v[80:95], v[140:143], a[204:207], v[80:95]\n"
        "ds_read_b128 v[124:127], v15 offset:0\n"
        "v_exp_f32 v72, v72\n"
        "v_exp_f32 v73, v73\n"
        "v_cvt_pk_bf16_f32 v99, v70, v71\n"
        "v_mfma_f32_32x32x16_bf16 a[0:15], v[104:107], v[160:163], a[0:15]\n"
        "s_waitcnt vmcnt(14)\n"
        "ds_read_b64_tr_b16 v[144:145], v16 offset:0\n"
        "v_exp_f32 v74, v74\n"
        "v_exp_f32 v75, v75\n"
        "v_cvt_pk_bf16_f32 v100, v72, v73\n"
        "v_smfmac_f32_16x16x64_bf16 v[240:243], a[240:243], v[184:191], v54\n"
        "buffer_load_dwordx4 v[184:187], v232, s[40:43], 0 offen sc1\n"
        "buffer_load_dwordx4 v[188:191], v233, s[40:43], 0 offen sc1\n"
        "v_mfma_f32_32x32x16_bf16 a[16:31], v[104:107], v[164:167], a[16:31]\n"
        "ds_read_b64_tr_b16 v[146:147], v17 offset:0\n"
        "v_exp_f32 v76, v76\n"
        "v_exp_f32 v77, v77\n"
        "v_cvt_pk_bf16_f32 v101, v74, v75\n"
        "v_mad_u32_u16 v232, v226, s70, v37 op_sel:[0,0,0,0]\n"
        "v_mfma_f32_32x32x16_bf16 a[0:15], v[108:111], v[168:171], a[0:15]\n"
        "ds_read_b64_tr_b16 v[148:149], v18 offset:0\n"
        "v_exp_f32 v78, v78\n"
        "v_exp_f32 v79, v79\n"
        "v_cvt_pk_bf16_f32 v102, v76, v77\n"
        "v_mad_u32_u16 v233, v226, s70, v37 op_sel:[1,0,0,0]\n"
        "v_mfma_f32_32x32x16_bf16 a[16:31], v[108:111], v[172:175], a[16:31]\n"
        "ds_read_b64_tr_b16 v[150:151], v19 offset:0\n"
        "v_exp_f32 v80, v80\n"
        "v_exp_f32 v81, v81\n"
        "v_cvt_pk_bf16_f32 v103, v78, v79\n"
        "v_mfma_f32_32x32x16_bf16 v[64:79], v[128:131], a[208:211], 0\n"
        "ds_read_b64_tr_b16 v[152:153], v16 offset:2048\n"
        "v_exp_f32 v82, v82\n"
        "v_exp_f32 v83, v83\n"
        "v_cvt_pk_bf16_f32 v104, v80, v81\n"
        "v_mfma_f32_32x32x16_bf16 v[64:79], v[132:135], a[212:215], v[64:79]\n"
        "ds_read_b64_tr_b16 v[154:155], v17 offset:2048\n"
        "v_exp_f32 v84, v84\n"
        "v_exp_f32 v85, v85\n"
        "v_cvt_pk_bf16_f32 v105, v82, v83\n"
        "v_mfma_f32_32x32x16_bf16 v[64:79], v[136:139], a[216:219], v[64:79]\n"
        "s_waitcnt vmcnt(14)\n"
        "ds_read_b64_tr_b16 v[156:157], v18 offset:2048\n"
        "v_exp_f32 v86, v86\n"
        "v_exp_f32 v87, v87\n"
        "v_cvt_pk_bf16_f32 v106, v84, v85\n"
        "v_smfmac_f32_16x16x64_bf16 v[240:243], a[240:243], v[192:199], v54\n"
        "buffer_load_dwordx4 v[192:195], v232, s[40:43], 0 offen sc1\n"
        "buffer_load_dwordx4 v[196:199], v233, s[40:43], 0 offen sc1\n"
        "v_mfma_f32_32x32x16_bf16 v[64:79], v[140:143], a[220:223], v[64:79]\n"
        "ds_read_b64_tr_b16 v[158:159], v19 offset:2048\n"
        "v_exp_f32 v88, v88\n"
        "v_exp_f32 v89, v89\n"
        "v_cvt_pk_bf16_f32 v107, v86, v87\n"
        "v_mfma_f32_32x32x16_bf16 a[32:47], v[96:99], v[160:163], a[32:47]\n"
        "v_exp_f32 v90, v90\n"
        "v_exp_f32 v91, v91\n"
        "v_cvt_pk_bf16_f32 v108, v88, v89\n"
        "v_mad_u32_u16 v232, v227, s70, v37 op_sel:[0,0,0,0]\n"
        "v_mfma_f32_32x32x16_bf16 a[48:63], v[96:99], v[164:167], a[48:63]\n"
        "v_exp_f32 v92, v92\n"
        "v_exp_f32 v93, v93\n"
        "v_cvt_pk_bf16_f32 v109, v90, v91\n"
        "v_mad_u32_u16 v233, v227, s70, v37 op_sel:[1,0,0,0]\n"
        "v_mfma_f32_32x32x16_bf16 a[32:47], v[100:103], v[168:171], a[32:47]\n"
        "v_exp_f32 v94, v94\n"
        "v_exp_f32 v95, v95\n"
        "v_cvt_pk_bf16_f32 v110, v92, v93\n"
        "v_and_b32 v51, 0x70, v234\n"
        "v_mfma_f32_32x32x16_bf16 a[48:63], v[100:103], v[172:175], a[48:63]\n"
        "s_waitcnt vmcnt(14)\n"
        "v_exp_f32 v64, v64\n"
        "v_exp_f32 v65, v65\n"
        "v_cvt_pk_bf16_f32 v111, v94, v95\n"
        "v_add3_u32 v50, v50, v51, s77\n"
        "v_smfmac_f32_16x16x64_bf16 v[240:243], a[240:243], v[200:207], v54\n"
        "s_waitcnt lgkmcnt(11)\n"
        "buffer_load_dwordx4 v[200:203], v232, s[40:43], 0 offen sc1\n"
        "buffer_load_dwordx4 v[204:207], v233, s[40:43], 0 offen sc1\n"
        "v_mfma_f32_32x32x16_bf16 v[80:95], v[128:131], a[224:227], 0\n"
        "v_exp_f32 v66, v66\n"
        "v_exp_f32 v67, v67\n"
        "v_cvt_pk_bf16_f32 v96, v64, v65\n"
        "v_mad_u32_u16 v232, v228, s70, v37 op_sel:[0,0,0,0]\n"
        "v_mfma_f32_32x32x16_bf16 v[80:95], v[132:135], a[228:231], v[80:95]\n"
        "v_exp_f32 v68, v68\n"
        "v_exp_f32 v69, v69\n"
        "v_cvt_pk_bf16_f32 v97, v66, v67\n"
        "v_mad_u32_u16 v233, v228, s70, v37 op_sel:[1,0,0,0]\n"
        "v_mfma_f32_32x32x16_bf16 v[80:95], v[136:139], a[232:235], v[80:95]\n"
        "v_exp_f32 v70, v70\n"
        "v_exp_f32 v71, v71\n"
        "v_cvt_pk_bf16_f32 v98, v68, v69\n"
        "v_and_b32 v51, 1, v60\n"
        "v_mfma_f32_32x32x16_bf16 v[80:95], v[140:143], a[236:239], v[80:95]\n"
        "v_exp_f32 v72, v72\n"
        "v_exp_f32 v73, v73\n"
        "v_cvt_pk_bf16_f32 v99, v70, v71\n"
        "v_lshl_add_u32 v55, v51, 3, v50\n"
        "v_mfma_f32_32x32x16_bf16 a[64:79], v[104:107], v[160:163], a[64:79]\n"
        "s_memrealtime s[86:87]\n"
        "v_exp_f32 v74, v74\n"
        "v_exp_f32 v75, v75\n"
        "v_cvt_pk_bf16_f32 v100, v72, v73\n"
        "v_mul_u32_u24 v50, 144, v59\n"
        "v_mfma_f32_32x32x16_bf16 a[80:95], v[104:107], v[164:167], a[80:95]\n"
        "s_waitcnt vmcnt(10)\n"
        "v_exp_f32 v76, v76\n"
        "v_exp_f32 v77, v77\n"
        "v_cvt_pk_bf16_f32 v101, v74, v75\n"
        "v_lshl_add_u32 v50, v60, 4, v50\n"
        "v_smfmac_f32_16x16x64_bf16 v[240:243], a[240:243], v[208:215], v54\n"
        "buffer_load_dwordx4 v[208:211], v232, s[40:43], 0 offen sc1\n"
        "buffer_load_dwordx4 v[212:215], v233, s[40:43], 0 offen sc1\n"
        "v_mfma_f32_32x32x16_bf16 a[64:79], v[108:111], v[168:171], a[64:79]\n"
        "s_waitcnt lgkmcnt(0)\n"
        "ds_read_b128 v[224:227], v35 offset:2176\n"
        "v_exp_f32 v78, v78\n"
        "v_exp_f32 v79, v79\n"
        "v_cvt_pk_bf16_f32 v102, v76, v77\n"
        "v_mfma_f32_32x32x16_bf16 a[80:95], v[108:111], v[172:175], a[80:95]\n"
        "v_exp_f32 v80, v80\n"
        "v_exp_f32 v81, v81\n"
        "v_cvt_pk_bf16_f32 v103, v78, v79\n"
        "v_mad_u32_u16 v232, v229, s70, v37 op_sel:[0,0,0,0]\n"
        "v_mfma_f32_32x32x16_bf16 v[64:79], v[112:115], a[160:163], 0\n"
        "s_lshl_b32 s72, s59, 12\n"
        "v_exp_f32 v82, v82\n"
        "v_exp_f32 v83, v83\n"
        "v_cvt_pk_bf16_f32 v104, v80, v81\n"
        "v_mad_u32_u16 v233, v229, s70, v37 op_sel:[1,0,0,0]\n"
        "v_mfma_f32_32x32x16_bf16 v[64:79], v[116:119], a[164:167], v[64:79]\n"
        "s_add_u32 m0, s72, 133760\n"
        "s_nop 0\n"
        "buffer_load_dwordx4 v38, s[36:39], 0 offen offset:0 sc1 lds\n"
        "v_exp_f32 v84, v84\n"
        "v_exp_f32 v85, v85\n"
        "v_cvt_pk_bf16_f32 v105, v82, v83\n"
        "v_mfma_f32_32x32x16_bf16 v[64:79], v[120:123], a[168:171], v[64:79]\n"
        "s_waitcnt vmcnt(11)\n"
        "buffer_load_dwordx4 v38, s[36:39], 0 offen offset:1024 sc1 lds\n"
        "v_exp_f32 v86, v86\n"
        "v_exp_f32 v87, v87\n"
        "v_cvt_pk_bf16_f32 v106, v84, v85\n"
        "v_smfmac_f32_16x16x64_bf16 v[240:243], a[240:243], v[216:223], v54\n"
        "buffer_load_dwordx4 v[216:219], v232, s[40:43], 0 offen sc1\n"
        "buffer_load_dwordx4 v[220:223], v233, s[40:43], 0 offen sc1\n"
        "v_mfma_f32_32x32x16_bf16 v[64:79], v[124:127], a[172:175], v[64:79]\n"
        "s_lshr_b32 s87, s60, 1\n"
        "v_exp_f32 v88, v88\n"
        "v_exp_f32 v89, v89\n"
        "v_cvt_pk_bf16_f32 v107, v86, v87\n"
        "v_mad_u32_u16 v232, v230, s70, v37 op_sel:[0,0,0,0]\n"
        "v_mfma_f32_32x32x16_bf16 a[96:111], v[96:99], v[160:163], a[96:111]\n"
        "s_mul_i32 s87, s87, 12\n"
        "v_exp_f32 v90, v90\n"
        "v_exp_f32 v91, v91\n"
        "v_cvt_pk_bf16_f32 v108, v88, v89\n"
        "v_mad_u32_u16 v233, v230, s70, v37 op_sel:[1,0,0,0]\n"
        "v_mfma_f32_32x32x16_bf16 a[112:127], v[96:99], v[164:167], a[112:127]\n"
        "s_add_u32 s86, s86, s87\n"
        "buffer_load_dwordx4 v38, s[36:39], 0 offen offset:2048 sc1 lds\n"
        "v_exp_f32 v92, v92\n"
        "v_exp_f32 v93, v93\n"
        "v_cvt_pk_bf16_f32 v109, v90, v91\n"
        "v_mfma_f32_32x32x16_bf16 a[96:111], v[100:103], v[168:171], a[96:111]\n"
        "s_and_b32 s86, s86, 0xffffff\n"
        "buffer_load_dwordx4 v38, s[36:39], 0 offen offset:3072 sc1 lds\n"
        "v_exp_f32 v94, v94\n"
        "v_exp_f32 v95, v95\n"
        "v_cvt_pk_bf16_f32 v110, v92, v93\n"
        "v_mfma_f32_32x32x16_bf16 a[112:127], v[100:103], v[172:175], a[112:127]\n"
        "s_lshl_b32 s72, s59, 10\n"
        "v_exp_f32 v64, v64\n"
        "v_exp_f32 v65, v65\n"
        "v_cvt_pk_bf16_f32 v111, v94, v95\n"
        "v_add_u32 v3, s77, v50\n"
        "v_mfma_f32_32x32x16_bf16 v[80:95], v[112:115], a[176:179], 0\n"
        "s_waitcnt vmcnt(14)\n"
        "v_exp_f32 v66, v66\n"
        "v_exp_f32 v67, v67\n"
        "v_cvt_pk_bf16_f32 v96, v64, v65\n"
        "v_smfmac_f32_16x16x64_bf16 v[240:243], a[244:247], v[176:183], v54\n"
        "buffer_load_dwordx4 v[176:179], v232, s[40:43], 0 offen sc1\n"
        "buffer_load_dwordx4 v[180:183], v233, s[40:43], 0 offen sc1\n"
        "v_mfma_f32_32x32x16_bf16 v[80:95], v[116:119], a[180:183], v[80:95]\n"
        "s_add_u32 m0, s72, 150144\n"
        "v_exp_f32 v68, v68\n"
        "v_exp_f32 v69, v69\n"
        "v_cvt_pk_bf16_f32 v97, v66, v67\n"
        "v_mad_u32_u16 v232, v231, s70, v37 op_sel:[0,0,0,0]\n"
        "v_mfma_f32_32x32x16_bf16 v[80:95], v[120:123], a[184:187], v[80:95]\n"
        "s_or_b32 s86, s86, s56\n"
        "v_exp_f32 v70, v70\n"
        "v_exp_f32 v71, v71\n"
        "v_cvt_pk_bf16_f32 v98, v68, v69\n"
        "v_mad_u32_u16 v233, v231, s70, v37 op_sel:[1,0,0,0]\n"
        "v_mfma_f32_32x32x16_bf16 v[80:95], v[124:127], a[188:191], v[80:95]\n"
        "s_mul_i32 s77, s84, 144\n"
        "buffer_load_dwordx4 v56, s[36:39], 0 offen sc1 lds\n"
        "v_exp_f32 v72, v72\n"
        "v_exp_f32 v73, v73\n"
        "v_cvt_pk_bf16_f32 v99, v70, v71\n"
        "v_mfma_f32_32x32x16_bf16 a[128:143], v[104:107], v[160:163], a[128:143]\n"
        "s_add_u32 s77, s77, 154384\n"
        "v_exp_f32 v74, v74\n"
        "v_exp_f32 v75, v75\n"
        "v_cvt_pk_bf16_f32 v100, v72, v73\n"
        "v_mov_b32 v30, s86\n"
        "v_mfma_f32_32x32x16_bf16 a[144:159], v[104:107], v[164:167], a[144:159]\n"
        "s_waitcnt vmcnt(15)\n"
        "v_exp_f32 v76, v76\n"
        "v_exp_f32 v77, v77\n"
        "v_cvt_pk_bf16_f32 v101, v74, v75\n"
        "v_add_u32 v34, s77, v50\n"
        "v_smfmac_f32_16x16x64_bf16 v[240:243], a[244:247], v[184:191], v54\n"
        "s_waitcnt lgkmcnt(0)\n"
        "buffer_load_dwordx4 v[184:187], v232, s[40:43], 0 offen sc1\n"
        "buffer_load_dwordx4 v[188:191], v233, s[40:43], 0 offen sc1\n"
        "v_mfma_f32_32x32x16_bf16 a[128:143], v[108:111], v[168:171], a[128:143]\n"
        "s_mov_b32 s65, 15\n"
        "v_exp_f32 v78, v78\n"
        "v_exp_f32 v79, v79\n"
        "v_cvt_pk_bf16_f32 v102, v76, v77\n"
        "v_mad_u32_u16 v232, v224, s70, v37 op_sel:[0,0,0,0]\n"
        "v_mfma_f32_32x32x16_bf16 a[144:159], v[108:111], v[172:175], a[144:159]\n"
        "v_exp_f32 v80, v80\n"
        "v_exp_f32 v81, v81\n"
        "v_cvt_pk_bf16_f32 v103, v78, v79\n"
        "v_mad_u32_u16 v233, v224, s70, v37 op_sel:[1,0,0,0]\n"
        "v_mfma_f32_32x32x16_bf16 v[64:79], v[112:115], a[192:195], 0\n"
        "ds_read_b128 v[128:131], v12 offset:4096\n"
        "v_exp_f32 v82, v82\n"
        "v_exp_f32 v83, v83\n"
        "v_cvt_pk_bf16_f32 v104, v80, v81\n"
        "v_mfma_f32_32x32x16_bf16 v[64:79], v[116:119], a[196:199], v[64:79]\n"
        "ds_read_b128 v[132:135], v13 offset:4096\n"
        "v_exp_f32 v84, v84\n"
        "v_exp_f32 v85, v85\n"
        "v_cvt_pk_bf16_f32 v105, v82, v83\n"
        "v_mfma_f32_32x32x16_bf16 v[64:79], v[120:123], a[200:203], v[64:79]\n"
        "ds_read_b128 v[136:139], v14 offset:4096\n"
        "v_exp_f32 v86, v86\n"
        "v_exp_f32 v87, v87\n"
        "v_cvt_pk_bf16_f32 v106, v84, v85\n"
        "v_mfma_f32_32x32x16_bf16 v[64:79], v[124:127], a[204:207], v[64:79]\n"
        "s_waitcnt vmcnt(15)\n"
        "ds_read_b128 v[140:143], v15 offset:4096\n"
        "v_exp_f32 v88, v88\n"
        "v_exp_f32 v89, v89\n"
        "v_cvt_pk_bf16_f32 v107, v86, v87\n"
        "v_smfmac_f32_16x16x64_bf16 v[240:243], a[244:247], v[192:199], v54\n"
        "buffer_load_dwordx4 v[192:195], v232, s[40:43], 0 offen sc1\n"
        "buffer_load_dwordx4 v[196:199], v233, s[40:43], 0 offen sc1\n"
        "v_mfma_f32_32x32x16_bf16 a[0:15], v[96:99], v[144:147], a[0:15]\n"
        "ds_read_b64_tr_b16 v[160:161], v16 offset:4096\n"
        "v_exp_f32 v90, v90\n"
        "v_exp_f32 v91, v91\n"
        "v_cvt_pk_bf16_f32 v108, v88, v89\n"
        "v_mad_u32_u16 v232, v225, s70, v37 op_sel:[0,0,0,0]\n"
        "v_mfma_f32_32x32x16_bf16 a[16:31], v[96:99], v[148:151], a[16:31]\n"
        "ds_read_b64_tr_b16 v[162:163], v17 offset:4096\n"
        "v_exp_f32 v92, v92\n"
        "v_exp_f32 v93, v93\n"
        "v_cvt_pk_bf16_f32 v109, v90, v91\n"
        "v_mad_u32_u16 v233, v225, s70, v37 op_sel:[1,0,0,0]\n"
        "v_mfma_f32_32x32x16_bf16 a[0:15], v[100:103], v[152:155], a[0:15]\n"
        "ds_read_b64_tr_b16 v[164:165], v18 offset:4096\n"
        "v_exp_f32 v94, v94\n"
        "v_exp_f32 v95, v95\n"
        "v_cvt_pk_bf16_f32 v110, v92, v93\n"
        "v_mfma_f32_32x32x16_bf16 a[16:31], v[100:103], v[156:159], a[16:31]\n"
        "ds_read_b64_tr_b16 v[166:167], v19 offset:4096\n"
        "v_exp_f32 v64, v64\n"
        "v_exp_f32 v65, v65\n"
        "v_cvt_pk_bf16_f32 v111, v94, v95\n"
        "v_mfma_f32_32x32x16_bf16 v[80:95], v[112:115], a[208:211], 0\n"
        "s_waitcnt vmcnt(15)\n"
        "ds_read_b64_tr_b16 v[168:169], v16 offset:6144\n"
        "v_exp_f32 v66, v66\n"
        "v_exp_f32 v67, v67\n"
        "v_cvt_pk_bf16_f32 v96, v64, v65\n"
        "v_smfmac_f32_16x16x64_bf16 v[240:243], a[244:247], v[200:207], v54\n"
        "buffer_load_dwordx4 v[200:203], v232, s[40:43], 0 offen sc1\n"
        "buffer_load_dwordx4 v[204:207], v233, s[40:43], 0 offen sc1\n"
        "v_mfma_f32_32x32x16_bf16 v[80:95], v[116:119], a[212:215], v[80:95]\n"
        "ds_read_b64_tr_b16 v[170:171], v17 offset:6144\n"
        "v_exp_f32 v68, v68\n"
        "v_exp_f32 v69, v69\n"
        "v_cvt_pk_bf16_f32 v97, v66, v67\n"
        "v_mfma_f32_32x32x16_bf16 v[80:95], v[120:123], a[216:219], v[80:95]\n"
        "ds_read_b64_tr_b16 v[172:173], v18 offset:6144\n"
        "v_exp_f32 v70, v70\n"
        "v_exp_f32 v71, v71\n"
        "v_cvt_pk_bf16_f32 v98, v68, v69\n"
        "v_mfma_f32_32x32x16_bf16 v[80:95], v[124:127], a[220:223], v[80:95]\n"
        "ds_read_b64_tr_b16 v[174:175], v19 offset:6144\n"
        "v_exp_f32 v72, v72\n"
        "v_exp_f32 v73, v73\n"
        "v_cvt_pk_bf16_f32 v99, v70, v71\n"
        "v_mfma_f32_32x32x16_bf16 a[32:47], v[104:107], v[144:147], a[32:47]\n"
        "ds_read_b128 v[228:231], v35 offset:2192\n"
        "v_exp_f32 v74, v74\n"
        "v_exp_f32 v75, v75\n"
        "v_cvt_pk_bf16_f32 v100, v72, v73\n"
        "v_mfma_f32_32x32x16_bf16 a[48:63], v[104:107], v[148:151], a[48:63]\n"
        "v_exp_f32 v76, v76\n"
        "v_exp_f32 v77, v77\n"
        "v_cvt_pk_bf16_f32 v101, v74, v75\n"
        "v_mad_u32_u16 v232, v226, s70, v37 op_sel:[0,0,0,0]\n"
        "v_mfma_f32_32x32x16_bf16 a[32:47], v[108:111], v[152:155], a[32:47]\n"
        "s_waitcnt vmcnt(15)\n"
        "v_exp_f32 v78, v78\n"
        "v_exp_f32 v79, v79\n"
        "v_cvt_pk_bf16_f32 v102, v76, v77\n"
        "v_mad_u32_u16 v233, v226, s70, v37 op_sel:[1,0,0,0]\n"
        "v_smfmac_f32_16x16x64_bf16 v[240:243], a[244:247], v[208:215], v54\n"
        "buffer_load_dwordx4 v[208:211], v232, s[40:43], 0 offen sc1\n"
        "buffer_load_dwordx4 v[212:215], v233, s[40:43], 0 offen sc1\n"
        "v_mfma_f32_32x32x16_bf16 a[48:63], v[108:111], v[156:159], a[48:63]\n"
        "v_exp_f32 v80, v80\n"
        "v_exp_f32 v81, v81\n"
        "v_cvt_pk_bf16_f32 v103, v78, v79\n"
        "v_mad_u32_u16 v232, v227, s70, v37 op_sel:[0,0,0,0]\n"
        "v_mfma_f32_32x32x16_bf16 v[64:79], v[112:115], a[224:227], 0\n"
        "v_exp_f32 v82, v82\n"
        "v_exp_f32 v83, v83\n"
        "v_cvt_pk_bf16_f32 v104, v80, v81\n"
        "v_mad_u32_u16 v233, v227, s70, v37 op_sel:[1,0,0,0]\n"
        "v_mfma_f32_32x32x16_bf16 v[64:79], v[116:119], a[228:231], v[64:79]\n"
        "buffer_store_dwordx2 v[30:31], v21, s[36:39], 0 offen sc1\n"
        "v_exp_f32 v84, v84\n"
        "v_exp_f32 v85, v85\n"
        "v_cvt_pk_bf16_f32 v105, v82, v83\n"
        "v_mfma_f32_32x32x16_bf16 v[64:79], v[120:123], a[232:235], v[64:79]\n"
        "v_exp_f32 v86, v86\n"
        "v_exp_f32 v87, v87\n"
        "v_cvt_pk_bf16_f32 v106, v84, v85\n"
        "v_lshrrev_b32 v50, 1, v59\n"
        "v_mfma_f32_32x32x16_bf16 v[64:79], v[124:127], a[236:239], v[64:79]\n"
        "s_waitcnt vmcnt(14)\n"
        "v_exp_f32 v88, v88\n"
        "v_exp_f32 v89, v89\n"
        "v_cvt_pk_bf16_f32 v107, v86, v87\n"
        "v_xor_b32 v50, v50, v60\n"
        "v_smfmac_f32_16x16x64_bf16 v[240:243], a[244:247], v[216:223], v54\n"
        "s_waitcnt lgkmcnt(0)\n"
        "buffer_load_dwordx4 v[216:219], v232, s[40:43], 0 offen sc1\n"
        "buffer_load_dwordx4 v[220:223], v233, s[40:43], 0 offen sc1\n"
        "v_mfma_f32_32x32x16_bf16 a[64:79], v[96:99], v[144:147], a[64:79]\n"
        "v_exp_f32 v90, v90\n"
        "v_exp_f32 v91, v91\n"
        "v_cvt_pk_bf16_f32 v108, v88, v89\n"
        "v_mad_u32_u16 v232, v228, s70, v37 op_sel:[0,0,0,0]\n"
        "v_mfma_f32_32x32x16_bf16 a[80:95], v[96:99], v[148:151], a[80:95]\n"
        "v_exp_f32 v92, v92\n"
        "v_exp_f32 v93, v93\n"
        "v_cvt_pk_bf16_f32 v109, v90, v91\n"
        "v_mad_u32_u16 v233, v228, s70, v37 op_sel:[1,0,0,0]\n"
        "v_mfma_f32_32x32x16_bf16 a[64:79], v[100:103], v[152:155], a[64:79]\n"
        "v_exp_f32 v94, v94\n"
        "v_exp_f32 v95, v95\n"
        "v_cvt_pk_bf16_f32 v110, v92, v93\n"
        "v_lshlrev_b32 v50, 4, v50\n"
        "v_mfma_f32_32x32x16_bf16 a[80:95], v[100:103], v[156:159], a[80:95]\n"
        "v_exp_f32 v64, v64\n"
        "v_exp_f32 v65, v65\n"
        "v_cvt_pk_bf16_f32 v111, v94, v95\n"
        "v_lshl_add_u32 v50, v59, 7, v50\n"
        "v_mfma_f32_32x32x16_bf16 v[80:95], v[128:131], a[160:163], 0\n"
        "ds_read_b64 v[24:25], v36 offset:128\n"
        "v_exp_f32 v66, v66\n"
        "v_exp_f32 v67, v67\n"
        "v_cvt_pk_bf16_f32 v96, v64, v65\n"
        "v_mfma_f32_32x32x16_bf16 v[80:95], v[132:135], a[164:167], v[80:95]\n"
        "s_waitcnt vmcnt(12)\n"
        "ds_read_b64 v[26:27], v36 offset:672\n"
        "v_exp_f32 v68, v68\n"
        "v_exp_f32 v69, v69\n"
        "v_cvt_pk_bf16_f32 v97, v66, v67\n"
        "v_smfmac_f32_16x16x64_bf16 v[240:243], a[244:247], v[176:183], v54\n"
        "buffer_load_dwordx4 v[176:179], v232, s[40:43], 0 offen sc1\n"
        "buffer_load_dwordx4 v[180:183], v233, s[40:43], 0 offen sc1\n"
        "v_mfma_f32_32x32x16_bf16 v[80:95], v[136:139], a[168:171], v[80:95]\n"
        "ds_read_b64 v[28:29], v36 offset:1216\n"
        "v_exp_f32 v70, v70\n"
        "v_exp_f32 v71, v71\n"
        "v_cvt_pk_bf16_f32 v98, v68, v69\n"
        "v_mad_u32_u16 v232, v229, s70, v37 op_sel:[0,0,0,0]\n"
        "v_mfma_f32_32x32x16_bf16 v[80:95], v[140:143], a[172:175], v[80:95]\n"
        "ds_read_b64 v[30:31], v36 offset:1760\n"
        "v_exp_f32 v72, v72\n"
        "v_exp_f32 v73, v73\n"
        "v_cvt_pk_bf16_f32 v99, v70, v71\n"
        "v_mad_u32_u16 v233, v229, s70, v37 op_sel:[1,0,0,0]\n"
        "v_mfma_f32_32x32x16_bf16 a[96:111], v[104:107], v[144:147], a[96:111]\n"
        "ds_read_b64 v[32:33], v36 offset:2304\n"
        "v_exp_f32 v74, v74\n"
        "v_exp_f32 v75, v75\n"
        "v_cvt_pk_bf16_f32 v100, v72, v73\n"
        "v_add_u32 v36, 32, v36\n"
        "v_mfma_f32_32x32x16_bf16 a[112:127], v[104:107], v[148:151], a[112:127]\n"
        "v_exp_f32 v76, v76\n"
        "v_exp_f32 v77, v77\n"
        "v_cvt_pk_bf16_f32 v101, v74, v75\n"
        "v_add_u32 v22, 150144, v50\n"
        "v_mfma_f32_32x32x16_bf16 a[96:111], v[108:111], v[152:155], a[96:111]\n"
        "s_waitcnt vmcnt(11)\n"
        "v_exp_f32 v78, v78\n"
        "v_exp_f32 v79, v79\n"
        "v_cvt_pk_bf16_f32 v102, v76, v77\n"
        "v_xor_b32 v23, 64, v22\n"
        "v_smfmac_f32_16x16x64_bf16 v[240:243], a[244:247], v[184:191], v54\n"
        "buffer_load_dwordx4 v[184:187], v232, s[40:43], 0 offen sc1\n"
        "buffer_load_dwordx4 v[188:191], v233, s[40:43], 0 offen sc1\n"
        "v_mfma_f32_32x32x16_bf16 a[112:127], v[108:111], v[156:159], a[112:127]\n"
        "v_exp_f32 v80, v80\n"
        "v_exp_f32 v81, v81\n"
        "v_cvt_pk_bf16_f32 v103, v78, v79\n"
        "v_mad_u32_u16 v232, v230, s70, v37 op_sel:[0,0,0,0]\n"
        "v_mfma_f32_32x32x16_bf16 v[64:79], v[128:131], a[176:179], 0\n"
        "v_exp_f32 v82, v82\n"
        "v_exp_f32 v83, v83\n"
        "v_cvt_pk_bf16_f32 v104, v80, v81\n"
        "v_mad_u32_u16 v233, v230, s70, v37 op_sel:[1,0,0,0]\n"
        "v_mfma_f32_32x32x16_bf16 v[64:79], v[132:135], a[180:183], v[64:79]\n"
        "v_exp_f32 v84, v84\n"
        "v_exp_f32 v85, v85\n"
        "v_cvt_pk_bf16_f32 v105, v82, v83\n"
        "v_mfma_f32_32x32x16_bf16 v[64:79], v[136:139], a[184:187], v[64:79]\n"
        "v_exp_f32 v86, v86\n"
        "v_exp_f32 v87, v87\n"
        "v_cvt_pk_bf16_f32 v106, v84, v85\n"
        "v_mfma_f32_32x32x16_bf16 v[64:79], v[140:143], a[188:191], v[64:79]\n"
        "v_exp_f32 v88, v88\n"
        "v_exp_f32 v89, v89\n"
        "v_cvt_pk_bf16_f32 v107, v86, v87\n"
        "v_mfma_f32_32x32x16_bf16 a[128:143], v[96:99], v[144:147], a[128:143]\n"
        "s_waitcnt vmcnt(11)\n"
        "v_exp_f32 v90, v90\n"
        "v_exp_f32 v91, v91\n"
        "v_cvt_pk_bf16_f32 v108, v88, v89\n"
        "v_smfmac_f32_16x16x64_bf16 v[244:247], a[240:243], v[192:199], v54\n"
        "buffer_load_dwordx4 v[192:195], v232, s[40:43], 0 offen sc1\n"
        "buffer_load_dwordx4 v[196:199], v233, s[40:43], 0 offen sc1\n"
        "v_mfma_f32_32x32x16_bf16 a[144:159], v[96:99], v[148:151], a[144:159]\n"
        "v_exp_f32 v92, v92\n"
        "v_exp_f32 v93, v93\n"
        "v_cvt_pk_bf16_f32 v109, v90, v91\n"
        "v_mad_u32_u16 v232, v231, s70, v37 op_sel:[0,0,0,0]\n"
        "v_mfma_f32_32x32x16_bf16 a[128:143], v[100:103], v[152:155], a[128:143]\n"
        "v_exp_f32 v94, v94\n"
        "v_exp_f32 v95, v95\n"
        "v_cvt_pk_bf16_f32 v110, v92, v93\n"
        "v_mad_u32_u16 v233, v231, s70, v37 op_sel:[1,0,0,0]\n"
        "v_mfma_f32_32x32x16_bf16 a[144:159], v[100:103], v[156:159], a[144:159]\n"
        "v_exp_f32 v64, v64\n"
        "v_exp_f32 v65, v65\n"
        "v_cvt_pk_bf16_f32 v111, v94, v95\n"
        "v_mfma_f32_32x32x16_bf16 v[80:95], v[128:131], a[192:195], 0\n"
        "ds_read_b128 v[112:115], v12 offset:8192\n"
        "v_exp_f32 v66, v66\n"
        "v_exp_f32 v67, v67\n"
        "v_cvt_pk_bf16_f32 v96, v64, v65\n"
        "v_mfma_f32_32x32x16_bf16 v[80:95], v[132:135], a[196:199], v[80:95]\n"
        "s_waitcnt vmcnt(11)\n"
        "ds_read_b128 v[116:119], v13 offset:8192\n"
        "v_exp_f32 v68, v68\n"
        "v_exp_f32 v69, v69\n"
        "v_cvt_pk_bf16_f32 v97, v66, v67\n"
        "v_smfmac_f32_16x16x64_bf16 v[244:247], a[240:243], v[200:207], v54\n"
        "buffer_load_dwordx4 v[200:203], v232, s[40:43], 0 offen sc1\n"
        "buffer_load_dwordx4 v[204:207], v233, s[40:43], 0 offen sc1\n"
        "v_mfma_f32_32x32x16_bf16 v[80:95], v[136:139], a[200:203], v[80:95]\n"
        "ds_read_b128 v[120:123], v14 offset:8192\n"
        "v_exp_f32 v70, v70\n"
        "v_exp_f32 v71, v71\n"
        "v_cvt_pk_bf16_f32 v98, v68, v69\n"
        "v_mfma_f32_32x32x16_bf16 v[80:95], v[140:143], a[204:207], v[80:95]\n"
        "ds_read_b128 v[124:127], v15 offset:8192\n"
        "v_exp_f32 v72, v72\n"
        "v_exp_f32 v73, v73\n"
        "v_cvt_pk_bf16_f32 v99, v70, v71\n"
        "v_mfma_f32_32x32x16_bf16 a[0:15], v[104:107], v[160:163], a[0:15]\n"
        "ds_read_b64_tr_b16 v[144:145], v16 offset:8192\n"
        "v_exp_f32 v74, v74\n"
        "v_exp_f32 v75, v75\n"
        "v_cvt_pk_bf16_f32 v100, v72, v73\n"
        "v_mfma_f32_32x32x16_bf16 a[16:31], v[104:107], v[164:167], a[16:31]\n"
        "ds_read_b64_tr_b16 v[146:147], v17 offset:8192\n"
        "v_exp_f32 v76, v76\n"
        "v_exp_f32 v77, v77\n"
        "v_cvt_pk_bf16_f32 v101, v74, v75\n"
        "v_mfma_f32_32x32x16_bf16 a[0:15], v[108:111], v[168:171], a[0:15]\n"
        "ds_read_b64_tr_b16 v[148:149], v18 offset:8192\n"
        "v_exp_f32 v78, v78\n"
        "v_exp_f32 v79, v79\n"
        "v_cvt_pk_bf16_f32 v102, v76, v77\n"
        "v_mfma_f32_32x32x16_bf16 a[16:31], v[108:111], v[172:175], a[16:31]\n"
        "s_waitcnt vmcnt(11)\n"
        "ds_read_b64_tr_b16 v[150:151], v19 offset:8192\n"
        "v_exp_f32 v80, v80\n"
        "v_exp_f32 v81, v81\n"
        "v_cvt_pk_bf16_f32 v103, v78, v79\n"
        "v_smfmac_f32_16x16x64_bf16 v[244:247], a[240:243], v[208:215], v54\n"
        "v_mfma_f32_32x32x16_bf16 v[64:79], v[128:131], a[208:211], 0\n"
        "ds_read_b64_tr_b16 v[152:153], v16 offset:10240\n"
        "v_exp_f32 v82, v82\n"
        "v_exp_f32 v83, v83\n"
        "v_cvt_pk_bf16_f32 v104, v80, v81\n"
        "v_mfma_f32_32x32x16_bf16 v[64:79], v[132:135], a[212:215], v[64:79]\n"
        "ds_read_b64_tr_b16 v[154:155], v17 offset:10240\n"
        "v_exp_f32 v84, v84\n"
        "v_exp_f32 v85, v85\n"
        "v_cvt_pk_bf16_f32 v105, v82, v83\n"
        "v_mfma_f32_32x32x16_bf16 v[64:79], v[136:139], a[216:219], v[64:79]\n"
        "ds_read_b64_tr_b16 v[156:157], v18 offset:10240\n"
        "v_exp_f32 v86, v86\n"
        "v_exp_f32 v87, v87\n"
        "v_cvt_pk_bf16_f32 v106, v84, v85\n"
        "v_mfma_f32_32x32x16_bf16 v[64:79], v[140:143], a[220:223], v[64:79]\n"
        "ds_read_b64_tr_b16 v[158:159], v19 offset:10240\n"
        "v_exp_f32 v88, v88\n"
        "v_exp_f32 v89, v89\n"
        "v_cvt_pk_bf16_f32 v107, v86, v87\n"
        "v_mfma_f32_32x32x16_bf16 a[32:47], v[96:99], v[160:163], a[32:47]\n"
        "s_waitcnt vmcnt(8)\n"
        "ds_read_b32 v38, v57\n"
        "v_exp_f32 v90, v90\n"
        "v_exp_f32 v91, v91\n"
        "v_cvt_pk_bf16_f32 v108, v88, v89\n"
        "v_smfmac_f32_16x16x64_bf16 v[244:247], a[240:243], v[216:223], v54\n"
        "v_mfma_f32_32x32x16_bf16 a[48:63], v[96:99], v[164:167], a[48:63]\n"
        "ds_read_b32 v39, v63 offset:4\n"
        "v_exp_f32 v92, v92\n"
        "v_exp_f32 v93, v93\n"
        "v_cvt_pk_bf16_f32 v109, v90, v91\n"
        "v_mfma_f32_32x32x16_bf16 a[32:47], v[100:103], v[168:171], a[32:47]\n"
        "v_exp_f32 v94, v94\n"
        "v_exp_f32 v95, v95\n"
        "v_cvt_pk_bf16_f32 v110, v92, v93\n"
        "v_mfma_f32_32x32x16_bf16 a[48:63], v[100:103], v[172:175], a[48:63]\n"
        "v_exp_f32 v64, v64\n"
        "v_exp_f32 v65, v65\n"
        "v_cvt_pk_bf16_f32 v111, v94, v95\n"
        "v_mfma_f32_32x32x16_bf16 v[80:95], v[128:131], a[224:227], 0\n"
        "buffer_load_dwordx2 v[50:51], v45, s[36:39], 0 offen sc1\n"
        "v_exp_f32 v66, v66\n"
        "v_exp_f32 v67, v67\n"
        "v_cvt_pk_bf16_f32 v96, v64, v65\n"
        "v_mfma_f32_32x32x16_bf16 v[80:95], v[132:135], a[228:231], v[80:95]\n"
        "s_waitcnt lgkmcnt(0)\n"
        "v_exp_f32 v68, v68\n"
        "v_exp_f32 v69, v69\n"
        "v_cvt_pk_bf16_f32 v97, v66, v67\n"
        "v_sub_u32 v38, v38, v58\n"
        "v_mfma_f32_32x32x16_bf16 v[80:95], v[136:139], a[232:235], v[80:95]\n"
        "s_waitcnt vmcnt(7)\n"
        "v_exp_f32 v70, v70\n"
        "v_exp_f32 v71, v71\n"
        "v_cvt_pk_bf16_f32 v98, v68, v69\n"
        "v_lshl_or_b32 v39, v39, 16, v38\n"
        "v_smfmac_f32_16x16x64_bf16 v[244:247], a[240:243], v[176:183], v54\n"
        "v_mfma_f32_32x32x16_bf16 v[80:95], v[140:143], a[236:239], v[80:95]\n"
        "v_exp_f32 v72, v72\n"
        "v_exp_f32 v73, v73\n"
        "v_cvt_pk_bf16_f32 v99, v70, v71\n"
        "v_cmp_lt_u32 vcc, 256, v39\n"
        "v_mfma_f32_32x32x16_bf16 a[64:79], v[104:107], v[160:163], a[64:79]\n"
        "ds_read_b128 v[128:131], v12 offset:12288\n"
        "v_exp_f32 v74, v74\n"
        "v_exp_f32 v75, v75\n"
        "v_cvt_pk_bf16_f32 v100, v72, v73\n"
        "v_mfma_f32_32x32x16_bf16 a[80:95], v[104:107], v[164:167], a[80:95]\n"
        "s_mul_i32 s72, s59, 10\n"
        "ds_read_b128 v[132:135], v13 offset:12288\n"
        "v_exp_f32 v76, v76\n"
        "v_exp_f32 v77, v77\n"
        "v_cvt_pk_bf16_f32 v101, v74, v75\n"
        "v_mfma_f32_32x32x16_bf16 a[64:79], v[108:111], v[168:171], a[64:79]\n"
        "ds_read_b128 v[136:139], v14 offset:12288\n"
        "v_exp_f32 v78, v78\n"
        "v_exp_f32 v79, v79\n"
        "v_cvt_pk_bf16_f32 v102, v76, v77\n"
        "v_mfma_f32_32x32x16_bf16 a[80:95], v[108:111], v[172:175], a[80:95]\n"
        "s_waitcnt vmcnt(5)\n"
        "ds_read_b128 v[140:143], v15 offset:12288\n"
        "v_exp_f32 v80, v80\n"
        "v_exp_f32 v81, v81\n"
        "v_cvt_pk_bf16_f32 v103, v78, v79\n"
        "v_smfmac_f32_16x16x64_bf16 v[244:247], a[240:243], v[184:191], v54\n"
        "v_mfma_f32_32x32x16_bf16 v[64:79], v[112:115], a[160:163], 0\n"
        "s_cmp_lg_u64 vcc, 0\n"
        "s_cselect_b32 s73, 0, 0x3ff\n"
        "v_exp_f32 v82, v82\n"
        "v_exp_f32 v83, v83\n"
        "v_cvt_pk_bf16_f32 v104, v80, v81\n"
        "v_cmp_lt_u32 vcc, 160, v38\n"
        "v_mfma_f32_32x32x16_bf16 v[64:79], v[116:119], a[164:167], v[64:79]\n"
        "buffer_load_dwordx2 v[248:249], v46, s[92:95], 0 offen offset:0\n"
        "v_exp_f32 v84, v84\n"
        "v_exp_f32 v85, v85\n"
        "v_cvt_pk_bf16_f32 v105, v82, v83\n"
        "v_mfma_f32_32x32x16_bf16 v[64:79], v[120:123], a[168:171], v[64:79]\n"
        "s_lshr_b64 s[86:87], vcc, s72\n"
        "buffer_load_dwordx2 v[250:251], v46, s[92:95], 0 offen offset:128\n"
        "v_exp_f32 v86, v86\n"
        "v_exp_f32 v87, v87\n"
        "v_cvt_pk_bf16_f32 v106, v84, v85\n"
        "v_mfma_f32_32x32x16_bf16 v[64:79], v[124:127], a[172:175], v[64:79]\n"
        "s_and_b32 s86, s86, s73\n"
        "buffer_load_dwordx2 v[252:253], v47, s[92:95], 0 offen offset:256\n"
        "v_exp_f32 v88, v88\n"
        "v_exp_f32 v89, v89\n"
        "v_cvt_pk_bf16_f32 v107, v86, v87\n"
        "v_mfma_f32_32x32x16_bf16 a[96:111], v[96:99], v[160:163], a[96:111]\n"
        "buffer_load_dwordx2 v[254:255], v47, s[92:95], 0 offen offset:384\n"
        "v_exp_f32 v90, v90\n"
        "v_exp_f32 v91, v91\n"
        "v_cvt_pk_bf16_f32 v108, v88, v89\n"
        "v_mfma_f32_32x32x16_bf16 a[112:127], v[96:99], v[164:167], a[112:127]\n"
        "s_waitcnt vmcnt(7)\n"
        "global_load_dwordx2 v[10:11], v48, s[10:11]\n"
        "v_exp_f32 v92, v92\n"
        "v_exp_f32 v93, v93\n"
        "v_cvt_pk_bf16_f32 v109, v90, v91\n"
        "v_smfmac_f32_16x16x64_bf16 v[244:247], a[240:243], v[192:199], v54\n"
        "v_mfma_f32_32x32x16_bf16 a[96:111], v[100:103], v[168:171], a[96:111]\n"
        "v_exp_f32 v94, v94\n"
        "v_exp_f32 v95, v95\n"
        "v_cvt_pk_bf16_f32 v110, v92, v93\n"
        "v_mfma_f32_32x32x16_bf16 a[112:127], v[100:103], v[172:175], a[112:127]\n"
        "s_waitcnt lgkmcnt(0)\n"
        "v_exp_f32 v64, v64\n"
        "v_exp_f32 v65, v65\n"
        "v_cvt_pk_bf16_f32 v111, v94, v95\n"
        "v_mfma_f32_32x32x16_bf16 v[80:95], v[128:131], a[160:163], 0\n"
        "v_exp_f32 v66, v66\n"
        "v_exp_f32 v67, v67\n"
        "v_cvt_pk_bf16_f32 v96, v64, v65\n"
        "v_mfma_f32_32x32x16_bf16 v[80:95], v[132:135], a[164:167], v[80:95]\n"
        "v_exp_f32 v68, v68\n"
        "v_exp_f32 v69, v69\n"
        "v_cvt_pk_bf16_f32 v97, v66, v67\n"
        "v_mfma_f32_32x32x16_bf16 v[80:95], v[136:139], a[168:171], v[80:95]\n"
        "s_waitcnt vmcnt(6)\n"
        "v_exp_f32 v70, v70\n"
        "v_exp_f32 v71, v71\n"
        "v_cvt_pk_bf16_f32 v98, v68, v69\n"
        "v_smfmac_f32_16x16x64_bf16 v[244:247], a[240:243], v[200:207], v54\n"
        "v_mfma_f32_32x32x16_bf16 v[80:95], v[140:143], a[172:175], v[80:95]\n"
        "v_exp_f32 v72, v72\n"
        "v_exp_f32 v73, v73\n"
        "v_cvt_pk_bf16_f32 v99, v70, v71\n"
        "v_mfma_f32_32x32x16_bf16 a[128:143], v[104:107], v[160:163], a[128:143]\n"
        "v_exp_f32 v74, v74\n"
        "v_exp_f32 v75, v75\n"
        "v_cvt_pk_bf16_f32 v100, v72, v73\n"
        "v_mad_u32_u16 v232, v24, s70, v37 op_sel:[0,0,0,0]\n"
        "v_mfma_f32_32x32x16_bf16 a[144:159], v[104:107], v[164:167], a[144:159]\n"
        "buffer_load_dwordx4 a[160:163], v232, s[40:43], 0 offen sc1\n"
        "v_exp_f32 v76, v76\n"
        "v_exp_f32 v77, v77\n"
        "v_cvt_pk_bf16_f32 v101, v74, v75\n"
        "v_mfma_f32_32x32x16_bf16 a[128:143], v[108:111], v[168:171], a[128:143]\n"
        "v_exp_f32 v78, v78\n"
        "v_exp_f32 v79, v79\n"
        "v_cvt_pk_bf16_f32 v102, v76, v77\n"
        "v_mad_u32_u16 v233, v24, s70, v37 op_sel:[1,0,0,0]\n"
        "v_mfma_f32_32x32x16_bf16 a[144:159], v[108:111], v[172:175], a[144:159]\n"
        "buffer_load_dwordx4 a[164:167], v233, s[40:43], 0 offen sc1\n"
        "v_exp_f32 v80, v80\n"
        "v_exp_f32 v81, v81\n"
        "v_cvt_pk_bf16_f32 v103, v78, v79\n"
        "v_mfma_f32_32x32x16_bf16 v[64:79], v[112:115], a[176:179], 0\n"
        "ds_read_b64_tr_b16 v[160:161], v16 offset:12288\n"
        "v_exp_f32 v82, v82\n"
        "v_exp_f32 v83, v83\n"
        "v_cvt_pk_bf16_f32 v104, v80, v81\n"
        "v_mad_u32_u16 v232, v25, s70, v37 op_sel:[0,0,0,0]\n"
        "v_mfma_f32_32x32x16_bf16 v[64:79], v[116:119], a[180:183], v[64:79]\n"
        "ds_read_b64_tr_b16 v[162:163], v17 offset:12288\n"
        "v_exp_f32 v84, v84\n"
        "v_exp_f32 v85, v85\n"
        "v_cvt_pk_bf16_f32 v105, v82, v83\n"
        "v_mfma_f32_32x32x16_bf16 v[64:79], v[120:123], a[184:187], v[64:79]\n"
        "ds_read_b64_tr_b16 v[164:165], v18 offset:12288\n"
        "v_exp_f32 v86, v86\n"
        "v_exp_f32 v87, v87\n"
        "v_cvt_pk_bf16_f32 v106, v84, v85\n"
        "v_mfma_f32_32x32x16_bf16 v[64:79], v[124:127], a[188:191], v[64:79]\n"
        "ds_read_b64_tr_b16 v[166:167], v19 offset:12288\n"
        "v_exp_f32 v88, v88\n"
        "v_exp_f32 v89, v89\n"
        "v_cvt_pk_bf16_f32 v107, v86, v87\n"
        "v_mfma_f32_32x32x16_bf16 a[0:15], v[96:99], v[144:147], a[0:15]\n"
        "ds_read_b64_tr_b16 v[168:169], v16 offset:14336\n"
        "v_exp_f32 v90, v90\n"
        "v_exp_f32 v91, v91\n"
        "v_cvt_pk_bf16_f32 v108, v88, v89\n"
        "v_mfma_f32_32x32x16_bf16 a[16:31], v[96:99], v[148:151], a[16:31]\n"
        "ds_read_b64_tr_b16 v[170:171], v17 offset:14336\n"
        "v_exp_f32 v92, v92\n"
        "v_exp_f32 v93, v93\n"
        "v_cvt_pk_bf16_f32 v109, v90, v91\n"
        "v_mfma_f32_32x32x16_bf16 a[0:15], v[100:103], v[152:155], a[0:15]\n"
        "ds_read_b64_tr_b16 v[172:173], v18 offset:14336\n"
        "v_exp_f32 v94, v94\n"
        "v_exp_f32 v95, v95\n"
        "v_cvt_pk_bf16_f32 v110, v92, v93\n"
        "v_mfma_f32_32x32x16_bf16 a[16:31], v[100:103], v[156:159], a[16:31]\n"
        "ds_read_b64_tr_b16 v[174:175], v19 offset:14336\n"
        "v_exp_f32 v64, v64\n"
        "v_exp_f32 v65, v65\n"
        "v_cvt_pk_bf16_f32 v111, v94, v95\n"
        "v_mfma_f32_32x32x16_bf16 v[80:95], v[128:131], a[176:179], 0\n"
        "ds_read_b64 v[224:225], v36 offset:128\n"
        "v_exp_f32 v66, v66\n"
        "v_exp_f32 v67, v67\n"
        "v_cvt_pk_bf16_f32 v96, v64, v65\n"
        "v_mfma_f32_32x32x16_bf16 v[80:95], v[132:135], a[180:183], v[80:95]\n"
        "ds_read_b64 v[226:227], v36 offset:672\n"
        "v_exp_f32 v68, v68\n"
        "v_exp_f32 v69, v69\n"
        "v_cvt_pk_bf16_f32 v97, v66, v67\n"
        "v_mfma_f32_32x32x16_bf16 v[80:95], v[136:139], a[184:187], v[80:95]\n"
        "ds_read_b64 v[228:229], v36 offset:1216\n"
        "v_exp_f32 v70, v70\n"
        "v_exp_f32 v71, v71\n"
        "v_cvt_pk_bf16_f32 v98, v68, v69\n"
        "v_mfma_f32_32x32x16_bf16 v[80:95], v[140:143], a[188:191], v[80:95]\n"
        "s_waitcnt lgkmcnt(3)\n"
        "ds_read_b64 v[230:231], v36 offset:1760\n"
        "v_exp_f32 v72, v72\n"
        "v_exp_f32 v73, v73\n"
        "v_cvt_pk_bf16_f32 v99, v70, v71\n"
        "v_mfma_f32_32x32x16_bf16 a[0:15], v[104:107], v[160:163], a[0:15]\n"
        "ds_read_b64 v[234:235], v36 offset:2304\n"
        "v_exp_f32 v74, v74\n"
        "v_exp_f32 v75, v75\n"
        "v_cvt_pk_bf16_f32 v100, v72, v73\n"
        "v_add_u32 v36, 32, v36\n"
        "v_mfma_f32_32x32x16_bf16 a[16:31], v[104:107], v[164:167], a[16:31]\n"
        "buffer_load_dwordx4 a[168:171], v232, s[40:43], 0 offen sc1\n"
        "v_exp_f32 v76, v76\n"
        "v_exp_f32 v77, v77\n"
        "v_cvt_pk_bf16_f32 v101, v74, v75\n"
        "v_mfma_f32_32x32x16_bf16 a[0:15], v[108:111], v[168:171], a[0:15]\n"
        "v_exp_f32 v78, v78\n"
        "v_exp_f32 v79, v79\n"
        "v_cvt_pk_bf16_f32 v102, v76, v77\n"
        "v_mad_u32_u16 v233, v25, s70, v37 op_sel:[1,0,0,0]\n"
        "v_mfma_f32_32x32x16_bf16 a[16:31], v[108:111], v[172:175], a[16:31]\n"
        "buffer_load_dwordx4 a[172:175], v233, s[40:43], 0 offen sc1\n"
        "v_exp_f32 v80, v80\n"
        "v_exp_f32 v81, v81\n"
        "v_cvt_pk_bf16_f32 v103, v78, v79\n"
        "v_mfma_f32_32x32x16_bf16 v[64:79], v[112:115], a[192:195], 0\n"
        "s_waitcnt lgkmcnt(0)\n"
        "v_exp_f32 v82, v82\n"
        "v_exp_f32 v83, v83\n"
        "v_cvt_pk_bf16_f32 v104, v80, v81\n"
        "v_mad_u32_u16 v232, v26, s70, v37 op_sel:[0,0,0,0]\n"
        "v_mfma_f32_32x32x16_bf16 v[64:79], v[116:119], a[196:199], v[64:79]\n"
        "buffer_load_dwordx4 a[176:179], v232, s[40:43], 0 offen sc1\n"
        "v_exp_f32 v84, v84\n"
        "v_exp_f32 v85, v85\n"
        "v_cvt_pk_bf16_f32 v105, v82, v83\n"
        "v_mfma_f32_32x32x16_bf16 v[64:79], v[120:123], a[200:203], v[64:79]\n"
        "ds_write_b128 v20, a[4:7] offset:0\n"
        "v_exp_f32 v86, v86\n"
        "v_exp_f32 v87, v87\n"
        "v_cvt_pk_bf16_f32 v106, v84, v85\n"
        "v_mad_u32_u16 v233, v26, s70, v37 op_sel:[1,0,0,0]\n"
        "v_mfma_f32_32x32x16_bf16 v[64:79], v[124:127], a[204:207], v[64:79]\n"
        "buffer_load_dwordx4 a[180:183], v233, s[40:43], 0 offen sc1\n"
        "v_exp_f32 v88, v88\n"
        "v_exp_f32 v89, v89\n"
        "v_cvt_pk_bf16_f32 v107, v86, v87\n"
        "v_accvgpr_read_b32 v176, a0\n"
        "v_mfma_f32_32x32x16_bf16 a[32:47], v[96:99], v[144:147], a[32:47]\n"
        "ds_write_b128 v20, a[8:11] offset:10240\n"
        "v_exp_f32 v90, v90\n"
        "v_exp_f32 v91, v91\n"
        "v_cvt_pk_bf16_f32 v108, v88, v89\n"
        "v_mad_u32_u16 v232, v27, s70, v37 op_sel:[0,0,0,0]\n"
        "v_mfma_f32_32x32x16_bf16 a[48:63], v[96:99], v[148:151], a[48:63]\n"
        "buffer_load_dwordx4 a[184:187], v232, s[40:43], 0 offen sc1\n"
        "v_exp_f32 v92, v92\n"
        "v_exp_f32 v93, v93\n"
        "v_cvt_pk_bf16_f32 v109, v90, v91\n"
        "v_accvgpr_read_b32 v177, a1\n"
        "v_mfma_f32_32x32x16_bf16 a[32:47], v[100:103], v[152:155], a[32:47]\n"
        "ds_write_b128 v20, a[12:15] offset:20480\n"
        "v_exp_f32 v94, v94\n"
        "v_exp_f32 v95, v95\n"
        "v_cvt_pk_bf16_f32 v110, v92, v93\n"
        "v_mad_u32_u16 v233, v27, s70, v37 op_sel:[1,0,0,0]\n"
        "v_mfma_f32_32x32x16_bf16 a[48:63], v[100:103], v[156:159], a[48:63]\n"
        "buffer_load_dwordx4 a[188:191], v233, s[40:43], 0 offen sc1\n"
        "v_exp_f32 v64, v64\n"
        "v_exp_f32 v65, v65\n"
        "v_cvt_pk_bf16_f32 v111, v94, v95\n"
        "v_accvgpr_read_b32 v178, a2\n"
        "v_mfma_f32_32x32x16_bf16 v[80:95], v[128:131], a[192:195], 0\n"
        "s_waitcnt vmcnt(13)\n"
        "ds_write_b128 v20, a[20:23] offset:1024\n"
        "v_exp_f32 v66, v66\n"
        "v_exp_f32 v67, v67\n"
        "v_cvt_pk_bf16_f32 v96, v64, v65\n"
        "v_lshrrev_b64 v[52:53], 24, v[50:51]\n"
        "v_mfma_f32_32x32x16_bf16 v[80:95], v[132:135], a[196:199], v[80:95]\n"
        "v_exp_f32 v68, v68\n"
        "v_exp_f32 v69, v69\n"
        "v_cvt_pk_bf16_f32 v97, v66, v67\n"
        "v_cmp_ne_u64 vcc, s[54:55], v[52:53]\n"
        "v_accvgpr_read_b32 v179, a3\n"
        "v_mfma_f32_32x32x16_bf16 v[80:95], v[136:139], a[200:203], v[80:95]\n"
        "s_cbranch_vccnz 2f\n"
        "3:\n"
        "ds_write_b128 v20, a[24:27] offset:11264\n"
        "v_exp_f32 v70, v70\n"
        "v_exp_f32 v71, v71\n"
        "v_cvt_pk_bf16_f32 v98, v68, v69\n"
        "v_accvgpr_read_b32 v180, a16\n"
        "v_mfma_f32_32x32x16_bf16 v[80:95], v[140:143], a[204:207], v[80:95]\n"
        "v_exp_f32 v72, v72\n"
        "v_exp_f32 v73, v73\n"
        "v_cvt_pk_bf16_f32 v99, v70, v71\n"
        "v_accvgpr_read_b32 v181, a17\n"
        "v_mfma_f32_32x32x16_bf16 a[32:47], v[104:107], v[160:163], a[32:47]\n"
        "ds_write_b128 v20, a[28:31] offset:21504\n"
        "v_exp_f32 v74, v74\n"
        "v_exp_f32 v75, v75\n"
        "v_cvt_pk_bf16_f32 v100, v72, v73\n"
        "v_mad_u32_u16 v232, v28, s70, v37 op_sel:[0,0,0,0]\n"
        "v_mfma_f32_32x32x16_bf16 a[48:63], v[104:107], v[164:167], a[48:63]\n"
        "buffer_load_dwordx4 a[192:195], v232, s[40:43], 0 offen sc1\n"
        "v_exp_f32 v76, v76\n"
        "v_exp_f32 v77, v77\n"
        "v_cvt_pk_bf16_f32 v101, v74, v75\n"
        "v_accvgpr_read_b32 v182, a18\n"
        "v_mfma_f32_32x32x16_bf16 a[32:47], v[108:111], v[168:171], a[32:47]\n"
        "v_exp_f32 v78, v78\n"
        "v_exp_f32 v79, v79\n"
        "v_cvt_pk_bf16_f32 v102, v76, v77\n"
        "v_mad_u32_u16 v233, v28, s70, v37 op_sel:[1,0,0,0]\n"
        "v_accvgpr_read_b32 v183, a19\n"
        "v_mfma_f32_32x32x16_bf16 a[48:63], v[108:111], v[172:175], a[48:63]\n"
        "buffer_load_dwordx4 a[196:199], v233, s[40:43], 0 offen sc1\n"
        "v_exp_f32 v80, v80\n"
        "v_exp_f32 v81, v81\n"
        "v_cvt_pk_bf16_f32 v103, v78, v79\n"
        "v_mfma_f32_32x32x16_bf16 v[64:79], v[112:115], a[208:211], 0\n"
        "s_and_b32 s74, s86, 3\n"
        "v_exp_f32 v82, v82\n"
        "v_exp_f32 v83, v83\n"
        "v_cvt_pk_bf16_f32 v104, v80, v81\n"
        "v_mad_u32_u16 v232, v29, s70, v37 op_sel:[0,0,0,0]\n"
        "v_mfma_f32_32x32x16_bf16 v[64:79], v[116:119], a[212:215], v[64:79]\n"
        "s_cbranch_scc1 70f\n"
        "80:\n"
        "buffer_load_dwordx4 a[200:203], v232, s[40:43], 0 offen sc1\n"
        "v_exp_f32 v84, v84\n"
        "v_exp_f32 v85, v85\n"
        "v_cvt_pk_bf16_f32 v105, v82, v83\n"
        "v_mfma_f32_32x32x16_bf16 v[64:79], v[120:123], a[216:219], v[64:79]\n"
        "s_and_b32 s74, s86, 12\n"
        "ds_write_b128 v20, a[36:39] offset:2048\n"
        "v_exp_f32 v86, v86\n"
        "v_exp_f32 v87, v87\n"
        "v_cvt_pk_bf16_f32 v106, v84, v85\n"
        "v_mad_u32_u16 v233, v29, s70, v37 op_sel:[1,0,0,0]\n"
        "v_mfma_f32_32x32x16_bf16 v[64:79], v[124:127], a[220:223], v[64:79]\n"
        "s_cbranch_scc1 71f\n"
        "81:\n"
        "buffer_load_dwordx4 a[204:207], v233, s[40:43], 0 offen sc1\n"
        "v_exp_f32 v88, v88\n"
        "v_exp_f32 v89, v89\n"
        "v_cvt_pk_bf16_f32 v107, v86, v87\n"
        "v_accvgpr_read_b32 v184, a32\n"
        "v_mfma_f32_32x32x16_bf16 a[64:79], v[96:99], v[144:147], a[64:79]\n"
        "ds_write_b128 v20, a[40:43] offset:12288\n"
        "v_exp_f32 v90, v90\n"
        "v_exp_f32 v91, v91\n"
        "v_cvt_pk_bf16_f32 v108, v88, v89\n"
        "v_accvgpr_read_b32 v185, a33\n"
        "v_mfma_f32_32x32x16_bf16 a[80:95], v[96:99], v[148:151], a[80:95]\n"
        "v_exp_f32 v92, v92\n"
        "v_exp_f32 v93, v93\n"
        "v_cvt_pk_bf16_f32 v109, v90, v91\n"
        "v_accvgpr_read_b32 v186, a34\n"
        "v_mfma_f32_32x32x16_bf16 a[64:79], v[100:103], v[152:155], a[64:79]\n"
        "ds_write_b128 v20, a[44:47] offset:22528\n"
        "v_exp_f32 v94, v94\n"
        "v_exp_f32 v95, v95\n"
        "v_cvt_pk_bf16_f32 v110, v92, v93\n"
        "v_accvgpr_read_b32 v187, a35\n"
        "v_mfma_f32_32x32x16_bf16 a[80:95], v[100:103], v[156:159], a[80:95]\n"
        "v_exp_f32 v64, v64\n"
        "v_exp_f32 v65, v65\n"
        "v_cvt_pk_bf16_f32 v111, v94, v95\n"
        "v_accvgpr_read_b32 v188, a48\n"
        "v_mfma_f32_32x32x16_bf16 v[80:95], v[128:131], a[208:211], 0\n"
        "ds_write_b128 v20, a[52:55] offset:3072\n"
        "v_exp_f32 v66, v66\n"
        "v_exp_f32 v67, v67\n"
        "v_cvt_pk_bf16_f32 v96, v64, v65\n"
        "v_accvgpr_read_b32 v189, a49\n"
        "v_mfma_f32_32x32x16_bf16 v[80:95], v[132:135], a[212:215], v[80:95]\n"
        "v_exp_f32 v68, v68\n"
        "v_exp_f32 v69, v69\n"
        "v_cvt_pk_bf16_f32 v97, v66, v67\n"
        "v_accvgpr_read_b32 v190, a50\n"
        "v_mfma_f32_32x32x16_bf16 v[80:95], v[136:139], a[216:219], v[80:95]\n"
        "ds_write_b128 v20, a[56:59] offset:13312\n"
        "v_exp_f32 v70, v70\n"
        "v_exp_f32 v71, v71\n"
        "v_cvt_pk_bf16_f32 v98, v68, v69\n"
        "v_accvgpr_read_b32 v191, a51\n"
        "v_mfma_f32_32x32x16_bf16 v[80:95], v[140:143], a[220:223], v[80:95]\n"
        "v_exp_f32 v72, v72\n"
        "v_exp_f32 v73, v73\n"
        "v_cvt_pk_bf16_f32 v99, v70, v71\n"
        "v_mfma_f32_32x32x16_bf16 a[64:79], v[104:107], v[160:163], a[64:79]\n"
        "ds_write_b128 v20, a[60:63] offset:23552\n"
        "v_exp_f32 v74, v74\n"
        "v_exp_f32 v75, v75\n"
        "v_cvt_pk_bf16_f32 v100, v72, v73\n"
        "v_mad_u32_u16 v232, v30, s70, v37 op_sel:[0,0,0,0]\n"
        "v_mfma_f32_32x32x16_bf16 a[80:95], v[104:107], v[164:167], a[80:95]\n"
        "buffer_load_dwordx4 a[208:211], v232, s[40:43], 0 offen sc1\n"
        "v_exp_f32 v76, v76\n"
        "v_exp_f32 v77, v77\n"
        "v_cvt_pk_bf16_f32 v101, v74, v75\n"
        "v_mfma_f32_32x32x16_bf16 a[64:79], v[108:111], v[168:171], a[64:79]\n"
        "v_exp_f32 v78, v78\n"
        "v_exp_f32 v79, v79\n"
        "v_cvt_pk_bf16_f32 v102, v76, v77\n"
        "v_mad_u32_u16 v233, v30, s70, v37 op_sel:[1,0,0,0]\n"
        "v_mfma_f32_32x32x16_bf16 a[80:95], v[108:111], v[172:175], a[80:95]\n"
        "buffer_load_dwordx4 a[212:215], v233, s[40:43], 0 offen sc1\n"
        "v_exp_f32 v80, v80\n"
        "v_exp_f32 v81, v81\n"
        "v_cvt_pk_bf16_f32 v103, v78, v79\n"
        "v_mfma_f32_32x32x16_bf16 v[64:79], v[112:115], a[224:227], 0\n"
        "s_and_b32 s74, s86, 48\n"
        "v_exp_f32 v82, v82\n"
        "v_exp_f32 v83, v83\n"
        "v_cvt_pk_bf16_f32 v104, v80, v81\n"
        "v_mad_u32_u16 v232, v31, s70, v37 op_sel:[0,0,0,0]\n"
        "v_mfma_f32_32x32x16_bf16 v[64:79], v[116:119], a[228:231], v[64:79]\n"
        "s_cbranch_scc1 72f\n"
        "82:\n"
        "buffer_load_dwordx4 a[216:219], v232, s[40:43], 0 offen sc1\n"
        "v_exp_f32 v84, v84\n"
        "v_exp_f32 v85, v85\n"
        "v_cvt_pk_bf16_f32 v105, v82, v83\n"
        "v_mfma_f32_32x32x16_bf16 v[64:79], v[120:123], a[232:235], v[64:79]\n"
        "s_and_b32 s74, s86, 192\n"
        "ds_write_b128 v20, a[68:71] offset:4096\n"
        "v_exp_f32 v86, v86\n"
        "v_exp_f32 v87, v87\n"
        "v_cvt_pk_bf16_f32 v106, v84, v85\n"
        "v_mad_u32_u16 v233, v31, s70, v37 op_sel:[1,0,0,0]\n"
        "v_mfma_f32_32x32x16_bf16 v[64:79], v[124:127], a[236:239], v[64:79]\n"
        "s_cbranch_scc1 73f\n"
        "83:\n"
        "buffer_load_dwordx4 a[220:223], v233, s[40:43], 0 offen sc1\n"
        "v_exp_f32 v88, v88\n"
        "v_exp_f32 v89, v89\n"
        "v_cvt_pk_bf16_f32 v107, v86, v87\n"
        "v_accvgpr_read_b32 v192, a64\n"
        "v_mfma_f32_32x32x16_bf16 a[96:111], v[96:99], v[144:147], a[96:111]\n"
        "ds_write_b128 v20, a[72:75] offset:14336\n"
        "v_exp_f32 v90, v90\n"
        "v_exp_f32 v91, v91\n"
        "v_cvt_pk_bf16_f32 v108, v88, v89\n"
        "v_accvgpr_read_b32 v193, a65\n"
        "v_mfma_f32_32x32x16_bf16 a[112:127], v[96:99], v[148:151], a[112:127]\n"
        "v_exp_f32 v92, v92\n"
        "v_exp_f32 v93, v93\n"
        "v_cvt_pk_bf16_f32 v109, v90, v91\n"
        "v_accvgpr_read_b32 v194, a66\n"
        "v_mfma_f32_32x32x16_bf16 a[96:111], v[100:103], v[152:155], a[96:111]\n"
        "ds_write_b128 v20, a[76:79] offset:24576\n"
        "v_exp_f32 v94, v94\n"
        "v_exp_f32 v95, v95\n"
        "v_cvt_pk_bf16_f32 v110, v92, v93\n"
        "v_accvgpr_read_b32 v195, a67\n"
        "v_mfma_f32_32x32x16_bf16 a[112:127], v[100:103], v[156:159], a[112:127]\n"
        "v_exp_f32 v64, v64\n"
        "v_exp_f32 v65, v65\n"
        "v_cvt_pk_bf16_f32 v111, v94, v95\n"
        "v_accvgpr_read_b32 v196, a80\n"
        "v_mfma_f32_32x32x16_bf16 v[80:95], v[128:131], a[224:227], 0\n"
        "s_and_b32 s74, s86, 768\n"
        "ds_write_b128 v20, a[84:87] offset:5120\n"
        "v_exp_f32 v66, v66\n"
        "v_exp_f32 v67, v67\n"
        "v_cvt_pk_bf16_f32 v96, v64, v65\n"
        "v_accvgpr_read_b32 v197, a81\n"
        "v_mfma_f32_32x32x16_bf16 v[80:95], v[132:135], a[228:231], v[80:95]\n"
        "s_cbranch_scc1 74f\n"
        "84:\n"
        "v_exp_f32 v68, v68\n"
        "v_exp_f32 v69, v69\n"
        "v_cvt_pk_bf16_f32 v97, v66, v67\n"
        "v_accvgpr_read_b32 v198, a82\n"
        "v_mfma_f32_32x32x16_bf16 v[80:95], v[136:139], a[232:235], v[80:95]\n"
        "ds_write_b128 v20, a[88:91] offset:15360\n"
        "v_exp_f32 v70, v70\n"
        "v_exp_f32 v71, v71\n"
        "v_cvt_pk_bf16_f32 v98, v68, v69\n"
        "v_accvgpr_read_b32 v199, a83\n"
        "v_mfma_f32_32x32x16_bf16 v[80:95], v[140:143], a[236:239], v[80:95]\n"
        "v_exp_f32 v72, v72\n"
        "v_exp_f32 v73, v73\n"
        "v_cvt_pk_bf16_f32 v99, v70, v71\n"
        "v_mfma_f32_32x32x16_bf16 a[96:111], v[104:107], v[160:163], a[96:111]\n"
        "ds_write_b128 v20, a[92:95] offset:25600\n"
        "v_exp_f32 v74, v74\n"
        "v_exp_f32 v75, v75\n"
        "v_cvt_pk_bf16_f32 v100, v72, v73\n"
        "v_mfma_f32_32x32x16_bf16 a[112:127], v[104:107], v[164:167], a[112:127]\n"
        "v_exp_f32 v76, v76\n"
        "v_exp_f32 v77, v77\n"
        "v_cvt_pk_bf16_f32 v101, v74, v75\n"
        "v_mfma_f32_32x32x16_bf16 a[96:111], v[108:111], v[168:171], a[96:111]\n"
        "s_waitcnt lgkmcnt(0)\n"
        "v_exp_f32 v78, v78\n"
        "v_exp_f32 v79, v79\n"
        "v_cvt_pk_bf16_f32 v102, v76, v77\n"
        "v_mfma_f32_32x32x16_bf16 a[112:127], v[108:111], v[172:175], a[112:127]\n"
        "s_barrier\n"
        "v_exp_f32 v80, v80\n"
        "v_exp_f32 v81, v81\n"
        "v_cvt_pk_bf16_f32 v103, v78, v79\n"
        "v_mfma_f32_32x32x16_bf16 a[128:143], v[96:99], v[144:147], a[128:143]\n"
        "ds_read_b128 v[112:115], v40 offset:0\n"
        "ds_read_b128 v[116:119], v41 offset:10240\n"
        "ds_read_b128 v[120:123], v42 offset:20480\n"
        "v_exp_f32 v82, v82\n"
        "v_exp_f32 v83, v83\n"
        "v_cvt_pk_bf16_f32 v104, v80, v81\n"
        "v_mad_u32_u16 v232, v32, s70, v37 op_sel:[0,0,0,0]\n"
        "v_mfma_f32_32x32x16_bf16 a[144:159], v[96:99], v[148:151], a[144:159]\n"
        "ds_read_b128 v[124:127], v40 offset:1024\n"
        "ds_read_b128 v[128:131], v41 offset:11264\n"
        "ds_read_b128 v[132:135], v42 offset:21504\n"
        "buffer_load_dwordx4 a[224:227], v232, s[40:43], 0 offen sc1\n"
        "v_exp_f32 v84, v84\n"
        "v_exp_f32 v85, v85\n"
        "v_cvt_pk_bf16_f32 v105, v82, v83\n"
        "v_mad_u32_u16 v233, v32, s70, v37 op_sel:[1,0,0,0]\n"
        "v_mfma_f32_32x32x16_bf16 a[128:143], v[100:103], v[152:155], a[128:143]\n"
        "ds_write_b128 v20, a[100:103] offset:6144\n"
        "buffer_load_dwordx4 a[228:231], v233, s[40:43], 0 offen sc1\n"
        "ds_read_b128 v[136:139], v40 offset:2048\n"
        "ds_read_b128 v[140:143], v41 offset:12288\n"
        "v_exp_f32 v86, v86\n"
        "v_exp_f32 v87, v87\n"
        "v_cvt_pk_bf16_f32 v106, v84, v85\n"
        "v_mad_u32_u16 v232, v33, s70, v37 op_sel:[0,0,0,0]\n"
        "v_mfma_f32_32x32x16_bf16 a[144:159], v[100:103], v[156:159], a[144:159]\n"
        "ds_write_b128 v20, a[104:107] offset:16384\n"
        "buffer_load_dwordx4 a[232:235], v232, s[40:43], 0 offen sc1\n"
        "ds_read_b128 v[64:67], v42 offset:22528\n"
        "ds_read_b128 v[68:71], v40 offset:3072\n"
        "v_exp_f32 v88, v88\n"
        "v_exp_f32 v89, v89\n"
        "v_cvt_pk_bf16_f32 v107, v86, v87\n"
        "v_exp_f32 v90, v90\n"
        "v_exp_f32 v91, v91\n"
        "v_cvt_pk_bf16_f32 v108, v88, v89\n"
        "v_mad_u32_u16 v233, v33, s70, v37 op_sel:[1,0,0,0]\n"
        "v_mfma_f32_32x32x16_bf16 a[128:143], v[104:107], v[160:163], a[128:143]\n"
        "ds_write_b128 v20, a[108:111] offset:26624\n"
        "buffer_load_dwordx4 a[236:239], v233, s[40:43], 0 offen sc1\n"
        "ds_read_b128 v[72:75], v41 offset:13312\n"
        "ds_read_b128 v[76:79], v42 offset:23552\n"
        "v_exp_f32 v92, v92\n"
        "v_exp_f32 v93, v93\n"
        "v_cvt_pk_bf16_f32 v109, v90, v91\n"
        "v_mfma_f32_32x32x16_bf16 a[144:159], v[104:107], v[164:167], a[144:159]\n"
        "ds_write_b128 v20, a[116:119] offset:7168\n"
        "ds_read_b128 v[216:219], v40 offset:4096\n"
        "ds_read_b128 v[220:223], v41 offset:14336\n"
        "ds_read_b128 v[224:227], v42 offset:24576\n"
        "v_exp_f32 v94, v94\n"
        "v_exp_f32 v95, v95\n"
        "v_cvt_pk_bf16_f32 v110, v92, v93\n"
        "v_cvt_pk_bf16_f32 v111, v94, v95\n"
        "ds_write_b128 v20, a[120:123] offset:17408\n"
        "s_nop 0\n"
        "v_mfma_f32_32x32x16_bf16 a[128:143], v[108:111], v[168:171], a[128:143]\n"
        "ds_write_b128 v20, a[124:127] offset:27648\n"
        "ds_read_b128 v[228:231], v40 offset:5120\n"
        "ds_read_b128 v[144:147], v41 offset:15360\n"
        "ds_read_b128 v[148:151], v42 offset:25600\n"
        "v_mfma_f32_32x32x16_bf16 a[144:159], v[108:111], v[172:175], a[144:159]\n"
        "s_waitcnt lgkmcnt(14)\n"
        "v_pk_add_f32 v[112:113], v[112:113], v[116:117]\n"
        "v_pk_add_f32 v[114:115], v[114:115], v[118:119]\n"
        "v_pk_add_f32 v[176:177], v[176:177], v[120:121]\n"
        "v_pk_add_f32 v[178:179], v[178:179], v[122:123]\n"
        "v_pk_add_f32 v[176:177], v[176:177], v[112:113]\n"
        "v_pk_add_f32 v[178:179], v[178:179], v[114:115]\n"
        "v_pk_add_f32 v[124:125], v[124:125], v[128:129]\n"
        "v_pk_add_f32 v[126:127], v[126:127], v[130:131]\n"
        "v_pk_add_f32 v[180:181], v[180:181], v[132:133]\n"
        "v_pk_add_f32 v[182:183], v[182:183], v[134:135]\n"
        "v_pk_add_f32 v[180:181], v[180:181], v[124:125]\n"
        "v_pk_add_f32 v[182:183], v[182:183], v[126:127]\n"
        "v_cvt_pk_bf16_f32 v80, v176, v177\n"
        "v_cvt_pk_bf16_f32 v81, v178, v179\n"
        "v_cvt_pk_bf16_f32 v82, v180, v181\n"
        "v_cvt_pk_bf16_f32 v83, v182, v183\n"
        "buffer_store_dwordx4 v[80:83], v43, s[36:39], s64 offen sc1\n"
        "s_waitcnt lgkmcnt(3)\n"
        "s_barrier\n"
        "ds_read_b128 v[112:115], v40 offset:6144\n"
        "ds_read_b128 v[116:119], v41 offset:16384\n"
        "ds_read_b128 v[120:123], v42 offset:26624\n"
        "ds_read_b128 v[124:127], v40 offset:7168\n"
        "ds_read_b128 v[128:131], v41 offset:17408\n"
        "ds_read_b128 v[132:135], v42 offset:27648\n"
        "v_pk_add_f32 v[136:137], v[136:137], v[140:141]\n"
        "v_pk_add_f32 v[138:139], v[138:139], v[142:143]\n"
        "v_pk_add_f32 v[184:185], v[184:185], v[64:65]\n"
        "v_pk_add_f32 v[186:187], v[186:187], v[66:67]\n"
        "v_pk_add_f32 v[184:185], v[184:185], v[136:137]\n"
        "v_pk_add_f32 v[186:187], v[186:187], v[138:139]\n"
        "v_pk_add_f32 v[68:69], v[68:69], v[72:73]\n"
        "v_pk_add_f32 v[70:71], v[70:71], v[74:75]\n"
        "v_pk_add_f32 v[188:189], v[188:189], v[76:77]\n"
        "v_pk_add_f32 v[190:191], v[190:191], v[78:79]\n"
        "v_pk_add_f32 v[188:189], v[188:189], v[68:69]\n"
        "v_pk_add_f32 v[190:191], v[190:191], v[70:71]\n"
        "v_cvt_pk_bf16_f32 v84, v184, v185\n"
        "v_cvt_pk_bf16_f32 v85, v186, v187\n"
        "v_cvt_pk_bf16_f32 v86, v188, v189\n"
        "v_cvt_pk_bf16_f32 v87, v190, v191\n"
        "buffer_store_dwordx4 v[84:87], v43, s[36:39], s67 offen sc1\n"
        "v_pk_add_f32 v[216:217], v[216:217], v[220:221]\n"
        "v_pk_add_f32 v[218:219], v[218:219], v[222:223]\n"
        "v_pk_add_f32 v[192:193], v[192:193], v[224:225]\n"
        "v_pk_add_f32 v[194:195], v[194:195], v[226:227]\n"
        "v_pk_add_f32 v[192:193], v[192:193], v[216:217]\n"
        "v_pk_add_f32 v[194:195], v[194:195], v[218:219]\n"
        "s_waitcnt lgkmcnt(6)\n"
        "v_pk_add_f32 v[228:229], v[228:229], v[144:145]\n"
        "v_pk_add_f32 v[230:231], v[230:231], v[146:147]\n"
        "v_pk_add_f32 v[196:197], v[196:197], v[148:149]\n"
        "v_pk_add_f32 v[198:199], v[198:199], v[150:151]\n"
        "v_pk_add_f32 v[196:197], v[196:197], v[228:229]\n"
        "v_pk_add_f32 v[198:199], v[198:199], v[230:231]\n"
        "v_cvt_pk_bf16_f32 v88, v192, v193\n"
        "v_cvt_pk_bf16_f32 v89, v194, v195\n"
        "v_cvt_pk_bf16_f32 v90, v196, v197\n"
        "v_cvt_pk_bf16_f32 v91, v198, v199\n"
        "buffer_store_dwordx4 v[88:91], v43, s[36:39], s68 offen sc1\n"
        "v_accvgpr_read_b32 v200, a96\n"
        "v_accvgpr_read_b32 v201, a97\n"
        "v_accvgpr_read_b32 v202, a98\n"
        "v_accvgpr_read_b32 v203, a99\n"
        "v_accvgpr_read_b32 v204, a112\n"
        "v_accvgpr_read_b32 v205, a113\n"
        "v_accvgpr_read_b32 v206, a114\n"
        "v_accvgpr_read_b32 v207, a115\n"
        "s_waitcnt lgkmcnt(3)\n"
        "v_pk_add_f32 v[112:113], v[112:113], v[116:117]\n"
        "v_pk_add_f32 v[114:115], v[114:115], v[118:119]\n"
        "v_pk_add_f32 v[200:201], v[200:201], v[120:121]\n"
        "v_pk_add_f32 v[202:203], v[202:203], v[122:123]\n"
        "v_pk_add_f32 v[200:201], v[200:201], v[112:113]\n"
        "v_pk_add_f32 v[202:203], v[202:203], v[114:115]\n"
        "s_waitcnt lgkmcnt(0)\n"
        "v_pk_add_f32 v[124:125], v[124:125], v[128:129]\n"
        "v_pk_add_f32 v[126:127], v[126:127], v[130:131]\n"
        "v_pk_add_f32 v[204:205], v[204:205], v[132:133]\n"
        "v_pk_add_f32 v[206:207], v[206:207], v[134:135]\n"
        "v_pk_add_f32 v[204:205], v[204:205], v[124:125]\n"
        "v_pk_add_f32 v[206:207], v[206:207], v[126:127]\n"
        "v_cvt_pk_bf16_f32 v92, v200, v201\n"
        "v_cvt_pk_bf16_f32 v93, v202, v203\n"
        "v_cvt_pk_bf16_f32 v94, v204, v205\n"
        "v_cvt_pk_bf16_f32 v95, v206, v207\n"
        "buffer_store_dwordx4 v[92:95], v43, s[36:39], s61 offen sc1\n"
        "s_cmp_lg_u32 s69, 0\n"
        "s_cbranch_scc1 9f\n"
        "s_memrealtime s[62:63]\n"
        "s_waitcnt vmcnt(4)\n"
        "v_smfmac_f32_16x16x64_bf16 v[236:239], a[240:243], a[160:167], v54\n"
        "ds_write_b128 v20, a[132:135] offset:8192\n"
        "ds_read_b128 v[112:115], v22\n"
        "v_accvgpr_read_b32 v208, a128\n"
        "v_accvgpr_read_b32 v209, a129\n"
        "v_accvgpr_read_b32 v210, a130\n"
        "v_accvgpr_read_b32 v211, a131\n"
        "v_smfmac_f32_16x16x64_bf16 v[236:239], a[240:243], a[168:175], v54\n"
        "ds_read_b128 v[116:119], v23\n"
        "ds_write_b128 v20, a[136:139] offset:18432\n"
        "v_accvgpr_read_b32 v212, a144\n"
        "v_accvgpr_read_b32 v213, a145\n"
        "v_accvgpr_read_b32 v214, a146\n"
        "v_accvgpr_read_b32 v215, a147\n"
        "v_smfmac_f32_16x16x64_bf16 v[236:239], a[244:247], a[176:183], v54\n"
        "ds_read_b128 v[120:123], v34 offset:0\n"
        "ds_read_b128 v[124:127], v34 offset:64\n"
        "v_readlane_b32 s76, v50, s60\n"
        "v_mov_b32 v16, 0x3f803f80\n"
        "s_nop 0\n"
        "v_subrev_u32 v50, s76, v50\n"
        "v_bfe_i32 v50, v50, 0, 24\n"
        "v_smfmac_f32_16x16x64_bf16 v[236:239], a[244:247], a[184:191], v54\n"
        "ds_write_b128 v20, a[140:143] offset:28672\n"
        "ds_read_b128 a[80:83], v1 offset:8192\n"
        "v_max_i32_dpp v50, v50, v50 quad_perm:[1,0,3,2] row_mask:0xf bank_mask:0xf\n"
        "s_nop 1\n"
        "v_max_i32_dpp v50, v50, v50 quad_perm:[2,3,0,1] row_mask:0xf bank_mask:0xf\n"
        "s_nop 0\n"
        "v_readfirstlane_b32 s85, v50\n"
        "v_mov_b32 v17, 0x3f803f80\n"
        "v_smfmac_f32_16x16x64_bf16 v[240:243], a[240:243], a[192:199], v54\n"
        "ds_read_b128 a[84:87], v1 offset:9216\n"
        "ds_write_b128 v20, a[148:151] offset:9216\n"
        "v_cmp_eq_u32 vcc, 0, v60\n"
        "v_mov_b32 v12, 0x3f80\n"
        "s_nop 0\n"
        "v_cndmask_b32 v12, 0, v12, vcc\n"
        "v_mov_b32 v13, 0\n"
        "v_smfmac_f32_16x16x64_bf16 v[240:243], a[240:243], a[200:207], v54\n"
        "ds_read_b128 a[88:91], v1 offset:10240\n"
        "ds_read_b128 a[92:95], v1 offset:11264\n"
        "v_mov_b32 v14, 0\n"
        "v_mov_b32 v15, 0\n"
        "s_min_i32 s85, s85, 400\n"
        "s_movk_i32 s75, 160\n"
        "v_smfmac_f32_16x16x64_bf16 v[240:243], a[244:247], a[208:215], v54\n"
        "ds_write_b128 v20, a[152:155] offset:19456\n"
        "ds_read_b128 a[96:99], v1 offset:12288\n"
        "v_lshlrev_b32 v64, 16, v248\n"
        "v_lshlrev_b32 v65, 16, v250\n"
        "v_lshlrev_b32 v66, 16, v252\n"
        "v_lshlrev_b32 v67, 16, v254\n"
        "v_smfmac_f32_16x16x64_bf16 v[240:243], a[244:247], a[216:223], v54\n"
        "ds_read_b128 a[100:103], v1 offset:13312\n"
        "ds_write_b128 v20, a[156:159] offset:29696\n"
        "v_and_b32 v68, 0xffff0000, v248\n"
        "v_and_b32 v69, 0xffff0000, v250\n"
        "v_and_b32 v70, 0xffff0000, v252\n"
        "v_and_b32 v71, 0xffff0000, v254\n"
        "v_smfmac_f32_16x16x64_bf16 v[244:247], a[240:243], a[224:231], v54\n"
        "ds_read_b128 a[104:107], v1 offset:14336\n"
        "ds_read_b128 a[108:111], v1 offset:15360\n"
        "v_lshlrev_b32 v72, 16, v249\n"
        "v_lshlrev_b32 v73, 16, v251\n"
        "v_lshlrev_b32 v74, 16, v253\n"
        "v_lshlrev_b32 v75, 16, v255\n"
        "v_smfmac_f32_16x16x64_bf16 v[244:247], a[240:243], a[232:239], v54\n"
        "ds_read_b128 v[144:147], v1 offset:18432\n"
        "ds_read_b128 v[148:151], v1 offset:19456\n"
        "v_and_b32 v76, 0xffff0000, v249\n"
        "v_and_b32 v77, 0xffff0000, v251\n"
        "v_and_b32 v78, 0xffff0000, v253\n"
        "v_and_b32 v79, 0xffff0000, v255\n"
        "v_lshlrev_b32 v108, 16, v10\n"
        "v_and_b32 v109, 0xffff0000, v10\n"
        "v_lshlrev_b32 v110, 16, v11\n"
        "v_and_b32 v111, 0xffff0000, v11\n"
        "s_lshr_b32 s76, s85, 3\n"
        "s_add_u32 s85, s85, s76\n"
        "s_add_u32 s85, s85, s65\n"
        "s_waitcnt lgkmcnt(0)\n"
        "s_add_u32 s85, s85, s62\n"
        "v_mfma_f32_16x16x32_bf16 v[128:131], v[112:115], v[120:123], 0\n"
        "v_mfma_f32_16x16x32_bf16 v[128:131], v[116:119], v[124:127], v[128:131]\n"
        "s_barrier\n"
        "35:\n"
        "s_memrealtime s[62:63]\n"
        "s_waitcnt lgkmcnt(0)\n"
        "s_sub_u32 s76, s62, s85\n"
        "s_cmp_lt_i32 s76, 0\n"
        "s_cbranch_scc1 35b\n"
        "buffer_load_dwordx4 v[184:187], v44, s[36:39], 0 offen sc1\n"
        "buffer_load_dwordx4 v[188:191], v49, s[36:39], 0 offen sc1\n"
        "buffer_load_dwordx4 v[192:195], v44, s[36:39], s80 offen sc1\n"
        "buffer_load_dwordx4 v[196:199], v49, s[36:39], s80 offen sc1\n"
        "buffer_load_dwordx4 v[200:203], v44, s[36:39], s81 offen sc1\n"
        "buffer_load_dwordx4 v[204:207], v49, s[36:39], s81 offen sc1\n"
        "s_cmp_eq_u32 s86, 0\n"
        "s_cbranch_scc1 28f\n"
        "29:\n"
        "s_add_u32 s75, s75, 32\n"
        "v_cmp_lt_u32 vcc, s75, v38\n"
        "s_and_b32 s74, s86, 3\n"
        "s_cbranch_scc0 41f\n"
        "v_smfmac_f32_16x16x64_bf16 v[236:239], a[240:243], a[0:7], v54\n"
        "v_smfmac_f32_16x16x64_bf16 v[236:239], a[240:243], a[8:15], v54\n"
        "41:\n"
        "s_and_b32 s74, s86, 12\n"
        "s_cbranch_scc0 42f\n"
        "v_smfmac_f32_16x16x64_bf16 v[236:239], a[244:247], a[16:23], v54\n"
        "v_smfmac_f32_16x16x64_bf16 v[236:239], a[244:247], a[24:31], v54\n"
        "42:\n"
        "s_and_b32 s74, s86, 48\n"
        "s_cbranch_scc0 43f\n"
        "v_smfmac_f32_16x16x64_bf16 v[240:243], a[240:243], a[32:39], v54\n"
        "v_smfmac_f32_16x16x64_bf16 v[240:243], a[240:243], a[40:47], v54\n"
        "43:\n"
        "s_and_b32 s74, s86, 192\n"
        "s_cbranch_scc0 44f\n"
        "v_smfmac_f32_16x16x64_bf16 v[240:243], a[244:247], a[48:55], v54\n"
        "v_smfmac_f32_16x16x64_bf16 v[240:243], a[244:247], a[56:63], v54\n"
        "44:\n"
        "s_and_b32 s74, s86, 768\n"
        "s_cbranch_scc0 45f\n"
        "v_smfmac_f32_16x16x64_bf16 v[244:247], a[240:243], a[64:71], v54\n"
        "v_smfmac_f32_16x16x64_bf16 v[244:247], a[240:243], a[72:79], v54\n"
        "45:\n"
        "s_lshr_b64 s[86:87], vcc, s72\n"
        "s_and_b32 s86, s86, 0x3ff\n"
        "s_cbranch_scc1 22f\n"
        "28:\n"
        "v_cvt_pk_bf16_f32 v236, v236, v237\n"
        "v_cvt_pk_bf16_f32 v237, v238, v239\n"
        "s_nop 0\n"
        "v_cvt_pk_bf16_f32 v240, v240, v241\n"
        "v_cvt_pk_bf16_f32 v241, v242, v243\n"
        "v_cvt_pk_bf16_f32 v244, v244, v245\n"
        "v_cvt_pk_bf16_f32 v245, v246, v247\n"
        "ds_write_b64 v55, v[236:237] offset:0\n"
        "ds_write_b64 v55, v[240:241] offset:576\n"
        "ds_write_b64 v55, v[244:245] offset:1152\n"
        "ds_read_b128 v[152:155], v3 offset:0\n"
        "ds_read_b128 v[156:159], v3 offset:64\n"
        "ds_read_b128 v[136:139], v40 offset:8192\n"
        "ds_read_b128 v[140:143], v41 offset:18432\n"
        "ds_read_b128 v[216:219], v42 offset:28672\n"
        "ds_read_b128 v[220:223], v40 offset:9216\n"
        "ds_read_b128 v[224:227], v41 offset:19456\n"
        "ds_read_b128 v[228:231], v42 offset:29696\n"
        "v_exp_f32 v128, v128\n"
        "v_exp_f32 v129, v129\n"
        "v_exp_f32 v130, v130\n"
        "v_exp_f32 v131, v131\n"
        "v_cvt_pk_bf16_f32 v128, v128, v129\n"
        "v_cvt_pk_bf16_f32 v129, v130, v131\n"
        "s_waitcnt lgkmcnt(7)\n"
        "v_mfma_f32_16x16x32_bf16 v[160:163], v[152:155], a[80:83], 0\n"
        "ds_read_b128 a[112:115], v1 offset:0\n"
        "v_mfma_f32_16x16x32_bf16 v[164:167], v[152:155], a[88:91], 0\n"
        "ds_read_b128 a[116:119], v1 offset:1024\n"
        "v_mfma_f32_16x16x32_bf16 v[168:171], v[152:155], a[96:99], 0\n"
        "ds_read_b128 a[120:123], v1 offset:2048\n"
        "v_mfma_f32_16x16x32_bf16 v[172:175], v[152:155], a[104:107], 0\n"
        "ds_read_b128 a[124:127], v1 offset:3072\n"
        "s_waitcnt lgkmcnt(10)\n"
        "v_mfma_f32_16x16x32_bf16 v[160:163], v[156:159], a[84:87], v[160:163]\n"
        "ds_read_b128 a[128:131], v1 offset:4096\n"
        "v_mfma_f32_16x16x32_bf16 v[164:167], v[156:159], a[92:95], v[164:167]\n"
        "ds_read_b128 a[132:135], v1 offset:5120\n"
        "v_mfma_f32_16x16x32_bf16 v[168:171], v[156:159], a[100:103], v[168:171]\n"
        "ds_read_b128 a[136:139], v1 offset:6144\n"
        "v_mfma_f32_16x16x32_bf16 v[172:175], v[156:159], a[108:111], v[172:175]\n"
        "ds_read_b128 a[140:143], v1 offset:7168\n"
        "v_mfma_f32_16x16x16_bf16 v[132:135], v[128:129], v[16:17], 0\n"
        "v_mfma_f32_16x16x16_bf16 v[80:83], v[128:129], v[144:145], 0\n"
        "v_mfma_f32_16x16x16_bf16 v[84:87], v[128:129], v[146:147], 0\n"
        "v_mfma_f32_16x16x16_bf16 v[88:91], v[128:129], v[148:149], 0\n"
        "v_mfma_f32_16x16x16_bf16 v[92:95], v[128:129], v[150:151], 0\n"
        "s_waitcnt lgkmcnt(11)\n"
        "v_pk_add_f32 v[136:137], v[136:137], v[140:141]\n"
        "v_pk_add_f32 v[138:139], v[138:139], v[142:143]\n"
        "v_pk_add_f32 v[208:209], v[208:209], v[216:217]\n"
        "v_pk_add_f32 v[210:211], v[210:211], v[218:219]\n"
        "v_pk_add_f32 v[208:209], v[208:209], v[136:137]\n"
        "v_pk_add_f32 v[210:211], v[210:211], v[138:139]\n"
        "s_waitcnt lgkmcnt(8)\n"
        "v_pk_add_f32 v[220:221], v[220:221], v[224:225]\n"
        "v_pk_add_f32 v[222:223], v[222:223], v[226:227]\n"
        "v_pk_add_f32 v[212:213], v[212:213], v[228:229]\n"
        "v_pk_add_f32 v[214:215], v[214:215], v[230:231]\n"
        "v_pk_add_f32 v[212:213], v[212:213], v[220:221]\n"
        "v_pk_add_f32 v[214:215], v[214:215], v[222:223]\n"
        "s_cmp_eq_u32 s73, 0\n"
        "s_cbranch_scc1 31f\n"
        "v_pk_fma_f32 v[64:65], v[160:161], v[108:109], v[64:65]\n"
        "v_pk_fma_f32 v[66:67], v[162:163], v[110:111], v[66:67]\n"
        "v_pk_fma_f32 v[68:69], v[164:165], v[108:109], v[68:69]\n"
        "v_pk_fma_f32 v[70:71], v[166:167], v[110:111], v[70:71]\n"
        "v_pk_fma_f32 v[72:73], v[168:169], v[108:109], v[72:73]\n"
        "v_pk_fma_f32 v[74:75], v[170:171], v[110:111], v[74:75]\n"
        "v_pk_fma_f32 v[76:77], v[172:173], v[108:109], v[76:77]\n"
        "v_pk_fma_f32 v[78:79], v[174:175], v[110:111], v[78:79]\n"
        "s_waitcnt vmcnt(0)\n"
        "v_mfma_f32_4x4x4_16b_bf16 v[216:219], a[252:253], v[184:185], v[208:211]\n"
        "v_max3_u32 v38, v184, v185, v186\n"
        "v_mfma_f32_4x4x4_16b_bf16 v[220:223], a[252:253], v[186:187], v[212:215]\n"
        "v_max3_u32 v38, v38, v187, v188\n"
        "v_mfma_f32_4x4x4_16b_bf16 v[224:227], a[252:253], v[188:189], v[176:179]\n"
        "v_max3_u32 v38, v38, v189, v190\n"
        "v_mfma_f32_4x4x4_16b_bf16 v[228:231], a[252:253], v[190:191], v[180:183]\n"
        "v_max3_u32 v38, v38, v191, v192\n"
        "v_mfma_f32_4x4x4_16b_bf16 v[216:219], a[252:253], v[192:193], v[216:219]\n"
        "v_max3_u32 v38, v38, v193, v194\n"
        "v_mfma_f32_4x4x4_16b_bf16 v[220:223], a[252:253], v[194:195], v[220:223]\n"
        "v_max3_u32 v38, v38, v195, v196\n"
        "v_mfma_f32_4x4x4_16b_bf16 v[224:227], a[252:253], v[196:197], v[224:227]\n"
        "v_max3_u32 v38, v38, v197, v198\n"
        "v_mfma_f32_4x4x4_16b_bf16 v[228:231], a[252:253], v[198:199], v[228:231]\n"
        "v_max3_u32 v38, v38, v199, v200\n"
        "v_mfma_f32_4x4x4_16b_bf16 v[216:219], a[252:253], v[200:201], v[216:219]\n"
        "v_max3_u32 v38, v38, v201, v202\n"
        "v_mfma_f32_4x4x4_16b_bf16 v[220:223], a[252:253], v[202:203], v[220:223]\n"
        "v_max3_u32 v38, v38, v203, v204\n"
        "v_mfma_f32_4x4x4_16b_bf16 v[224:227], a[252:253], v[204:205], v[224:227]\n"
        "v_max3_u32 v38, v38, v205, v206\n"
        "v_mfma_f32_4x4x4_16b_bf16 v[228:231], a[252:253], v[206:207], v[228:231]\n"
        "v_max_u32 v38, v38, v207\n"
        "v_cmp_eq_u32 vcc, -1, v38\n"
        "s_cbranch_vccnz 25f\n"
        "26:\n"
        "v_cvt_pk_bf16_f32 v216, v216, v217\n"
        "v_cvt_pk_bf16_f32 v217, v218, v219\n"
        "v_cvt_pk_bf16_f32 v220, v220, v221\n"
        "v_cvt_pk_bf16_f32 v221, v222, v223\n"
        "v_cvt_pk_bf16_f32 v224, v224, v225\n"
        "v_cvt_pk_bf16_f32 v225, v226, v227\n"
        "v_cvt_pk_bf16_f32 v228, v228, v229\n"
        "v_cvt_pk_bf16_f32 v229, v230, v231\n"
        "ds_write_b64 v4, v[216:217] offset:0\n"
        "ds_write_b64 v4, v[220:221] offset:3072\n"
        "ds_write_b64 v2, v[224:225] offset:64\n"
        "ds_write_b64 v2, v[228:229] offset:3136\n"
        "s_waitcnt lgkmcnt(0)\n"
        "s_barrier\n"
        "ds_read_b64_tr_b16 v[112:113], v5 offset:0\n"
        "ds_read_b64_tr_b16 v[114:115], v5 offset:1536\n"
        "ds_read_b64_tr_b16 v[116:117], v5 offset:3072\n"
        "ds_read_b64_tr_b16 v[118:119], v5 offset:4608\n"
        "s_waitcnt lgkmcnt(2)\n"
        "v_mfma_f32_16x16x32_bf16 v[132:135], v[112:115], v[12:15], v[132:135]\n"
        "v_mfma_f32_16x16x32_bf16 v[80:83], v[112:115], a[112:115], v[80:83]\n"
        "v_mfma_f32_16x16x32_bf16 v[84:87], v[112:115], a[120:123], v[84:87]\n"
        "v_mfma_f32_16x16x32_bf16 v[88:91], v[112:115], a[128:131], v[88:91]\n"
        "v_mfma_f32_16x16x32_bf16 v[92:95], v[112:115], a[136:139], v[92:95]\n"
        "s_waitcnt lgkmcnt(0)\n"
        "v_mfma_f32_16x16x32_bf16 v[80:83], v[116:119], a[116:119], v[80:83]\n"
        "v_mfma_f32_16x16x32_bf16 v[84:87], v[116:119], a[124:127], v[84:87]\n"
        "v_mfma_f32_16x16x32_bf16 v[88:91], v[116:119], a[132:135], v[88:91]\n"
        "v_mfma_f32_16x16x32_bf16 v[92:95], v[116:119], a[140:143], v[92:95]\n"
        "v_rcp_f32 v132, v132\n"
        "v_rcp_f32 v133, v133\n"
        "v_rcp_f32 v134, v134\n"
        "v_rcp_f32 v135, v135\n"
        "s_nop 0\n"
        "v_pk_fma_f32 v[64:65], v[80:81], v[132:133], v[64:65]\n"
        "v_pk_fma_f32 v[68:69], v[84:85], v[132:133], v[68:69]\n"
        "v_pk_fma_f32 v[72:73], v[88:89], v[132:133], v[72:73]\n"
        "v_pk_fma_f32 v[76:77], v[92:93], v[132:133], v[76:77]\n"
        "v_cvt_pk_bf16_f32 v248, v64, v68\n"
        "v_cvt_pk_bf16_f32 v249, v72, v76\n"
        "v_cvt_pk_bf16_f32 v250, v65, v69\n"
        "v_cvt_pk_bf16_f32 v251, v73, v77\n"
        "buffer_store_dwordx2 v[248:249], v46, s[48:51], 0 offen offset:0 nt\n"
        "buffer_store_dwordx2 v[250:251], v46, s[48:51], 0 offen offset:128 nt\n"
        "v_pk_fma_f32 v[66:67], v[82:83], v[134:135], v[66:67]\n"
        "v_pk_fma_f32 v[70:71], v[86:87], v[134:135], v[70:71]\n"
        "v_pk_fma_f32 v[74:75], v[90:91], v[134:135], v[74:75]\n"
        "v_pk_fma_f32 v[78:79], v[94:95], v[134:135], v[78:79]\n"
        "v_cvt_pk_bf16_f32 v252, v66, v70\n"
        "v_cvt_pk_bf16_f32 v253, v74, v78\n"
        "v_cvt_pk_bf16_f32 v254, v67, v71\n"
        "v_cvt_pk_bf16_f32 v255, v75, v79\n"
        "buffer_store_dwordx2 v[252:253], v47, s[48:51], 0 offen offset:256 nt\n"
        "buffer_store_dwordx2 v[254:255], v47, s[48:51], 0 offen offset:384 nt\n"
        "s_cmp_eq_u32 s58, 248\n"
        "s_cbranch_scc1 24f\n"
        "s_endpgm\n"
        "24:\n"
        "v_mov_b32 v80, s52\n"
        "v_mov_b32 v81, s53\n"
        "v_mov_b32 v82, 1312384\n"
        "v_bfrev_b32 v83, 1\n"
        "v_cmp_eq_u32 vcc, 0, v8\n"
        "s_nop 1\n"
        "v_cndmask_b32 v82, v83, v82, vcc\n"
        "buffer_store_dwordx2 v[80:81], v82, s[36:39], 0 offen sc1\n"
        "s_branch 9f\n"
        "70:\n"
        "v_mad_u32_u16 v61, v224, s70, v37 op_sel:[0,0,0,0]\n"
        "buffer_load_dwordx4 a[0:3], v61, s[40:43], 0 offen sc1\n"
        "v_mad_u32_u16 v62, v224, s70, v37 op_sel:[1,0,0,0]\n"
        "buffer_load_dwordx4 a[4:7], v62, s[40:43], 0 offen sc1\n"
        "v_mad_u32_u16 v61, v225, s70, v37 op_sel:[0,0,0,0]\n"
        "buffer_load_dwordx4 a[8:11], v61, s[40:43], 0 offen sc1\n"
        "v_mad_u32_u16 v62, v225, s70, v37 op_sel:[1,0,0,0]\n"
        "buffer_load_dwordx4 a[12:15], v62, s[40:43], 0 offen sc1\n"
        "s_branch 80b\n"
        "71:\n"
        "v_mad_u32_u16 v61, v226, s70, v37 op_sel:[0,0,0,0]\n"
        "buffer_load_dwordx4 a[16:19], v61, s[40:43], 0 offen sc1\n"
        "v_mad_u32_u16 v62, v226, s70, v37 op_sel:[1,0,0,0]\n"
        "buffer_load_dwordx4 a[20:23], v62, s[40:43], 0 offen sc1\n"
        "v_mad_u32_u16 v61, v227, s70, v37 op_sel:[0,0,0,0]\n"
        "buffer_load_dwordx4 a[24:27], v61, s[40:43], 0 offen sc1\n"
        "v_mad_u32_u16 v62, v227, s70, v37 op_sel:[1,0,0,0]\n"
        "buffer_load_dwordx4 a[28:31], v62, s[40:43], 0 offen sc1\n"
        "s_branch 81b\n"
        "72:\n"
        "v_mad_u32_u16 v61, v228, s70, v37 op_sel:[0,0,0,0]\n"
        "buffer_load_dwordx4 a[32:35], v61, s[40:43], 0 offen sc1\n"
        "v_mad_u32_u16 v62, v228, s70, v37 op_sel:[1,0,0,0]\n"
        "buffer_load_dwordx4 a[36:39], v62, s[40:43], 0 offen sc1\n"
        "v_mad_u32_u16 v61, v229, s70, v37 op_sel:[0,0,0,0]\n"
        "buffer_load_dwordx4 a[40:43], v61, s[40:43], 0 offen sc1\n"
        "v_mad_u32_u16 v62, v229, s70, v37 op_sel:[1,0,0,0]\n"
        "buffer_load_dwordx4 a[44:47], v62, s[40:43], 0 offen sc1\n"
        "s_branch 82b\n"
        "73:\n"
        "v_mad_u32_u16 v61, v230, s70, v37 op_sel:[0,0,0,0]\n"
        "buffer_load_dwordx4 a[48:51], v61, s[40:43], 0 offen sc1\n"
        "v_mad_u32_u16 v62, v230, s70, v37 op_sel:[1,0,0,0]\n"
        "buffer_load_dwordx4 a[52:55], v62, s[40:43], 0 offen sc1\n"
        "v_mad_u32_u16 v61, v231, s70, v37 op_sel:[0,0,0,0]\n"
        "buffer_load_dwordx4 a[56:59], v61, s[40:43], 0 offen sc1\n"
        "v_mad_u32_u16 v62, v231, s70, v37 op_sel:[1,0,0,0]\n"
        "buffer_load_dwordx4 a[60:63], v62, s[40:43], 0 offen sc1\n"
        "s_branch 83b\n"
        "74:\n"
        "v_mad_u32_u16 v61, v234, s70, v37 op_sel:[0,0,0,0]\n"
        "buffer_load_dwordx4 a[64:67], v61, s[40:43], 0 offen sc1\n"
        "v_mad_u32_u16 v62, v234, s70, v37 op_sel:[1,0,0,0]\n"
        "buffer_load_dwordx4 a[68:71], v62, s[40:43], 0 offen sc1\n"
        "v_mad_u32_u16 v61, v235, s70, v37 op_sel:[0,0,0,0]\n"
        "buffer_load_dwordx4 a[72:75], v61, s[40:43], 0 offen sc1\n"
        "v_mad_u32_u16 v62, v235, s70, v37 op_sel:[1,0,0,0]\n"
        "buffer_load_dwordx4 a[76:79], v62, s[40:43], 0 offen sc1\n"
        "s_branch 84b\n"
        "22:\n"
        "ds_read_b64 v[24:25], v36 offset:128\n"
        "ds_read_b64 v[26:27], v36 offset:672\n"
        "ds_read_b64 v[28:29], v36 offset:1216\n"
        "ds_read_b64 v[30:31], v36 offset:1760\n"
        "ds_read_b64 v[32:33], v36 offset:2304\n"
        "v_add_u32 v36, 32, v36\n"
        "s_waitcnt lgkmcnt(0)\n"
        "s_and_b32 s74, s86, 3\n"
        "s_cbranch_scc0 46f\n"
        "v_mad_u32_u16 v232, v24, s70, v37 op_sel:[0,0,0,0]\n"
        "buffer_load_dwordx4 a[0:3], v232, s[40:43], 0 offen sc1\n"
        "v_mad_u32_u16 v233, v24, s70, v37 op_sel:[1,0,0,0]\n"
        "buffer_load_dwordx4 a[4:7], v233, s[40:43], 0 offen sc1\n"
        "v_mad_u32_u16 v232, v25, s70, v37 op_sel:[0,0,0,0]\n"
        "buffer_load_dwordx4 a[8:11], v232, s[40:43], 0 offen sc1\n"
        "v_mad_u32_u16 v233, v25, s70, v37 op_sel:[1,0,0,0]\n"
        "buffer_load_dwordx4 a[12:15], v233, s[40:43], 0 offen sc1\n"
        "46:\n"
        "s_and_b32 s74, s86, 12\n"
        "s_cbranch_scc0 47f\n"
        "v_mad_u32_u16 v232, v26, s70, v37 op_sel:[0,0,0,0]\n"
        "buffer_load_dwordx4 a[16:19], v232, s[40:43], 0 offen sc1\n"
        "v_mad_u32_u16 v233, v26, s70, v37 op_sel:[1,0,0,0]\n"
        "buffer_load_dwordx4 a[20:23], v233, s[40:43], 0 offen sc1\n"
        "v_mad_u32_u16 v232, v27, s70, v37 op_sel:[0,0,0,0]\n"
        "buffer_load_dwordx4 a[24:27], v232, s[40:43], 0 offen sc1\n"
        "v_mad_u32_u16 v233, v27, s70, v37 op_sel:[1,0,0,0]\n"
        "buffer_load_dwordx4 a[28:31], v233, s[40:43], 0 offen sc1\n"
        "47:\n"
        "s_and_b32 s74, s86, 48\n"
        "s_cbranch_scc0 48f\n"
        "v_mad_u32_u16 v232, v28, s70, v37 op_sel:[0,0,0,0]\n"
        "buffer_load_dwordx4 a[32:35], v232, s[40:43], 0 offen sc1\n"
        "v_mad_u32_u16 v233, v28, s70, v37 op_sel:[1,0,0,0]\n"
        "buffer_load_dwordx4 a[36:39], v233, s[40:43], 0 offen sc1\n"
        "v_mad_u32_u16 v232, v29, s70, v37 op_sel:[0,0,0,0]\n"
        "buffer_load_dwordx4 a[40:43], v232, s[40:43], 0 offen sc1\n"
        "v_mad_u32_u16 v233, v29, s70, v37 op_sel:[1,0,0,0]\n"
        "buffer_load_dwordx4 a[44:47], v233, s[40:43], 0 offen sc1\n"
        "48:\n"
        "s_and_b32 s74, s86, 192\n"
        "s_cbranch_scc0 49f\n"
        "v_mad_u32_u16 v232, v30, s70, v37 op_sel:[0,0,0,0]\n"
        "buffer_load_dwordx4 a[48:51], v232, s[40:43], 0 offen sc1\n"
        "v_mad_u32_u16 v233, v30, s70, v37 op_sel:[1,0,0,0]\n"
        "buffer_load_dwordx4 a[52:55], v233, s[40:43], 0 offen sc1\n"
        "v_mad_u32_u16 v232, v31, s70, v37 op_sel:[0,0,0,0]\n"
        "buffer_load_dwordx4 a[56:59], v232, s[40:43], 0 offen sc1\n"
        "v_mad_u32_u16 v233, v31, s70, v37 op_sel:[1,0,0,0]\n"
        "buffer_load_dwordx4 a[60:63], v233, s[40:43], 0 offen sc1\n"
        "49:\n"
        "s_and_b32 s74, s86, 768\n"
        "s_cbranch_scc0 50f\n"
        "v_mad_u32_u16 v232, v32, s70, v37 op_sel:[0,0,0,0]\n"
        "buffer_load_dwordx4 a[64:67], v232, s[40:43], 0 offen sc1\n"
        "v_mad_u32_u16 v233, v32, s70, v37 op_sel:[1,0,0,0]\n"
        "buffer_load_dwordx4 a[68:71], v233, s[40:43], 0 offen sc1\n"
        "v_mad_u32_u16 v232, v33, s70, v37 op_sel:[0,0,0,0]\n"
        "buffer_load_dwordx4 a[72:75], v232, s[40:43], 0 offen sc1\n"
        "v_mad_u32_u16 v233, v33, s70, v37 op_sel:[1,0,0,0]\n"
        "buffer_load_dwordx4 a[76:79], v233, s[40:43], 0 offen sc1\n"
        "50:\n"
        "s_waitcnt vmcnt(0)\n"
        "s_branch 29b\n"
        "25:\n"
        "s_mov_b32 s71, 0x100000\n"
        "1:\n"
        "s_sleep 2\n"
        "buffer_load_dwordx4 v[184:187], v44, s[36:39], 0 offen sc1\n"
        "buffer_load_dwordx4 v[188:191], v49, s[36:39], 0 offen sc1\n"
        "buffer_load_dwordx4 v[192:195], v44, s[36:39], s80 offen sc1\n"
        "buffer_load_dwordx4 v[196:199], v49, s[36:39], s80 offen sc1\n"
        "buffer_load_dwordx4 v[200:203], v44, s[36:39], s81 offen sc1\n"
        "buffer_load_dwordx4 v[204:207], v49, s[36:39], s81 offen sc1\n"
        "s_sub_u32 s71, s71, 1\n"
        "s_waitcnt vmcnt(0)\n"
        "s_cbranch_scc1 37f\n"
        "v_max3_u32 v38, v184, v185, v186\n"
        "v_max3_u32 v38, v38, v187, v188\n"
        "v_max3_u32 v38, v38, v189, v190\n"
        "v_max3_u32 v38, v38, v191, v192\n"
        "v_max3_u32 v38, v38, v193, v194\n"
        "v_max3_u32 v38, v38, v195, v196\n"
        "v_max3_u32 v38, v38, v197, v198\n"
        "v_max3_u32 v38, v38, v199, v200\n"
        "v_max3_u32 v38, v38, v201, v202\n"
        "v_max3_u32 v38, v38, v203, v204\n"
        "v_max3_u32 v38, v38, v205, v206\n"
        "v_max_u32 v38, v38, v207\n"
        "v_cmp_eq_u32 vcc, -1, v38\n"
        "s_cbranch_vccnz 1b\n"
        "37:\n"
        "v_mfma_f32_4x4x4_16b_bf16 v[216:219], a[252:253], v[184:185], v[208:211]\n"
        "v_mfma_f32_4x4x4_16b_bf16 v[220:223], a[252:253], v[186:187], v[212:215]\n"
        "v_mfma_f32_4x4x4_16b_bf16 v[224:227], a[252:253], v[188:189], v[176:179]\n"
        "v_mfma_f32_4x4x4_16b_bf16 v[228:231], a[252:253], v[190:191], v[180:183]\n"
        "v_mfma_f32_4x4x4_16b_bf16 v[216:219], a[252:253], v[192:193], v[216:219]\n"
        "v_mfma_f32_4x4x4_16b_bf16 v[220:223], a[252:253], v[194:195], v[220:223]\n"
        "v_mfma_f32_4x4x4_16b_bf16 v[224:227], a[252:253], v[196:197], v[224:227]\n"
        "v_mfma_f32_4x4x4_16b_bf16 v[228:231], a[252:253], v[198:199], v[228:231]\n"
        "v_mfma_f32_4x4x4_16b_bf16 v[216:219], a[252:253], v[200:201], v[216:219]\n"
        "v_mfma_f32_4x4x4_16b_bf16 v[220:223], a[252:253], v[202:203], v[220:223]\n"
        "v_mfma_f32_4x4x4_16b_bf16 v[224:227], a[252:253], v[204:205], v[224:227]\n"
        "v_mfma_f32_4x4x4_16b_bf16 v[228:231], a[252:253], v[206:207], v[228:231]\n"
        "s_nop 4\n"
        "s_branch 26b\n"
        "7:\n"
        "s_mov_b32 s71, 0x100000\n"
        "4:\n"
        "s_sleep 4\n"
        "buffer_load_dwordx2 v[208:209], v32, s[36:39], 0 offen sc1\n"
        "s_sub_u32 s71, s71, 1\n"
        "s_waitcnt vmcnt(0)\n"
        "s_cbranch_scc1 8b\n"
        "v_lshrrev_b64 v[210:211], 24, v[208:209]\n"
        "v_cmp_ne_u64 vcc, s[54:55], v[210:211]\n"
        "s_cbranch_vccnz 4b\n"
        "s_branch 8b\n"
        "2:\n"
        "s_memrealtime s[76:77]\n"
        "s_mov_b32 s71, 0x100000\n"
        "32:\n"
        "s_sleep 4\n"
        "buffer_load_dwordx2 v[50:51], v45, s[36:39], 0 offen sc1\n"
        "s_sub_u32 s71, s71, 1\n"
        "s_waitcnt vmcnt(0) lgkmcnt(0)\n"
        "s_cbranch_scc1 3b\n"
        "v_lshrrev_b64 v[52:53], 24, v[50:51]\n"
        "v_cmp_ne_u64 vcc, s[54:55], v[52:53]\n"
        "s_cbranch_vccnz 32b\n"
        "s_memrealtime s[78:79]\n"
        "s_add_u32 s65, s65, s76\n"
        "s_waitcnt lgkmcnt(0)\n"
        "s_sub_u32 s65, s65, s78\n"
        "s_branch 3b\n"
        "5:\n"
        "s_waitcnt lgkmcnt(0)\n"
        "v_sub_u32_sdwa v208, s63, v200 dst_sel:DWORD dst_unused:UNUSED_PAD src0_sel:DWORD src1_sel:WORD_0\n"
        "v_sub_u32_sdwa v209, s63, v201 dst_sel:DWORD dst_unused:UNUSED_PAD src0_sel:DWORD src1_sel:WORD_0\n"
        "v_sub_u32_sdwa v210, s63, v202 dst_sel:DWORD dst_unused:UNUSED_PAD src0_sel:DWORD src1_sel:WORD_0\n"
        "v_sub_u32_sdwa v211, s63, v203 dst_sel:DWORD dst_unused:UNUSED_PAD src0_sel:DWORD src1_sel:WORD_0\n"
        "v_sub_u32_sdwa v212, s63, v204 dst_sel:DWORD dst_unused:UNUSED_PAD src0_sel:DWORD src1_sel:WORD_0\n"
        "v_sub_u32_sdwa v213, s63, v205 dst_sel:DWORD dst_unused:UNUSED_PAD src0_sel:DWORD src1_sel:WORD_0\n"
        "v_sub_u32_sdwa v214, s63, v206 dst_sel:DWORD dst_unused:UNUSED_PAD src0_sel:DWORD src1_sel:WORD_0\n"
        "v_sub_u32_sdwa v215, s63, v207 dst_sel:DWORD dst_unused:UNUSED_PAD src0_sel:DWORD src1_sel:WORD_0\n"
        "ds_add_rtn_u32 v216, v208, v61\n"
        "ds_add_rtn_u32 v217, v209, v61\n"
        "ds_add_rtn_u32 v218, v210, v61\n"
        "ds_add_rtn_u32 v219, v211, v61\n"
        "ds_add_rtn_u32 v220, v212, v61\n"
        "ds_add_rtn_u32 v221, v213, v61\n"
        "ds_add_rtn_u32 v222, v214, v61\n"
        "ds_add_rtn_u32 v223, v215, v61\n"
        "s_waitcnt lgkmcnt(0)\n"
        "ds_write_b16_d16_hi v216, v200 offset:16384\n"
        "ds_write_b16_d16_hi v217, v201 offset:16384\n"
        "ds_write_b16_d16_hi v218, v202 offset:16384\n"
        "ds_write_b16_d16_hi v219, v203 offset:16384\n"
        "ds_write_b16_d16_hi v220, v204 offset:16384\n"
        "ds_write_b16_d16_hi v221, v205 offset:16384\n"
        "ds_write_b16_d16_hi v222, v206 offset:16384\n"
        "ds_write_b16_d16_hi v223, v207 offset:16384\n"
        "s_waitcnt lgkmcnt(0)\n"
        "s_branch 6b\n"
        "107:\n"
        "s_mov_b32 s71, 0x100000\n"
        "104:\n"
        "s_sleep 4\n"
        "buffer_load_dwordx2 v[208:209], v32, s[36:39], 0 offen sc1\n"
        "s_sub_u32 s71, s71, 1\n"
        "s_waitcnt vmcnt(0)\n"
        "s_cbranch_scc1 108b\n"
        "v_lshrrev_b64 v[210:211], 24, v[208:209]\n"
        "v_cmp_ne_u64 vcc, s[54:55], v[210:211]\n"
        "s_cbranch_vccnz 104b\n"
        "s_branch 108b\n"
        "105:\n"
        "s_waitcnt lgkmcnt(0)\n"
        "v_sub_u32_sdwa v208, s63, v200 dst_sel:DWORD dst_unused:UNUSED_PAD src0_sel:DWORD src1_sel:WORD_0\n"
        "v_sub_u32_sdwa v209, s63, v201 dst_sel:DWORD dst_unused:UNUSED_PAD src0_sel:DWORD src1_sel:WORD_0\n"
        "v_sub_u32_sdwa v210, s63, v202 dst_sel:DWORD dst_unused:UNUSED_PAD src0_sel:DWORD src1_sel:WORD_0\n"
        "v_sub_u32_sdwa v211, s63, v203 dst_sel:DWORD dst_unused:UNUSED_PAD src0_sel:DWORD src1_sel:WORD_0\n"
        "v_sub_u32_sdwa v212, s63, v204 dst_sel:DWORD dst_unused:UNUSED_PAD src0_sel:DWORD src1_sel:WORD_0\n"
        "v_sub_u32_sdwa v213, s63, v205 dst_sel:DWORD dst_unused:UNUSED_PAD src0_sel:DWORD src1_sel:WORD_0\n"
        "v_sub_u32_sdwa v214, s63, v206 dst_sel:DWORD dst_unused:UNUSED_PAD src0_sel:DWORD src1_sel:WORD_0\n"
        "v_sub_u32_sdwa v215, s63, v207 dst_sel:DWORD dst_unused:UNUSED_PAD src0_sel:DWORD src1_sel:WORD_0\n"
        "ds_add_rtn_u32 v216, v208, v61\n"
        "ds_add_rtn_u32 v217, v209, v61\n"
        "ds_add_rtn_u32 v218, v210, v61\n"
        "ds_add_rtn_u32 v219, v211, v61\n"
        "ds_add_rtn_u32 v220, v212, v61\n"
        "ds_add_rtn_u32 v221, v213, v61\n"
        "ds_add_rtn_u32 v222, v214, v61\n"
        "ds_add_rtn_u32 v223, v215, v61\n"
        "s_waitcnt lgkmcnt(0)\n"
        "ds_write_b16_d16_hi v216, v200 offset:16384\n"
        "ds_write_b16_d16_hi v217, v201 offset:16384\n"
        "ds_write_b16_d16_hi v218, v202 offset:16384\n"
        "ds_write_b16_d16_hi v219, v203 offset:16384\n"
        "ds_write_b16_d16_hi v220, v204 offset:16384\n"
        "ds_write_b16_d16_hi v221, v205 offset:16384\n"
        "ds_write_b16_d16_hi v222, v206 offset:16384\n"
        "ds_write_b16_d16_hi v223, v207 offset:16384\n"
        "s_waitcnt lgkmcnt(0)\n"
        "s_branch 106b\n"
        "39:\n"
        "s_add_u32 s82, s74, 3\n"
        "s_waitcnt vmcnt(0) lgkmcnt(0)\n"
        "s_branch 9f\n"
        "30:\n"
        "s_mov_b32 s82, 1\n"
        "s_waitcnt vmcnt(0) lgkmcnt(0)\n"
        "s_barrier\n"
        "s_branch 9f\n"
        "31:\n"
        "s_mov_b32 s82, 2\n"
        "s_waitcnt vmcnt(0) lgkmcnt(0)\n"
        "s_nop 7\n"
        "s_barrier\n"
        "9:\n"
        : "={s82}"(status), "={s58}"(b), "={s[52:53]}"(epoch), "={v30}"(tv), "={v[176:183]}"(own0), "={v[208:215]}"(own4),
          "={v[80:95]}"(agx), "={v[132:135]}"(lx)
        : [karg] "s"(__builtin_amdgcn_kernarg_segment_ptr()), [blk] "s"(blockIdx.x), [tid] "v"(threadIdx.x)
        : "v1", "v2", "v3", "v4", "v5", "v8", "v9", "v10", "v11", "v12", "v13", "v14", "v15", "v16", "v17", "v18",
          "v19", "v20", "v21", "v22", "v23", "v24", "v25", "v26", "v27", "v28", "v29", "v31", "v32", "v33", "v34", "v35",
          "v36", "v37", "v38", "v39", "v40", "v41", "v42", "v43", "v44", "v45", "v46", "v47", "v48", "v49", "v50", "v51",
          "v52", "v53", "v54", "v55", "v56", "v57", "v58", "v59", "v60", "v61", "v62", "v63", "v64", "v65", "v66", "v67",
          "v68", "v69", "v70", "v71", "v72", "v73", "v74", "v75", "v76", "v77", "v78", "v79", "v96", "v97", "v98", "v99",
          "v100", "v101", "v102", "v103", "v104", "v105", "v106", "v107", "v108", "v109", "v110", "v111", "v112", "v113", "v114", "v115",
          "v116", "v117", "v118", "v119", "v120", "v121", "v122", "v123", "v124", "v125", "v126", "v127", "v128", "v129", "v130", "v131",
          "v136", "v137", "v138", "v139", "v140", "v141", "v142", "v143", "v144", "v145", "v146", "v147", "v148", "v149", "v150", "v151",
          "v152", "v153", "v154", "v155", "v156", "v157", "v158", "v159", "v160", "v161", "v162", "v163", "v164", "v165", "v166", "v167",
          "v168", "v169", "v170", "v171", "v172", "v173", "v174", "v175", "v184", "v185", "v186", "v187", "v188", "v189", "v190", "v191",
          "v192", "v193", "v194", "v195", "v196", "v197", "v198", "v199", "v200", "v201", "v202", "v203", "v204", "v205", "v206", "v207",
          "v216", "v217", "v218", "v219", "v220", "v221", "v222", "v223", "v224", "v225", "v226", "v227", "v228", "v229", "v230", "v231",
          "v232", "v233", "v234", "v235", "v236", "v237", "v238", "v239", "v240", "v241", "v242", "v243", "v244", "v245", "v246", "v247",
          "v248", "v249", "v250", "v251", "v252", "v253", "v254", "v255", "a0", "a1", "a2", "a3", "a4", "a5", "a6", "a7",
          "a8", "a9", "a10", "a11", "a12", "a13", "a14", "a15", "a16", "a17", "a18", "a19", "a20", "a21", "a22", "a23",
          "a24", "a25", "a26", "a27", "a28", "a29", "a30", "a31", "a32", "a33", "a34", "a35", "a36", "a37", "a38", "a39",
          "a40", "a41", "a42", "a43", "a44", "a45", "a46", "a47", "a48", "a49", "a50", "a51", "a52", "a53", "a54", "a55",
          "a56", "a57", "a58", "a59", "a60", "a61", "a62", "a63", "a64", "a65", "a66", "a67", "a68", "a69", "a70", "a71",
          "a72", "a73", "a74", "a75", "a76", "a77", "a78", "a79", "a80", "a81", "a82", "a83", "a84", "a85", "a86", "a87",
          "a88", "a89", "a90", "a91", "a92", "a93", "a94", "a95", "a96", "a97", "a98", "a99", "a100", "a101", "a102", "a103",
          "a104", "a105", "a106", "a107", "a108", "a109", "a110", "a111", "a112", "a113", "a114", "a115", "a116", "a117", "a118", "a119",
          "a120", "a121", "a122", "a123", "a124", "a125", "a126", "a127", "a128", "a129", "a130", "a131", "a132", "a133", "a134", "a135",
          "a136", "a137", "a138", "a139", "a140", "a141", "a142", "a143", "a144", "a145", "a146", "a147", "a148", "a149", "a150", "a151",
          "a152", "a153", "a154", "a155", "a156", "a157", "a158", "a159", "a160", "a161", "a162", "a163", "a164", "a165", "a166", "a167",
          "a168", "a169", "a170", "a171", "a172", "a173", "a174", "a175", "a176", "a177", "a178", "a179", "a180", "a181", "a182", "a183",
          "a184", "a185", "a186", "a187", "a188", "a189", "a190", "a191", "a192", "a193", "a194", "a195", "a196", "a197", "a198", "a199",
          "a200", "a201", "a202", "a203", "a204", "a205", "a206", "a207", "a208", "a209", "a210", "a211", "a212", "a213", "a214", "a215",
          "a216", "a217", "a218", "a219", "a220", "a221", "a222", "a223", "a224", "a225", "a226", "a227", "a228", "a229", "a230", "a231",
          "a232", "a233", "a234", "a235", "a236", "a237", "a238", "a239", "a240", "a241", "a242", "a243", "a244", "a245", "a246", "a247",
          "a248", "a249", "a250", "a251", "a252", "a253", "a254", "a255", "s4", "s5", "s6", "s7", "s8", "s9", "s10", "s11",
          "s12", "s13", "s14", "s15", "s16", "s17", "s18", "s19", "s36", "s37", "s38", "s39", "s40", "s41", "s42", "s43",
          "s44", "s45", "s46", "s47", "s48", "s49", "s50", "s51", "s54", "s55", "s56", "s57", "s59", "s60", "s61", "s62",
          "s63", "s64", "s65", "s66", "s67", "s68", "s69", "s70", "s71", "s72", "s73", "s74", "s75", "s76", "s77", "s78",
          "s79", "s80", "s81", "s83", "s84", "s85", "s86", "s87", "s88", "s89", "s90", "s91", "s92", "s93", "s94", "s95",
          "vcc", "scc", "memory");
    if (__builtin_expect(status == 0, 1)) return;
    if (status >= 3) {
        if (status == 3) {
            const __amdgpu_buffer_rsrc_t rws = mkrsrc(ws, WS_END);
            epoch = *(const uint64_t*)(ws + WS_EP) + 1;
            prep_weights(__builtin_amdgcn_readfirstlane(threadIdx.x >> 6), threadIdx.x & 63, wv, wm, wo, rws);
            drain(); lds_barrier();
            prep_last_keys(__builtin_amdgcn_readfirstlane(threadIdx.x >> 6), threadIdx.x & 63, (const u32x4*)x, rws);
            drain(); lds_barrier();
            if (threadIdx.x == 0) st8(u32x2{(unsigned)epoch << 24, (unsigned)(epoch >> 8)}, rws, WS_FW);
        }
        return;
    }

    const int tid = threadIdx.x, lane = tid & 63, w = __builtin_amdgcn_readfirstlane(tid >> 6), qb = b >> 2, s = b & 3;
    const int n16 = lane & 15, g4 = lane >> 4, c8 = lane & 7;
    const bool owner = b < NB;
    const __amdgpu_buffer_rsrc_t rws = mkrsrc(ws, WS_END), rhp = mkrsrc(ws + WS_HP, NN * 128);
    const u32x2 tag = {(unsigned)epoch << 24, (unsigned)(epoch >> 8)};
    u32x4 part[5];
    auto put_t = [&](unsigned img, int t, u32x4 v) {
        *LDSP(u32x2, img + (lane & 31) * 80 + 16 * t + 8 * (lane >> 5)) = u32x2{v[0], v[1]};
        *LDSP(u32x2, img + (32 + (lane & 31)) * 80 + 16 * t + 8 * (lane >> 5)) = u32x2{v[2], v[3]};
    };
    if (status == 1) {
        for (int t = 0; t < 5; ++t) {
            const int u = (4 * t + ((lane >> 3) & 3) + 5 * s + 4) % 20;
            st16(*LDSP(u32x4, w * LDS_REG + LDS_QOFF + t * 1024 + lane * 16), rws, WS_QF + b * 20480 + (u % 5) * 4096 + w * 1024 + ((lane & 39) + 8 * (u / 5)) * 16);
        }
        if (owner) {
            for (int i = tid; i < 960; i += 256) st16(u32x4{~0u, ~0u, ~0u, ~0u}, rws, WS_IB + b * 15360 + i * 16);
            for (int i = tid; i < CHP / 4; i += 256) st16(*LDSP(u32x4, LDS_ENT + i * 16), rws, WS_ED + b * (CHP * 4) + i * 16);
        }
        drain(); lds_barrier();
        if (owner) st8(u32x2{tv, tag[1]}, rws, WS_TB + b * 2048 + tid * 8);
        poll8(rws, tid < NB ? WS_TB + tid * 2048 + min(b, NB - 1) * 8 : WS_FW, tag);
        lds_barrier();
        f32x4 own[10];
        attn_true_max(qb, s, w, lane, ws, lds, own);
        lds_barrier();
        const unsigned pub = (w != s && 4 * qb + w < NB) ? WS_IB + (4 * qb + w) * 15360 + ((w ^ s) - 1) * 5120 + lane * 16 : OOB;
#pragma unroll
        for (int t = 0; t < 5; ++t) {
            f32x4 a[2];
#pragma unroll
            for (int c = 0; c < 2; ++c) {
                a[c] = own[2 * t + c];
#pragma unroll
                for (int g = 1; g < 4; ++g) a[c] += *LDSP(f32x4, (w ^ g) * LDS_REG + ((g - 1) * 10 + 2 * t + c) * 1024 + lane * 16);
            }
            part[t] = u32x4{pk2(a[0][0], a[0][1]), pk2(a[0][2], a[0][3]), pk2(a[1][0], a[1][1]), pk2(a[1][2], a[1][3])};
#pragma unroll
            for (int j = 0; j < 4; ++j) part[t][j] = min(part[t][j], 0xfffffffeu);
            st16(part[t], rws, pub + t * 1024);
        }
        if (!owner) return;
        agx = f32x16{}; lx = f32x4{};
        if (w == s) {
#pragma unroll
            for (int t = 0; t < 5; ++t) put_t((s ^ 1) * LDS_REG, t, part[t]);
        }
    } else {
        put_t((s ^ 1) * LDS_REG, w, u32x4{pk2(own4[0], own4[1]), pk2(own4[2], own4[3]), pk2(own4[4], own4[5]), pk2(own4[6], own4[7])});
        if (w == 0) put_t((s ^ 1) * LDS_REG, 4, u32x4{pk2(own0[0], own0[1]), pk2(own0[2], own0[3]), pk2(own0[4], own0[5]), pk2(own0[6], own0[7])});
    }
    for (int i = tid; i < BN * 64; i += 256) *LDSP(float, LDS_ACC + i * 4) = 0.f;
    lds_barrier();
#pragma unroll 1
    for (int p = tid >> 3; p < NB; p += 32) {
        const unsigned t2 = ld8(rws, WS_TB + p * 2048 + b * 8)[0];
        const unsigned sp = WS_ED + (p * CHP + (t2 & 0xfffu)) * 4;
#pragma unroll 1
        for (unsigned k = 0; k < ((t2 >> 12) & 0xfffu); ++k) {
            const unsigned e = __builtin_amdgcn_raw_buffer_load_b32(rws, sp + 4 * k, 0, 16), dl4 = 4u * NN - 160u * b - (e & 0xffffu);
            const u32x4 v = ld16(rhp, (e >> 16) * 128 + c8 * 16);
#pragma unroll
            for (int j = 0; j < 4; ++j) {
                __hip_atomic_fetch_add(LDSP(float, LDS_ACC + dl4 * 64 + c8 * 32 + j * 8), bflo(v[j]), __ATOMIC_RELAXED, __HIP_MEMORY_SCOPE_WORKGROUP);
                __hip_atomic_fetch_add(LDSP(float, LDS_ACC + dl4 * 64 + c8 * 32 + j * 8 + 4), bfhi(v[j]), __ATOMIC_RELAXED, __HIP_MEMORY_SCOPE_WORKGROUP);
            }
        }
    }
    lds_barrier();
    for (int i = tid; i < BN * 8; i += 256) {
        const f32x4 a0 = *LDSP(f32x4, LDS_ACC + i * 32), a1 = *LDSP(f32x4, LDS_ACC + i * 32 + 16);
        *LDSP(u32x4, LDS_M + (i >> 3) * 144 + (i & 7) * 16) = u32x4{pk2(a0[0], a0[1]), pk2(a0[2], a0[3]), pk2(a1[0], a1[1]), pk2(a1[2], a1[3])};
    }
    if (w != s) {
        for (int spin = 0; spin < SPIN_MAX; ++spin) {
            unsigned mx = 0;
#pragma unroll
            for (int t = 0; t < 5; ++t) part[t] = ld16(rws, WS_IB + b * 15360 + ((w ^ s) - 1) * 5120 + t * 1024 + lane * 16);
#pragma unroll
            for (int t = 0; t < 5; ++t) mx = max(max(mx, max(part[t][0], part[t][1])), max(part[t][2], part[t][3]));
            if (__builtin_amdgcn_ballot_w64(mx == ~0u) == 0ull) break;
            asm volatile("s_sleep 1" ::: "memory");
        }
#pragma unroll
        for (int t = 0; t < 5; ++t) put_t((w ^ 1) * LDS_REG, t, part[t]);
    }
    lds_barrier();
    if (b == NB - 2 && tid == 0) st8(u32x2{(unsigned)epoch, (unsigned)(epoch >> 32)}, rws, WS_EP);
    const int c0 = (10 * w) & ~3;
    f32x4 ag[4], al[4];
#pragma unroll
    for (int j = 0; j < 4; ++j) { ag[j] = f32x4{agx[4 * j], agx[4 * j + 1], agx[4 * j + 2], agx[4 * j + 3]}; al[j] = f32x4{}; }
#pragma unroll
    for (int k = 0; k < 2; ++k) {
        const u32x4 lf = *LDSP(u32x4, LDS_M + (c0 + n16) * 144 + 64 * k + 16 * g4);
#pragma unroll
        for (int j = 0; j < 4; ++j) al[j] = mfma16(lf, ld16(rws, WS_WF + 8192 + (j * 2 + k) * 1024 + lane * 16), al[j]);
#pragma unroll
        for (int v = 0; v < 4; ++v) {
            const unsigned ta = v * LDS_REG + (32 * k + 8 * g4 + (n16 >> 2)) * 80 + (c0 + 4 * (n16 & 3)) * 2;
            const u32x2 lo = __builtin_bit_cast(u32x2, __builtin_amdgcn_ds_read_tr16_b64_v4i16(LDSP(s16x4, ta)));
            const u32x2 hi = __builtin_bit_cast(u32x2, __builtin_amdgcn_ds_read_tr16_b64_v4i16(LDSP(s16x4, ta + 320)));
            const u32x4 gf = {lo[0], lo[1], hi[0], hi[1]};
            if (k == 0) lx = mfma16(gf, u32x4{g4 == 0 ? 0x3f80u : 0u, 0u, 0u, 0u}, lx);
#pragma unroll
            for (int j = 0; j < 4; ++j) ag[j] = mfma16(gf, ld16(rws, WS_WF + (j * 2 + k) * 1024 + lane * 16), ag[j]);
        }
    }
#pragma unroll
    for (int i = 0; i < 4; ++i) {
        const int nl = c0 + 4 * g4 + i, nd = BN * b + nl;
        if ((unsigned)(nl - 10 * w) < 10u) {
            const u32x2 xv = *(const u32x2*)(x + nd * 64 + 4 * n16);
            const float rl = __builtin_amdgcn_rcpf(lx[i]), di = bflo(dinv[nd]);
            *(u32x2*)(out + nd * 64 + 4 * n16) =
                u32x2{pk2(bflo(xv[0]) + ag[0][i] * rl + al[0][i] * di, bfhi(xv[0]) + ag[1][i] * rl + al[1][i] * di),
                      pk2(bflo(xv[1]) + ag[2][i] * rl + al[2][i] * di, bfhi(xv[1]) + ag[3][i] * rl + al[3][i] * di)};
        }
    }
}

extern "C" void kernel_launch(void* const* d_in, const int* in_sizes, int n_in, void* d_out, int out_size, void* d_ws, size_t ws_size,
                              hipStream_t stream) {
    (void)in_sizes; (void)n_in; (void)out_size; (void)ws_size;
    gps_fused<<<256, 256, 0, stream>>>((const unsigned short*)d_in[0], (const int*)d_in[1], (const int*)d_in[2],
                                       (const unsigned short*)d_in[3], (const u32x4*)d_in[5], (const u32x4*)d_in[6], (char*)d_ws,
                                       (unsigned short*)d_out, (const u32x4*)d_in[4], (const u32x4*)d_in[7],
                                       (const unsigned short*)d_in[8]);
}
```
